```python
import torch

def _softmax(x, axis=-1):
    return torch.softmax(x, dim=axis)

def _gelu(x):
    return torch.nn.functional.gelu(x, approximate='tanh')

def _silu(x):
    return torch.nn.functional.silu(x)

def _sigmoid(x):
    return torch.sigmoid(x)

def _layernorm(x, axis=-1, eps=1e-05):
    m = x.mean(dim=axis, keepdim=True)
    v = x.var(dim=axis, keepdim=True, unbiased=False)
    return (x - m) / torch.sqrt(v + eps)

def _scatter_add(tgt, idx, src, axis=0):
    return tgt.clone().index_add_(axis, idx, src)

def _segment_sum(src, seg, n):
    out = torch.zeros((n,) + src.shape[1:], dtype=src.dtype)
    return out.index_add_(0, seg, src)

def _pad(x, pw):
    flat = tuple((v for pair in reversed(pw) for v in pair))
    return torch.nn.functional.pad(x, flat)
kernel_name = 'adaLN_cond__memory_aug_recurrent_mem_tr__mixer_s_mlp__prompt'

def setup_inputs():

    _w = lambda a: (lambda t: t.to(torch.bfloat16) if t.is_floating_point() else t)(torch.as_tensor(a))

    def _A_setup():
        B, S, Dm = (1, 2048, 1024)
        H, Dh = (32, 128)
        M, seg = (8, 32)
        seg = min(seg, S)
        S = S // seg * seg
        return {'x': _w((lambda *s: torch.randn(*s, dtype=torch.bfloat16) * 0.05)(B, S, Dm)), 'mem0': _w((lambda *s: torch.randn(*s, dtype=torch.bfloat16) * 0.4)(B, M, Dm)), 'Wq': _w((lambda *s: torch.randn(*s, dtype=torch.bfloat16) * 3.2)(Dm, H, Dh) * Dm ** (-0.5)), 'Wk': _w((lambda *s: torch.randn(*s, dtype=torch.bfloat16) * 3.2)(Dm, H, Dh) * Dm ** (-0.5)), 'Wv': _w((lambda *s: torch.randn(*s, dtype=torch.bfloat16) * 0.4)(Dm, H, Dh) * Dm ** (-0.5)), 'Wo': _w((lambda *s: torch.randn(*s, dtype=torch.bfloat16) * 0.4)(H, Dh, Dm) * (H * Dh) ** (-0.5)), 'seg_len': seg}
    _A_in = _A_setup()

    def _B_setup():
        B, S, D, F = (1, 2048, 1024, 3584)
        return {'x': _w((lambda *s: torch.randn(*s, dtype=torch.bfloat16) * 0.05)(B, S, D)), 'w1': _w((lambda *s: torch.randn(*s, dtype=torch.bfloat16) * 3.2)(D, D) * D ** (-0.5)), 'w2': _w((lambda *s: torch.randn(*s, dtype=torch.bfloat16) * 3.2)(D, D) * D ** (-0.5)), 'w3': _w((lambda *s: torch.randn(*s, dtype=torch.bfloat16) * 3.2)(D, F) * D ** (-0.5)), 'w4': _w((lambda *s: torch.randn(*s, dtype=torch.bfloat16) * 3.2)(F, D) * F ** (-0.5))}
    _B_in = _B_setup()
    return {'A_x': _A_in['x'], 'A_mem0': _A_in['mem0'], 'A_Wq': _A_in['Wq'], 'A_Wk': _A_in['Wk'], 'A_Wv': _A_in['Wv'], 'A_Wo': _A_in['Wo'], 'A_seg_len': _A_in['seg_len'], 'B_x': _B_in['x'], 'B_w1': _B_in['w1'], 'B_w2': _B_in['w2'], 'B_w3': _B_in['w3'], 'B_w4': _B_in['w4'], 'c': _w((lambda *s: torch.randn(*s, dtype=torch.bfloat16) * 0.1)(1, 1024) * 8.0), 'w_mod': _w((lambda *s: torch.randn(*s, dtype=torch.bfloat16) * 0.1)(1024, 6 * 1024) * 1024 ** (-0.5) * 8.0)}

def reference(A_x, A_mem0, A_Wq, A_Wk, A_Wv, A_Wo, A_seg_len, B_x, B_w1, B_w2, B_w3, B_w4, c, w_mod):

    def _A_ref(x, mem0, Wq, Wk, Wv, Wo, seg_len):
        B, S, Dm = x.shape
        H, Dh = (Wq.shape[1], Wq.shape[2])
        M = mem0.shape[1]
        scale = Dh ** (-0.5)
        n_seg = S // seg_len
        L = M + seg_len
        idx = torch.arange(L)
        mask = (idx[:, None] < M) | (idx[:, None] >= idx[None, :])
        mem = mem0
        outs = []
        for s in range(n_seg):
            xs = x[:, s * seg_len:(s + 1) * seg_len, :].clone()
            h = torch.concatenate([mem, xs], axis=1)
            q = torch.einsum('btd,dhe->bhte', h, Wq)
            k = torch.einsum('btd,dhe->bhte', h, Wk)
            v = torch.einsum('btd,dhe->bhte', h, Wv)
            scores = torch.einsum('bhqe,bhke->bhqk', q, k) * scale
            scores = torch.where(mask[None, None], scores, -1000000000.0)
            p = _softmax(scores, axis=-1)
            o = torch.einsum('bhqk,bhke->bhqe', p, v)
            o = torch.einsum('bhte,hed->btd', o, Wo)
            mem = o[:, :M, :].clone()
            outs.append(o[:, M:, :])
        return torch.concatenate(outs, axis=1)

    def _B_ref(x, w1, w2, w3, w4):
        B, S, D = x.shape
        h = _gelu(_layernorm(x, axis=-1) @ w1)
        c = D // 4
        g0 = _pad(h[:, :, 0 * c:1 * c], ((0, 0), (1, 0), (0, 0)))[:, :S, :]
        g1 = _pad(h[:, :, 1 * c:2 * c], ((0, 0), (0, 1), (0, 0)))[:, 1:, :]
        g2 = _pad(h[:, :, 2 * c:3 * c], ((0, 0), (2, 0), (0, 0)))[:, :S, :]
        g3 = _pad(h[:, :, 3 * c:], ((0, 0), (0, 2), (0, 0)))[:, 2:, :]
        hs = torch.concatenate([g0, g1, g2, g3], axis=-1) @ w2
        x1 = x + hs
        h2 = _gelu(_layernorm(x1, axis=-1) @ w3) @ w4
        return x1 + h2
    x = A_x
    mod = (_silu(c) @ w_mod).reshape(c.shape[0], 6, 1, -1)
    sa, ca, ga, sb, cb, gb = (mod[:, 0].clone(), mod[:, 1].clone(), mod[:, 2].clone(), mod[:, 3].clone(), mod[:, 4].clone(), mod[:, 5].clone())
    h = _layernorm(x, axis=-1) * (1 + ca) + sa
    x = x + ga * _A_ref(h, A_mem0, A_Wq, A_Wk, A_Wv, A_Wo, A_seg_len)
    h = _layernorm(x, axis=-1) * (1 + cb) + sb
    x = x + gb * _B_ref(h, B_w1, B_w2, B_w3, B_w4)
    return x
```

```cpp
#include <hip/hip_runtime.h>
#include <stdint.h>

#define LAS __attribute__((address_space(3)))
typedef unsigned short bf16_t;
typedef __bf16 bfx8 __attribute__((ext_vector_type(8)));
typedef __bf16 bfx16 __attribute__((ext_vector_type(16)));
typedef __bf16 bfx2 __attribute__((ext_vector_type(2)));
typedef short s16x4 __attribute__((ext_vector_type(4)));
typedef float f32x4 __attribute__((ext_vector_type(4)));
typedef float f32x2 __attribute__((ext_vector_type(2)));
typedef unsigned u32x2 __attribute__((ext_vector_type(2)));
typedef unsigned u32x4 __attribute__((ext_vector_type(4)));
typedef unsigned u32x8 __attribute__((ext_vector_type(8)));
typedef __amdgpu_buffer_rsrc_t rsrc_t;

constexpr int SEQ = 2048, DM = 1024, NH = 32, DH = 128, NSEG = 64, FF = 3584;
constexpr float LN_EPS = 1e-5f;
constexpr unsigned SENT = 0xFFFFFFFFu;
constexpr int AUX_SC1 = 16;

constexpr size_t OFF_CTRL = 0;
constexpr size_t OFF_MOD = 65536;
constexpr size_t OFF_X = 131072;
constexpr size_t X_BYTES = 6u << 20;
constexpr size_t OFF_MDBG = OFF_X + X_BYTES;
constexpr size_t OFF_Y = OFF_MDBG + 65 * 16384;
constexpr size_t Y_STEP = 524288;
constexpr size_t SENT_END = OFF_Y + 64 * Y_STEP;
constexpr int XM_SLOT = 131072, XM_COPY = 32768;
constexpr int XP_OFF = 524288, XP_SLOT = 196608, XP_COPY = 49152;
constexpr int XY_OFF = 2097152, XY_SLOT = 1048576;
constexpr size_t OFF_PT = SENT_END;
constexpr size_t PT_STEP = 81920;
constexpr size_t OFF_H = OFF_PT + 64 * PT_STEP;
constexpr size_t ACT_BYTES = (size_t)SEQ * DM * 2;
constexpr size_t OFF_QK = OFF_H + ACT_BYTES;
constexpr size_t OFF_U = OFF_QK + (size_t)SEQ * 8192 * 2;
constexpr size_t U_STEP = 2097152;
constexpr size_t OFF_X1 = OFF_U + 64 * U_STEP;
constexpr size_t OFF_H2 = OFF_X1 + ACT_BYTES;
constexpr size_t OFF_N2 = OFF_H2 + ACT_BYTES;
constexpr size_t OFF_G = OFF_N2 + ACT_BYTES;
constexpr size_t OFF_Y1 = OFF_G + ACT_BYTES + 4 * DM * 2;
constexpr size_t OFF_N3 = OFF_Y1 + ACT_BYTES;
constexpr size_t OFF_G3 = OFF_N3 + ACT_BYTES;
constexpr size_t WS_END = OFF_G3 + (size_t)SEQ * FF * 2;
static_assert(WS_END <= (size_t)256 * 1024 * 1024, "workspace");

struct Params {
  const bf16_t *x, *mem0, *Wq, *Wk, *Wv, *Wo, *w1, *w2, *w3, *w4, *c, *wmod;
  bf16_t* out;
  unsigned char* ws;
};

__device__ __forceinline__ float bf2f(unsigned short b) { return __builtin_bit_cast(float, (unsigned)b << 16); }
__device__ __forceinline__ float bflo(unsigned u) { return __builtin_bit_cast(float, u << 16); }
__device__ __forceinline__ float bfhi(unsigned u) { return __builtin_bit_cast(float, u & 0xFFFF0000u); }
__device__ __forceinline__ unsigned pk(float lo, float hi) {
  bfx2 v; v[0] = (__bf16)lo; v[1] = (__bf16)hi; return __builtin_bit_cast(unsigned, v);
}
__device__ __forceinline__ unsigned san(unsigned u) { return u < 0xFFFFFFFEu ? u : 0xFFFFFFFEu; }
__device__ __forceinline__ f32x4 mfma(u32x4 a, u32x4 b, f32x4 c) {
  return __builtin_amdgcn_mfma_f32_16x16x32_bf16(__builtin_bit_cast(bfx8, a), __builtin_bit_cast(bfx8, b), c, 0, 0, 0);
}
__device__ __forceinline__ f32x4 smfma(u32x4 a, u32x8 b, f32x4 c, int idx) {
  return __builtin_amdgcn_smfmac_f32_16x16x64_bf16(__builtin_bit_cast(bfx8, a), __builtin_bit_cast(bfx16, b), c, idx, 0, 0);
}
__device__ __forceinline__ u32x2 trread(LAS const char* p) {
  return __builtin_bit_cast(u32x2, __builtin_amdgcn_ds_read_tr16_b64_v4i16((LAS s16x4*)p));
}
__device__ __forceinline__ u32x4 lds128(LAS const char* p) { return *(LAS const u32x4*)p; }
__device__ __forceinline__ rsrc_t mkrsrc(const void* p) { return __builtin_amdgcn_make_buffer_rsrc((void*)p, 0, 0x7fffffff, 0x00020000); }
template <int AUX> __device__ __forceinline__ void glds(rsrc_t rs, LAS char* dst, int voff, int soff) {
  const unsigned d = (unsigned)(size_t)dst;
  if (AUX) asm volatile("s_mov_b32 m0, %0\n\ts_nop 0\n\tbuffer_load_dwordx4 %1, %2, %3 offen sc1 lds" ::"s"(d), "v"(voff), "s"(rs), "s"(soff) : "memory");
  else asm volatile("s_mov_b32 m0, %0\n\ts_nop 0\n\tbuffer_load_dwordx4 %1, %2, %3 offen lds" ::"s"(d), "v"(voff), "s"(rs), "s"(soff) : "memory");
}
__device__ __forceinline__ void swap16(float& a, float& b) {
  auto r = __builtin_amdgcn_permlane16_swap(__builtin_bit_cast(unsigned, a), __builtin_bit_cast(unsigned, b), false, false);
  a = __builtin_bit_cast(float, (unsigned)r[0]); b = __builtin_bit_cast(float, (unsigned)r[1]);
}
__device__ __forceinline__ void swap16u(unsigned& a, unsigned& b) {
  auto r = __builtin_amdgcn_permlane16_swap(a, b, false, false); a = r[0]; b = r[1];
}
__device__ __forceinline__ void swap32(float& a, float& b) {
  auto r = __builtin_amdgcn_permlane32_swap(__builtin_bit_cast(unsigned, a), __builtin_bit_cast(unsigned, b), false, false);
  a = __builtin_bit_cast(float, (unsigned)r[0]); b = __builtin_bit_cast(float, (unsigned)r[1]);
}
__device__ __forceinline__ float xmax1632(float v) {
  float a = v, b = v; swap16(a, b); v = fmaxf(a, b);
  a = v; b = v; swap32(a, b); return fmaxf(a, b);
}
__device__ __forceinline__ float xsum1632(float v) {
  float a = v, b = v; swap16(a, b); v = a + b;
  a = v; b = v; swap32(a, b); return a + b;
}
__device__ __forceinline__ float gelu_tanh(float x) {
  const float u = x * (1.0f + 0.044715f * x * x) * (-2.0f * 0.7978845608028654f * 1.4426950408889634f);
  return x * __builtin_amdgcn_rcpf(1.0f + __builtin_amdgcn_exp2f(u));
}
__device__ __forceinline__ unsigned char* xbase(unsigned char* ws) { return (unsigned char*)(((size_t)(ws + OFF_X) + 2097151) & ~(size_t)2097151); }
__device__ __forceinline__ unsigned xcc_id() { unsigned x; asm volatile("s_getreg_b32 %0, hwreg(HW_REG_XCC_ID)" : "=s"(x)); return x & 7u; }
#define WAITV(n) asm volatile("s_waitcnt vmcnt(" #n ")" ::: "memory")
#define WAITL(n) asm volatile("s_waitcnt lgkmcnt(" #n ")" ::: "memory")
template <int N> __device__ __forceinline__ void waitv() { asm volatile("s_waitcnt vmcnt(%0)" ::"i"(N) : "memory"); }
__device__ __forceinline__ void bar() { asm volatile("" ::: "memory"); __builtin_amdgcn_s_barrier(); asm volatile("" ::: "memory"); }

__global__ __launch_bounds__(512) void k_mod(Params p) {
  __shared__ float red[128][33];
  const int tid = threadIdx.x, b = blockIdx.x;
  if (b < 192) {
    const int cg = tid & 3, kr = tid >> 2;
    float acc[8];
#pragma unroll
    for (int i = 0; i < 8; ++i) acc[i] = 0.f;
    u32x4 w[8];
#pragma unroll
    for (int it = 0; it < 8; ++it) w[it] = *(const u32x4*)(p.wmod + (size_t)(it * 128 + kr) * 6144 + b * 32 + cg * 8);
#pragma unroll
    for (int it = 0; it < 8; ++it) {
      const float cv = bf2f(p.c[it * 128 + kr]);
      float sv = cv / (1.0f + __expf(-cv));
      sv = bflo(pk(sv, 0.f));
#pragma unroll
      for (int i = 0; i < 4; ++i) { acc[2 * i] += sv * bflo(w[it][i]); acc[2 * i + 1] += sv * bfhi(w[it][i]); }
    }
#pragma unroll
    for (int i = 0; i < 8; ++i) red[kr][cg * 8 + i] = acc[i];
    __syncthreads();
    if (tid < 32) {
      float s = 0.f;
      for (int r = 0; r < 128; ++r) s += red[r][tid];
      ((float*)(p.ws + OFF_MOD))[b * 32 + tid] = bflo(pk(s, 0.f));
    }
  } else if (b < 194) {
    if (tid == 0) {
      unsigned char* q = xbase(p.ws) + (b - 192) * 2097152;
      unsigned best[2] = {1u << 30, 1u << 30};
#pragma unroll
      for (int r = 0; r < 10; ++r) {
        unsigned v; unsigned long long t0, t1;
        asm volatile("s_memtime %0\n\ts_waitcnt lgkmcnt(0)" : "=s"(t0)::"memory");
        asm volatile("global_load_dword %0, %1, off sc1\n\ts_waitcnt vmcnt(0)" : "=v"(v) : "v"(q + (r & 1) * 8192 + (r >> 1) * 512) : "memory");
        asm volatile("s_memtime %0\n\ts_waitcnt lgkmcnt(0)" : "=s"(t1)::"memory");
        best[r & 1] = min(best[r & 1], (unsigned)(t1 - t0));
      }
      unsigned* ctrl = (unsigned*)(p.ws + OFF_CTRL);
      ctrl[16 + b - 192] = (unsigned)(best[1] < best[0]) ^ (xcc_id() >> 2);
    }
  } else {
    const u32x4 z = {0u, 0u, 0u, 0u};
    if (tid < 256) *(u32x4*)(p.ws + OFF_G + tid * 16) = z;
    else *(u32x4*)(p.ws + OFF_G + (size_t)(SEQ + 2) * DM * 2 + (tid - 256) * 16) = z;
  }
}

__device__ __forceinline__ float wsum(float v) {
#pragma unroll
  for (int o = 1; o < 64; o <<= 1) v += __shfl_xor(v, o);
  return v;
}
__device__ __forceinline__ void ld_row(const bf16_t* r, int lane, float (&v)[16]) {
  const u32x4 a = *(const u32x4*)(r + lane * 8), b = *(const u32x4*)(r + 512 + lane * 8);
#pragma unroll
  for (int i = 0; i < 4; ++i) { v[2 * i] = bflo(a[i]); v[2 * i + 1] = bfhi(a[i]); v[8 + 2 * i] = bflo(b[i]); v[8 + 2 * i + 1] = bfhi(b[i]); }
}
__device__ __forceinline__ void st_row(bf16_t* r, int lane, const float (&v)[16]) {
  u32x4 a, b;
#pragma unroll
  for (int i = 0; i < 4; ++i) { a[i] = pk(v[2 * i], v[2 * i + 1]); b[i] = pk(v[8 + 2 * i], v[8 + 2 * i + 1]); }
  *(u32x4*)(r + lane * 8) = a; *(u32x4*)(r + 512 + lane * 8) = b;
}
__device__ __forceinline__ void ld_vec(const float* m, int lane, float (&v)[16]) {
#pragma unroll
  for (int i = 0; i < 2; ++i) {
    const f32x4 a = *(const f32x4*)(m + lane * 8 + 4 * i), b = *(const f32x4*)(m + 512 + lane * 8 + 4 * i);
#pragma unroll
    for (int j = 0; j < 4; ++j) { v[4 * i + j] = a[j]; v[8 + 4 * i + j] = b[j]; }
  }
}
__device__ __forceinline__ void norm_row(float (&v)[16]) {
  float s = 0.f;
#pragma unroll
  for (int i = 0; i < 16; ++i) s += v[i];
  const float mean = wsum(s) * (1.0f / DM);
  float q = 0.f;
#pragma unroll
  for (int i = 0; i < 16; ++i) { v[i] -= mean; q += v[i] * v[i]; }
  const float rstd = 1.0f / sqrtf(wsum(q) * (1.0f / DM) + LN_EPS);
#pragma unroll
  for (int i = 0; i < 16; ++i) v[i] *= rstd;
}
__device__ __forceinline__ void round_row(float (&v)[16]) {
#pragma unroll
  for (int i = 0; i < 8; ++i) { const unsigned u = pk(v[2 * i], v[2 * i + 1]); v[2 * i] = bflo(u); v[2 * i + 1] = bfhi(u); }
}
template <int MODE> __global__ __launch_bounds__(512) void k_ln(Params p) {
  const int lane = threadIdx.x & 63, row = blockIdx.x * 8 + (threadIdx.x >> 6);
  const float* mod = (const float*)(p.ws + OFF_MOD);
  float v[16];
  const bf16_t* src = MODE == 0 ? p.x : (const bf16_t*)(p.ws + (MODE == 1 ? OFF_X1 : OFF_Y1));
  ld_row(src + (size_t)row * DM, lane, v);
  norm_row(v);
  if (MODE < 2) {
    float sc[16], sh[16];
    ld_vec(mod + (MODE == 0 ? 1 : 4) * DM, lane, sc);
    ld_vec(mod + (MODE == 0 ? 0 : 3) * DM, lane, sh);
#pragma unroll
    for (int i = 0; i < 16; ++i) v[i] = v[i] * (1.0f + sc[i]) + sh[i];
    st_row((bf16_t*)(p.ws + (MODE == 0 ? OFF_H : OFF_H2)) + (size_t)row * DM, lane, v);
    if (MODE == 1) {
      round_row(v);
      norm_row(v);
      st_row((bf16_t*)(p.ws + OFF_N2) + (size_t)row * DM, lane, v);
    }
  } else {
    st_row((bf16_t*)(p.ws + OFF_N3) + (size_t)row * DM, lane, v);
  }
}

template <int BM, int BN, int WM, int WN, int NST> struct Gemm {
  static constexpr int TM = BM / WM / 16, TN = BN / WN / 16;
  static constexpr int A_BYTES = BM * 128, B_BYTES = BN * 128, ST_BYTES = A_BYTES + B_BYTES;
  static constexpr int LA = BM / 64, LB = BN / 64, LD = LA + LB;
  static constexpr int RB = BN * 2;
  static_assert(WM * WN == 8 && (BN == 64 || BN == 128 || BN == 256) && BM % 64 == 0, "shape");

  template <bool SHIFT, int AUXA>
  static __device__ __forceinline__ void run(LAS char* lds, rsrc_t ra, int a_off  , int lda  , rsrc_t rb,
                                             int b_off  , int ldb  , int nk, f32x4 (&acc)[TN][TM]) {
    const int tid = threadIdx.x, w = __builtin_amdgcn_readfirstlane(tid >> 6), l = tid & 63, r16 = l & 15, g = l >> 4;
    const int wm = w / WN, wn = w % WN;
    const int va = (8 * w + (l >> 3)) * lda + (((l & 7) ^ ((4 * w + (l >> 4)) & 7)) << 4);
    int vb;
    if (BN == 256) { const int k = 2 * w + (l >> 5), f = (k & 3) | (((k >> 3) & 1) << 2); vb = k * ldb + (((l & 31) ^ (f << 1)) << 4); }
    else if (BN == 128) { const int k = 4 * w + (l >> 4), f = (k & 3) | (((k >> 3) & 1) << 2); vb = k * ldb + (((l & 15) ^ (f << 1)) << 4); }
    else { const int k = 8 * w + (l >> 3), f = ((k >> 1) & 1) | (((k >> 3) & 1) << 1); vb = k * ldb + (((l & 7) ^ (f << 1)) << 4); }
    constexpr int BROWS = 8192 / RB;
    auto stage = [&](int kt, int buf) {
      LAS char* dst = lds + buf * ST_BYTES + w * 1024;
      int sa = a_off + kt * 128;
      if (SHIFT) { const int qd = kt >> 2; sa += ((qd & 1) ? 1 : -1) * ((qd >> 1) + 1) * lda; }
#pragma unroll
      for (int i = 0; i < LA; ++i) glds<AUXA>(ra, dst + i * 8192, va, sa + i * 64 * lda);
      const int sb = b_off + kt * 64 * ldb;
#pragma unroll
      for (int i = 0; i < LB; ++i) glds<0>(rb, dst + A_BYTES + i * 8192, vb, sb + i * BROWS * ldb);
    };
    const int fa = (wm * TM * 16 + r16) * 128 + ((g ^ (r16 >> 1)) << 4);
    const int q = r16 >> 2, pp = r16 & 3;
    int fb[TN];
#pragma unroll
    for (int ni = 0; ni < TN; ++ni) {
      const int nt = wn * TN + ni;
      const int f = BN == 64 ? (((q >> 1) & 1) | ((g & 1) << 1)) : (q | ((g & 1) << 2));
      fb[ni] = A_BYTES + (8 * g + q) * RB + ((nt ^ f) << 5) + pp * 8;
    }
    u32x4 A0[TM], B0[TN], A1[TM], B1[TN];
    auto rdfrag = [&](int buf, int ks, u32x4 (&Af)[TM], u32x4 (&Bf)[TN]) {
      LAS const char* base = lds + buf * ST_BYTES;
#pragma unroll
      for (int ni = 0; ni < TN; ++ni) {
        const u32x2 lo = trread(base + fb[ni] + (32 * ks) * RB), hi = trread(base + fb[ni] + (32 * ks + 4) * RB);
        Bf[ni] = u32x4{lo.x, lo.y, hi.x, hi.y};
      }
#pragma unroll
      for (int mi = 0; mi < TM; ++mi) Af[mi] = lds128(base + (fa ^ (ks * 64)) + mi * 2048);
    };
    auto mma = [&](const u32x4 (&Af)[TM], const u32x4 (&Bf)[TN]) {
      __builtin_amdgcn_s_setprio(1);
#pragma unroll
      for (int mi = 0; mi < TM; ++mi)
#pragma unroll
        for (int ni = 0; ni < TN; ++ni) acc[ni][mi] = mfma(Bf[ni], Af[mi], acc[ni][mi]);
      __builtin_amdgcn_s_setprio(0);
    };
#pragma unroll
    for (int s = 0; s < NST; ++s) stage(s < nk ? s : nk - 1, s);
    waitv<(NST - 1) * LD>();
    bar();
    rdfrag(0, 0, A0, B0);
    int buf = 0;
    for (int kt = 0; kt < nk; ++kt) {
      rdfrag(buf, 1, A1, B1);
      mma(A0, B0);
      WAITL(0);
      waitv<(NST - 2) * LD>();
      bar();
      const int kn = kt + NST;
      stage(kn < nk ? kn : nk - 1, buf);
      buf = buf + 1 == NST ? 0 : buf + 1;
      rdfrag(buf, 0, A0, B0);
      mma(A1, B1);
    }
    WAITV(0);
    WAITL(0);
    bar();
  }
};

__device__ __forceinline__ void pair_swap(f32x4& a, f32x4& b) {
#pragma unroll
  for (int i = 0; i < 4; ++i) { float x = a[i], y = b[i]; swap16(x, y); a[i] = x; b[i] = y; }
}

extern __shared__ __attribute__((aligned(16))) char smem[];

__global__ __launch_bounds__(512, 2) void k_qk(Params p) {
  typedef Gemm<256, 256, 2, 4, 2> G;
  LAS char* lds = (LAS char*)smem;
  const int t = blockIdx.x, rb = t & 7, cb = t >> 3;
  const bf16_t* W = cb < 16 ? p.Wq : p.Wk;
  f32x4 acc[G::TN][G::TM];
#pragma unroll
  for (int i = 0; i < G::TN; ++i)
#pragma unroll
    for (int j = 0; j < G::TM; ++j) acc[i][j] = f32x4{0.f, 0.f, 0.f, 0.f};
  G::run<false, 0>(lds, mkrsrc(p.ws + OFF_H), rb * 256 * 2048, 2048, mkrsrc(W), (cb & 15) * 512, 8192, 16, acc);
  const int w = threadIdx.x >> 6, l = threadIdx.x & 63, wm = w / 4, wn = w % 4;
  unsigned char* dst = p.ws + OFF_QK;
#pragma unroll
  for (int mi = 0; mi < G::TM; ++mi)
#pragma unroll
    for (int np = 0; np < G::TN / 2; ++np) {
      const int tt = rb * 16 + wm * 8 + mi, c32 = cb * 8 + wn * 2 + np;
      const f32x4 a = acc[2 * np][mi], b = acc[2 * np + 1][mi];
      *(u32x4*)(dst + ((size_t)tt * 256 + c32) * 1024 + l * 16) = u32x4{pk(a[0], a[1]), pk(a[2], a[3]), pk(b[0], b[1]), pk(b[2], b[3])};
    }
}

__global__ __launch_bounds__(512, 2) void k_vu(Params p) {
  typedef Gemm<256, 128, 8, 1, 3> G;
  LAS char* lds = (LAS char*)smem;
  const int t = blockIdx.x, rb = t & 7, h = t >> 3;
  const int tid = threadIdx.x, w = __builtin_amdgcn_readfirstlane(tid >> 6), l = tid & 63, r16 = l & 15, g = l >> 4;
  u32x4 Vf[4][2];
  {
    f32x4 acc[8][2];
#pragma unroll
    for (int i = 0; i < 8; ++i) { acc[i][0] = f32x4{0.f, 0.f, 0.f, 0.f}; acc[i][1] = acc[i][0]; }
    G::run<false, 0>(lds, mkrsrc(p.ws + OFF_H), rb * 256 * 2048, 2048, mkrsrc(p.Wv), h * 256, 8192, 16, acc);
#pragma unroll
    for (int ks = 0; ks < 4; ++ks)
#pragma unroll
      for (int mi = 0; mi < 2; ++mi) {
        const f32x4 a = acc[2 * ks][mi], b = acc[2 * ks + 1][mi];
        Vf[ks][mi] = u32x4{pk(a[0], a[1]), pk(a[2], a[3]), pk(b[0], b[1]), pk(b[2], b[3])};
      }
  }
  const rsrc_t rw = mkrsrc(p.Wo + (size_t)h * DH * DM);
  const int kst = 4 * w + g;
  const int vw = kst * 2048 + ((r16 ^ ((kst & 7) << 1)) << 4);
  auto stage = [&](int c) {
    LAS char* dst = lds + (c & 3) * 32768 + w * 1024;
#pragma unroll
    for (int i = 0; i < 4; ++i) glds<0>(rw, dst + i * 8192, vw, i * 32 * 2048 + (c & 7) * 256);
  };
  const int q = r16 >> 2, pp = r16 & 3, er = 4 * g + q;
  unsigned char* U = p.ws + OFF_U + (size_t)(rb * 8 + w) * U_STEP + (size_t)h * 1024 + l * 16;
  stage(0); stage(1); stage(2);
  for (int c = 0; c < 8; ++c) {
    if (c == 0) WAITV(8); else if (c == 1) WAITV(16); else WAITV(24);
    bar();
    stage(c + 3);
    LAS const char* base = lds + (c & 3) * 32768 + er * 256 + pp * 8;
#pragma unroll
    for (int nt = 0; nt < 8; ++nt) {
      f32x4 u0 = {0.f, 0.f, 0.f, 0.f}, u1 = u0;
      LAS const char* bp = base + ((nt ^ (er & 7)) << 5);
#pragma unroll
      for (int ks = 0; ks < 4; ++ks) {
        const u32x2 lo = trread(bp + (32 * ks) * 256), hi = trread(bp + (32 * ks + 16) * 256);
        const u32x4 Bf = {lo.x, lo.y, hi.x, hi.y};
        u0 = mfma(Vf[ks][0], Bf, u0);
        u1 = mfma(Vf[ks][1], Bf, u1);
      }
      *(u32x4*)(U + (size_t)(c * 8 + nt) * 32768) = u32x4{pk(u0[0], u0[1]), pk(u0[2], u0[3]), pk(u1[0], u1[1]), pk(u1[2], u1[3])};
    }
    WAITL(0);
  }
  WAITV(0);
}

template <int EPI, int BM, int BN, int WM, int WN, int NST> __global__ __launch_bounds__(512, 2) void k_mlp(Params p) {
  typedef Gemm<BM, BN, WM, WN, NST> G;
  LAS char* lds = (LAS char*)smem;
  constexpr int NRB = SEQ / BM;
  const int t = blockIdx.x, rb = t % NRB, cb = t / NRB;
  f32x4 acc[G::TN][G::TM];
#pragma unroll
  for (int i = 0; i < G::TN; ++i)
#pragma unroll
    for (int j = 0; j < G::TM; ++j) acc[i][j] = f32x4{0.f, 0.f, 0.f, 0.f};
  if (EPI == 1) G::template run<false, 0>(lds, mkrsrc(p.ws + OFF_N2), rb * BM * 2048, 2048, mkrsrc(p.w1), cb * BN * 2, 2048, 16, acc);
  if (EPI == 2) G::template run<true, 0>(lds, mkrsrc(p.ws + OFF_G), (rb * BM + 2) * 2048, 2048, mkrsrc(p.w2), cb * BN * 2, 2048, 16, acc);
  if (EPI == 3) G::template run<false, 0>(lds, mkrsrc(p.ws + OFF_N3), rb * BM * 2048, 2048, mkrsrc(p.w3), cb * BN * 2, FF * 2, 16, acc);
  if (EPI == 4) G::template run<false, 0>(lds, mkrsrc(p.ws + OFF_G3), rb * BM * FF * 2, FF * 2, mkrsrc(p.w4), cb * BN * 2, 2048, FF / 64, acc);
  const int w = threadIdx.x >> 6, l = threadIdx.x & 63, r16 = l & 15, g = l >> 4, wm = w / WN, wn = w % WN;
#pragma unroll
  for (int mi = 0; mi < G::TM; ++mi)
#pragma unroll
    for (int np = 0; np < G::TN / 2; ++np) {
      f32x4 a = acc[2 * np][mi], b = acc[2 * np + 1][mi];
      pair_swap(a, b);
      const int row = rb * BM + (wm * G::TM + mi) * 16 + r16, col = cb * BN + (wn * G::TN + 2 * np) * 16 + 16 * (g & 1) + 8 * (g >> 1);
      if (EPI == 1 || EPI == 3) {
#pragma unroll
        for (int i = 0; i < 4; ++i) { a[i] = gelu_tanh(a[i]); b[i] = gelu_tanh(b[i]); }
      }
      if (EPI == 2 || EPI == 4) {
        const u32x4 r = *(const u32x4*)(p.ws + (EPI == 2 ? OFF_H2 : OFF_Y1) + ((size_t)row * DM + col) * 2);
        a[0] += bflo(r[0]); a[1] += bfhi(r[0]); a[2] += bflo(r[1]); a[3] += bfhi(r[1]);
        b[0] += bflo(r[2]); b[1] += bfhi(r[2]); b[2] += bflo(r[3]); b[3] += bfhi(r[3]);
      }
      if (EPI == 4) {
        const u32x4 r = *(const u32x4*)(p.ws + OFF_X1 + ((size_t)row * DM + col) * 2);
        const float* gb = (const float*)(p.ws + OFF_MOD) + 5 * DM + col;
        const f32x4 g0 = *(const f32x4*)gb, g1 = *(const f32x4*)(gb + 4);
        a[0] = bflo(r[0]) + g0[0] * a[0]; a[1] = bfhi(r[0]) + g0[1] * a[1]; a[2] = bflo(r[1]) + g0[2] * a[2]; a[3] = bfhi(r[1]) + g0[3] * a[3];
        b[0] = bflo(r[2]) + g1[0] * b[0]; b[1] = bfhi(r[2]) + g1[1] * b[1]; b[2] = bflo(r[3]) + g1[2] * b[2]; b[3] = bfhi(r[3]) + g1[3] * b[3];
      }
      const u32x4 o = {pk(a[0], a[1]), pk(a[2], a[3]), pk(b[0], b[1]), pk(b[2], b[3])};
      if (EPI == 1) *(u32x4*)(p.ws + OFF_G + ((size_t)(row + 2) * DM + col) * 2) = o;
      if (EPI == 2) *(u32x4*)(p.ws + OFF_Y1 + ((size_t)row * DM + col) * 2) = o;
      if (EPI == 3) *(u32x4*)(p.ws + OFF_G3 + ((size_t)row * FF + col) * 2) = o;
      if (EPI == 4) *(u32x4*)(p.out + (size_t)row * DM + col) = o;
    }
}

__global__ __launch_bounds__(512, 2) void k_pz(Params p) {
  LAS char* lds = (LAS char*)smem;
  const int s = blockIdx.x >> 2, qd = blockIdx.x & 3;
  const int tid = threadIdx.x, w = __builtin_amdgcn_readfirstlane(tid >> 6), l = tid & 63, r16 = l & 15, g = l >> 4;
  const rsrc_t rp = mkrsrc(p.ws + OFF_PT + (size_t)s * PT_STEP);
#pragma unroll
  for (int i = 0; i < 10; ++i) glds<0>(rp, lds + i * 8192 + w * 1024, tid * 16, i * 8192);
  const int j0 = qd * 16 + 2 * w;
  const unsigned char* U = p.ws + OFF_U + (size_t)s * U_STEP + (size_t)j0 * 32768 + l * 16;
  const unsigned char* Y = p.ws + OFF_Y + (size_t)s * Y_STEP + (size_t)j0 * 8192 + l * 16;
  f32x4 acc[2][2];
  acc[0][0] = f32x4{0.f, 0.f, 0.f, 0.f}; acc[0][1] = acc[0][0]; acc[1][0] = acc[0][0]; acc[1][1] = acc[0][0];
  WAITV(0);
  bar();
#pragma unroll
  for (int hb = 0; hb < 5; ++hb) {
    u32x4 z[2][8];
#pragma unroll
    for (int i = 0; i < 8; ++i) {
      z[0][i] = hb < 4 ? *(const u32x4*)(U + (hb * 8 + i) * 1024) : *(const u32x4*)(Y + i * 1024);
      z[1][i] = hb < 4 ? *(const u32x4*)(U + 32768 + (hb * 8 + i) * 1024) : *(const u32x4*)(Y + 8192 + i * 1024);
    }
#pragma unroll
    for (int i = 0; i < 8; ++i) {
      const int o0 = hb < 4 ? (hb * 8 + i) * 1024 : 65536 + i * 1024, o1 = hb < 4 ? o0 + 32768 : o0 + 8192;
      const u32x4 p0 = lds128(lds + o0 + l * 16), p1 = lds128(lds + o1 + l * 16);
      acc[0][0] = mfma(z[0][i], p0, acc[0][0]); acc[1][0] = mfma(z[1][i], p0, acc[1][0]);
      acc[0][1] = mfma(z[0][i], p1, acc[0][1]); acc[1][1] = mfma(z[1][i], p1, acc[1][1]);
    }
  }
#pragma unroll
  for (int qt = 0; qt < 2; ++qt) {
    f32x4 a = acc[0][qt], b = acc[1][qt];
    pair_swap(a, b);
    const int row = s * 32 + qt * 16 + r16, col = j0 * 16 + 16 * (g & 1) + 8 * (g >> 1);
    const u32x4 r = *(const u32x4*)(p.x + (size_t)row * DM + col);
    const float* ga = (const float*)(p.ws + OFF_MOD) + 2 * DM + col;
    const f32x4 g0 = *(const f32x4*)ga, g1 = *(const f32x4*)(ga + 4);
    a[0] = bflo(r[0]) + g0[0] * a[0]; a[1] = bfhi(r[0]) + g0[1] * a[1]; a[2] = bflo(r[1]) + g0[2] * a[2]; a[3] = bfhi(r[1]) + g0[3] * a[3];
    b[0] = bflo(r[2]) + g1[0] * b[0]; b[1] = bfhi(r[2]) + g1[1] * b[1]; b[2] = bflo(r[3]) + g1[2] * b[2]; b[3] = bfhi(r[3]) + g1[3] * b[3];
    *(u32x4*)(p.ws + OFF_X1 + ((size_t)row * DM + col) * 2) = u32x4{pk(a[0], a[1]), pk(a[2], a[3]), pk(b[0], b[1]), pk(b[2], b[3])};
  }
}

constexpr int NREG = 24;
constexpr int NLDS = 32 - NREG;
constexpr int CL_M = 0;
constexpr int CL_KX = CL_M + 16384;
constexpr int CL_IMG = CL_KX + 8192;
constexpr int CL_U0 = CL_IMG + 4096;
constexpr int CL_RED = CL_M;
constexpr int CL_W = CL_U0 + 4096;
constexpr int CL_END = CL_W + 8 * NLDS * 2048;
static_assert(CL_END <= 163840, "chain LDS");
constexpr unsigned SPIN_CAP = 400000u;
#define STAMPS 0
#define MDBG 0
#define STAMP(k)
#define STAMPV(k, v)
#define STAMP_DECL
#define STAMP_FLUSH(blk, s)

__device__ __forceinline__ unsigned umax3(unsigned a, unsigned b, unsigned c) { return max(max(a, b), c); }
__device__ __forceinline__ unsigned umax4(u32x4 v) { return max(umax3(v.x, v.y, v.z), v.w); }
__device__ __forceinline__ u32x4 ldsc1(rsrc_t rs, int voff, int soff) { return __builtin_amdgcn_raw_buffer_load_b128(rs, voff, soff, AUX_SC1); }
__device__ __forceinline__ int opaque(int x) { asm volatile("" : "+v"(x)); return x; }

__device__ __forceinline__ void wstage(rsrc_t rw, int soff, int ldw, LAS char* dst, int w, int l) {
  const int r = 4 * w + (l >> 4);
  const int f = (r & 1) | (((r >> 2) & 3) << 1);
  const int v = r * ldw + (((l & 15) ^ (f << 1)) << 4);
  glds<0>(rw, dst + w * 1024, v, soff);
  glds<0>(rw, dst + 8192 + w * 1024, v, soff + 32 * ldw);
}
__device__ __forceinline__ u32x8 wfrag(LAS const char* img, int nt, int l) {
  const int g = l >> 4, q = (l & 15) >> 2, pp = l & 3;
  const int d0 = 16 * (g >> 1) + 4 * (g & 1);
  const int rx = d0 + (q & 1) + 8 * (q >> 1);
  const int f = (rx & 1) | (((rx >> 2) & 3) << 1);
  LAS const char* a = img + rx * 256 + ((nt ^ f) << 5) + pp * 8;
  const u32x2 x0 = trread(a), x1 = trread(a + 2 * 256), x2 = trread(a + 32 * 256), x3 = trread(a + 34 * 256);
  return u32x8{x0.x, x0.y, x1.x, x1.y, x2.x, x2.y, x3.x, x3.y};
}
__device__ __forceinline__ void put_frag(LAS char* wl, int i, u32x8 f) {
  *(LAS u32x4*)(wl + i * 2048) = u32x4{f[0], f[1], f[2], f[3]};
  *(LAS u32x4*)(wl + i * 2048 + 1024) = u32x4{f[4], f[5], f[6], f[7]};
}
__device__ __forceinline__ u32x8 get_frag(LAS const char* wl, int i) {
  const u32x4 a = lds128(wl + i * 2048), b = lds128(wl + i * 2048 + 1024);
  return u32x8{a.x, a.y, a.z, a.w, b.x, b.y, b.z, b.w};
}
template <class SRC, class PUT>
__device__ __forceinline__ void load_weights(LAS char* lds, int w, int l, SRC src, PUT put) {
#pragma unroll
  for (int r = 0; r < 3; ++r) {
#pragma unroll
    for (int i = 0; i < 8; ++i) src(r * 8 + i, lds + CL_W + i * 16384);
#pragma unroll
    for (int i = 0; i < 8; i += 2) {
      if (i == 0) WAITV(12); else if (i == 2) WAITV(8); else if (i == 4) WAITV(4); else WAITV(0);
      bar();
      put(r * 8 + i, wfrag(lds + CL_W + i * 16384, w, l));
      put(r * 8 + i + 1, wfrag(lds + CL_W + (i + 1) * 16384, w, l));
    }
    WAITL(0);
    bar();
  }
#pragma unroll
  for (int r = 0; r < 4; ++r) {
    src(24 + 2 * r, lds); src(25 + 2 * r, lds + 16384);
    WAITV(0);
    bar();
    put(24 + 2 * r, wfrag(lds, w, l));
    put(25 + 2 * r, wfrag(lds + 16384, w, l));
    WAITL(0);
    bar();
  }
}

#define SCHED() __builtin_amdgcn_sched_barrier(0)
#define DB 2750
#define DA 800
#define DRETRY 3
#define WARM 1
__device__ __forceinline__ unsigned now32() { unsigned long long t; asm volatile("s_memtime %0\n\ts_waitcnt lgkmcnt(0)" : "=s"(t)::"memory"); return (unsigned)t; }
__device__ __forceinline__ void wait_until(unsigned t) {
#pragma nounroll
  while ((int)(now32() - t) < 0) {}
}
__device__ __forceinline__ f32x4 zero4() {
  f32x2 a, b; asm volatile("v_mov_b64 %0, 0\n\tv_mov_b64 %1, 0\n\ts_nop 1" : "=v"(a), "=v"(b)); return f32x4{a.x, a.y, b.x, b.y};
}
__device__ __forceinline__ u32x4 sent4() {
  u32x2 a, b; asm volatile("s_nop 1\n\tv_mov_b64 %0, -1\n\tv_mov_b64 %1, -1" : "=v"(a), "=v"(b)); return u32x4{a.x, a.y, b.x, b.y};
}
__device__ __forceinline__ float ror8(float v) { return __builtin_bit_cast(float, __builtin_amdgcn_mov_dpp(__builtin_bit_cast(int, v), 0x128  , 0xf, 0xf, true)); }
__device__ __forceinline__ float fmx(float a, float b) { return __builtin_fmaxf(a, b); }
__device__ __forceinline__ void st128(u32x4 v, rsrc_t rs, int voff, int soff) { __builtin_amdgcn_raw_buffer_store_b128(v, rs, voff, soff, AUX_SC1); }
__device__ __forceinline__ void st64(u32x2 v, rsrc_t rs, int voff, int soff) { __builtin_amdgcn_raw_buffer_store_b64(v, rs, voff, soff, AUX_SC1); }
__device__ __forceinline__ void st32(unsigned v, rsrc_t rs, int voff, int soff) { __builtin_amdgcn_raw_buffer_store_b32(v, rs, voff, soff, AUX_SC1); }

struct Chain {
  rsrc_t rx, ru, ry;
  int fa, fb;
  int cp;
  int j, w;
};

__device__ __forceinline__ void y_done(const Chain& c, int s, int l16, u32x4 y) {
  st128(y, c.ry, l16, s * (int)Y_STEP + c.j * 8192 + c.w * 1024);
  st128(sent4(), c.rx, l16, XY_OFF + (s & 1) * XY_SLOT + c.j * 16384 + c.w * 1024 + c.fb);
}
__device__ __forceinline__ bool wait_m(const Chain& c, int s, LAS char* lds, int l16, unsigned& spins, u32x4 y) {
  const int soff = (s & 3) * XM_SLOT + c.cp * XM_COPY + ((c.w >> 1) & 1) * 16384 + (c.w & 1) * 4096 + c.fa;
  LAS char* dst = lds + CL_M + (c.w - 4) * 4096 + l16;
  u32x4 a[4];
#pragma unroll
  for (int i = 0; i < 4; ++i) a[i] = ldsc1(c.rx, l16 + i * 1024, soff);
  SCHED();
  y_done(c, s - 1, l16, y);
  SCHED();
#pragma unroll
  for (int i = 0; i < 4; ++i) *(LAS u32x4*)(dst + i * 1024) = a[i];
  bool ok = __builtin_amdgcn_ballot_w64(max(max(umax4(a[0]), umax4(a[1])), max(umax4(a[2]), umax4(a[3]))) == SENT) == 0ull;
  spins = 0;
  if (__builtin_expect(!ok, 0)) {
#pragma nounroll
    do {
      __builtin_amdgcn_s_sleep(DRETRY);
      asm volatile("" ::: "memory");
#pragma unroll
      for (int i = 0; i < 4; ++i) a[i] = ldsc1(c.rx, l16 + i * 1024, soff);
#pragma unroll
      for (int i = 0; i < 4; ++i) *(LAS u32x4*)(dst + i * 1024) = a[i];
      ok = __builtin_amdgcn_ballot_w64(max(max(umax4(a[0]), umax4(a[1])), max(umax4(a[2]), umax4(a[3]))) == SENT) == 0ull;
    } while (++spins < SPIN_CAP && !ok);
  }
  return ok;
}
__device__ __forceinline__ void warm(unsigned& d, rsrc_t rs, int l, int soff) {
  asm volatile("buffer_load_dword %0, %1, %2, %3 offen" : "+v"(d) : "v"(l * 64), "s"(rs), "s"(soff) : "memory");
}

template <int I> __device__ __forceinline__ void load_u(u32x4& u, const Chain& c, int so, int l16) {
  asm volatile("buffer_load_dwordx4 %0, %1, %2, %3 offen offset:%4 sc1" : "=&v"(u) : "v"(l16), "s"(c.ru), "s"(so), "i"(I * 1024) : "memory");
}

__device__ __forceinline__ bool o_pass(const Params& p, const Chain& c, int s, LAS char* lds, int l, unsigned t_m, const u32x4 (&u)[4], u32x4& y
) {
  const int r16 = l & 15, g = l >> 4, l16 = l * 16, w = c.w, j = c.j;
  const int pmv = g * 128 + (r16 & 7) * 16;
  const int pv = opaque((r16 >> 3) * 512 + pmv);
  const int ps = XP_OFF + (s & 3) * XP_SLOT + c.cp * XP_COPY + c.fa, pst = ps + (w >> 2) * 16384 + (w & 3) * 2048;
  const int ys = XY_OFF + (s & 1) * XY_SLOT + j * 16384 + w * 1024 + c.fb;
  LAS char* redw = lds + opaque(CL_RED + w * 512 + pmv);
  unsigned spins = 0;
  bool ok;
  STAMP(10);
  STAMP_FLUSH(j, s);
  wait_until(t_m + DB);
  STAMP(5);
  f32x4 dl, dh;
#pragma nounroll
  do {
    asm volatile("" ::: "memory");
    y = ldsc1(c.rx, l16, ys);
    const u32x4 p0 = ldsc1(c.rx, pv, pst), p1 = ldsc1(c.rx, pv + 1024, pst), pm = ldsc1(c.rx, pmv, ps + 32768 + w * 512);
    dh = mfma(u[1], p0, f32x4{0.f, 0.f, 0.f, 0.f});
    dl = mfma(u[0], p0, f32x4{0.f, 0.f, 0.f, 0.f});
    dh = mfma(u[3], p1, dh);
    dl = mfma(u[2], p1, dl);
    dl = mfma(y, pm, dl);
    ok = __builtin_amdgcn_ballot_w64(max(umax4(y), umax3(umax4(p0), umax4(p1), umax4(pm))) == SENT) == 0ull;
    if (!ok) __builtin_amdgcn_s_sleep(DRETRY);
  } while (!ok && ++spins < SPIN_CAP);
  STAMP(6); STAMPV(11, spins);
#pragma unroll
  for (int i = 0; i < 4; ++i) { const float hi = dh[i]; dl[i] += ror8(hi); }
  if (r16 < 8) *(LAS f32x4*)redw = dl;
  return ok;
}
template <bool REDUCER, bool POLLER> __device__ __forceinline__ unsigned o_finish(const Params& p, const Chain& c, int s, LAS char* lds, int l, u32x4 y
) {
  const int r16 = l & 15, g = l >> 4, l16 = l * 16, w = c.w, j = c.j;
  unsigned t_r;
  if (REDUCER) {
    LAS const char* redr = lds + opaque(CL_RED + (r16 >> 3) * 2048 + g * 128 + (r16 & 7) * 16);
    const int mo = opaque((r16 >> 3) * XM_COPY + ((r16 & 7) + 8 * (g >> 1)) * 16 + (g & 1) * 8);
    const int ms = ((s + 1) & 3) * XM_SLOT + (j >> 5) * 16384 + (j & 31) * 256 + c.fa;
    WAITL(0);
    bar();
    f32x4 t = *(LAS const f32x4*)redr + *(LAS const f32x4*)(redr + 512) + (*(LAS const f32x4*)(redr + 1024) + *(LAS const f32x4*)(redr + 1536));
#pragma unroll
    for (int i = 0; i < 4; ++i) { const float ti = t[i]; t[i] = ti + ror8(ti); }
    const u32x2 o = {san(pk(t[0], t[1])), san(pk(t[2], t[3]))};
    st64(o, c.rx, mo, ms);
    st64(o, c.rx, mo, ms + 2 * XM_COPY);
    t_r = now32() - 150;
  } else {
    WAITL(0);
    bar();
    t_r = now32();
    if (!POLLER) __builtin_amdgcn_s_sleep(3);
  }
  STAMP(7);
  if (!POLLER) y_done(c, s, l16, y);
  return t_r;
}
#define ST_ARG

constexpr float SM_SC = 0.08838834764831845f * 1.4426950408889634f;

__device__ __forceinline__ Chain chain_init(const Params& p, int j) {
  Chain c;
  c.rx = mkrsrc(xbase(p.ws)); c.ru = mkrsrc(p.ws + OFF_U); c.ry = mkrsrc(p.ws + OFF_Y);
  const unsigned* ctrl = (const unsigned*)(p.ws + OFF_CTRL);
  c.fa = __builtin_amdgcn_readfirstlane(ctrl[16] & 1) * 8192; c.fb = __builtin_amdgcn_readfirstlane(ctrl[17] & 1) * 8192;
  c.cp = xcc_id() & 3; c.j = j; c.w = __builtin_amdgcn_readfirstlane(threadIdx.x >> 6);
  return c;
}
__device__ __forceinline__ void stage_m0(const Params& p, LAS char* lds, int w, int l) {
#pragma unroll
  for (int i = 0; i < 2; ++i)
    *(LAS u32x4*)(lds + CL_M + (2 * w + i) * 1024 + l * 16) = *(const u32x4*)(p.mem0 + (l & 7) * DM + 64 * (2 * w + i) + 16 * (l >> 4) + 8 * ((l >> 3) & 1));
}
__device__ __forceinline__ void rearm_m(const Chain& c, int s, int l) {
  st128(sent4(), c.rx, (l >> 4) * XM_COPY + (l & 15) * 16, ((s + 3) & 3) * XM_SLOT + (c.j >> 5) * 16384 + (c.j & 31) * 256 + c.fa);
}

__device__ __forceinline__ void p_tok(rsrc_t rpt, int s, int h, int qt, LAS char* lds, int l, const u32x4 (&Q)[4]) {
  const int r16 = l & 15, g = l >> 4, l16 = l * 16;
  LAS const char* fimg = lds + CL_IMG + l * 8;
  f32x4 s0 = {0.f, 0.f, 0.f, 0.f}, s1 = s0, sm = s0;
#pragma unroll
  for (int ks = 0; ks < 4; ++ks) {
    const u32x2 lo = trread(fimg + ks * 1024), hi = trread(fimg + ks * 1024 + 512);
    s0 = mfma(lds128(lds + CL_KX + ks * 1024 + l16), Q[ks], s0);
    s1 = mfma(lds128(lds + CL_KX + 4096 + ks * 1024 + l16), Q[ks], s1);
    sm = mfma(u32x4{lo.x, lo.y, hi.x, hi.y}, Q[ks], sm);
  }
  const float NEG = -__builtin_inff();
  const int qi = qt * 16 + r16 - 4 * g;
#pragma unroll
  for (int i = 0; i < 4; ++i) {
    if (i > qi) s0[i] = NEG;
    if (16 + i > qi) s1[i] = NEG;
    if (g < 2) sm[i] = NEG;
  }
  float mx = fmx(fmx(fmx(s0[0], s0[1]), fmx(s0[2], s0[3])), fmx(fmx(s1[0], s1[1]), fmx(s1[2], s1[3])));
  mx = fmx(mx, fmx(fmx(sm[0], sm[1]), fmx(sm[2], sm[3])));
  { float a = mx, b = mx; swap16(a, b); mx = fmx(a, b); a = mx; b = mx; swap32(a, b); mx = fmx(a, b); }
  mx *= SM_SC;
  float sum = 0.f;
#pragma unroll
  for (int i = 0; i < 4; ++i) {
    s0[i] = __builtin_amdgcn_exp2f(s0[i] * SM_SC - mx); s1[i] = __builtin_amdgcn_exp2f(s1[i] * SM_SC - mx); sm[i] = __builtin_amdgcn_exp2f(sm[i] * SM_SC - mx);
    sum += (s0[i] + s1[i]) + sm[i];
  }
  const float inv = __builtin_amdgcn_rcpf(xsum1632(sum));
  const u32x4 pt = {pk(s0[0] * inv, s0[1] * inv), pk(s0[2] * inv, s0[3] * inv), pk(s1[0] * inv, s1[1] * inv), pk(s1[2] * inv, s1[3] * inv)};
  const u32x2 pm = {pk(sm[0] * inv, sm[1] * inv), pk(sm[2] * inv, sm[3] * inv)};
  const int ps = s * (int)PT_STEP;
  st128(pt, rpt, l16, ps + (qt * 32 + h) * 1024);
  if (g >= 2) st64(pm, rpt, r16 * 16 + (g - 2) * 8, ps + 65536 + qt * 8192 + h * 256);
}

template <int CLS>
__device__ __forceinline__ bool qk_loop(const Params& p, const Chain& c, int h, LAS char* lds, const u32x8 (&W)[NREG]) {
  const int w = c.w;
  int l = threadIdx.x & 63;
  const rsrc_t rqk = mkrsrc(p.ws + OFF_QK), rpt = mkrsrc(p.ws + OFF_PT);
  STAMP_DECL;
  unsigned t_m = 0, t_r = 0;
  for (int s = 0;; ++s) {
    l = opaque(l);
    const bool last = s + 1 == NSEG;
    const int l16 = l * 16;
    const int idx = opaque((l & 8) ? 0xEEEE : 0x4444);
    LAS const char* wl = lds + CL_W + w * (NLDS * 2048) + l16;
    u32x8 B[2];
    B[0] = get_frag(wl, 0); B[1] = get_frag(wl, 1);
    f32x4 aq = zero4(), ak = zero4();
    WAITL(0);
    bar();
    u32x4 A[3];
#pragma unroll
    for (int t = 0; t < 3; ++t) A[t] = lds128(lds + CL_M + t * 1024 + l16);
    t_m = now32();
    STAMP(1);
    SCHED();
    u32x4 u[4];
    unsigned wd = 0;
    const int uso = s * (int)U_STEP + h * 32768 + w * 4096;
#pragma unroll
    for (int t = 0; t < 16; ++t) {
      aq = smfma(A[t % 3], W[3 * (t >> 1) + 2 * (t & 1)], aq, idx);
      ak = smfma(A[t % 3], (t & 1) ? B[(t >> 1) & 1] : W[3 * (t >> 1) + 1], ak, idx);
      SCHED();
      if (t + 3 < 16) A[t % 3] = lds128(lds + CL_M + (t + 3) * 1024 + l16);
      if ((t & 1) && (t >> 1) + 2 < NLDS) B[(t >> 1) & 1] = get_frag(wl, (t >> 1) + 2);
      if (t == 0) glds<AUX_SC1>(rqk, lds + CL_KX + w * 1024, l16, ((2 * s + (w >> 2)) * 256 + (32 + h) * 4 + (w & 3)) * 1024);
      if (t == 2) { if (CLS == 0) glds<AUX_SC1>(c.ru, lds + CL_U0, l16, uso); else load_u<0>(u[0], c, uso, l16); }
      if (t == 4) { if (CLS == 0) glds<AUX_SC1>(c.ru, lds + CL_U0 + 1024, l16, uso + 1024); else load_u<1>(u[1], c, uso, l16); }
      if (t == 6) { if (CLS == 0) glds<AUX_SC1>(c.ru, lds + CL_U0 + 2048, l16, uso + 2048); else load_u<2>(u[2], c, uso, l16); }
      if (t == 8) { if (CLS == 0) glds<AUX_SC1>(c.ru, lds + CL_U0 + 3072, l16, uso + 3072); else load_u<3>(u[3], c, uso, l16); }
      if (t == 10 && CLS >= 4) warm(wd, rqk, l, ((2 * ((w & 2) && !last ? s + 1 : s) + (w & 1)) * 256 + ((w >> 1) & 1) * 128 + h * 4) * 1024);
      SCHED();
    }
    l = opaque(l);
    {
      float r[4];
#pragma unroll
      for (int i = 0; i < 4; ++i) { float a = aq[i], b = ak[i]; swap32(a, b); r[i] = a + b; }
      *(LAS u32x2*)(lds + CL_IMG + w * 512 + (l & 15) * 32 + (l >> 4) * 8) = u32x2{pk(r[0], r[1]), pk(r[2], r[3])};
    }
    if (CLS >= 4) WAITV(5); else WAITV(4);
    if (CLS == 0) {
      const int r16 = l & 15, g = l >> 4, l16 = l * 16;
      LAS const char* fq = lds + CL_IMG + (l >> 2) * 32 + (l & 1) * 8;
      const int po = opaque(g * 128 + (r16 & 7) * 16 + (r16 >> 3) * 8), pmo = opaque((r16 >> 3) * XP_COPY + (r16 & 7) * 16 + (g & 1) * 8 + (g >> 1) * 4);
      const int ps = XP_OFF + (s & 3) * XP_SLOT + c.fa, pst = ps + (h >> 4) * 16384 + (h & 15) * 512;
      WAITL(0);
      STAMP(2);
      bar();
      STAMP(3);
      u32x4 Fq[4], Fk[4], KX[8];
      auto rdq = [&](int ks) { const u32x2 lo = trread(fq + ks * 1024), hi = trread(fq + ks * 1024 + 512); Fq[ks] = u32x4{lo.x, lo.y, hi.x, hi.y}; };
      auto rdk = [&](int ks) { const u32x2 lo = trread(fq + 16 + ks * 1024), hi = trread(fq + 16 + ks * 1024 + 512); Fk[ks] = u32x4{lo.x, lo.y, hi.x, hi.y}; };
      auto rdx = [&](int ks) { KX[ks] = lds128(lds + CL_KX + ks * 1024 + l16); KX[4 + ks] = lds128(lds + CL_KX + (4 + ks) * 1024 + l16); };
      rdq(0); rdx(0); rdk(0); rdq(1); rdx(1); rdk(1); rdq(2);
      SCHED();
      f32x4 s0 = {0.f, 0.f, 0.f, 0.f}, s1 = s0, sm = s0;
#pragma unroll
      for (int ks = 0; ks < 4; ++ks) {
        s0 = mfma(KX[ks], Fq[ks], s0);
        s1 = mfma(KX[4 + ks], Fq[ks], s1);
        sm = mfma(Fk[ks], Fq[ks], sm);
        SCHED();
        if (ks == 0) { rdx(2); rdk(2); rdq(3); }
        if (ks == 1) { rdx(3); rdk(3); }
        SCHED();
      }
      const bool hf = r16 >= 8;
      float v[5];
#pragma unroll
      for (int i = 0; i < 4; ++i) v[i] = (hf ? s1[i] : s0[i]) * SM_SC;
      float mx = fmx(fmx(v[0], v[1]), fmx(v[2], v[3]));
      { const float a = hf ? sm[1] : sm[0], b = hf ? sm[3] : sm[2]; v[4] = (g >= 2 ? b : a) * SM_SC; }
      mx = fmx(mx, v[4]);
      mx = fmx(mx, ror8(mx));
      { float a = mx, b = mx; swap16(a, b); mx = fmx(a, b); a = mx; b = mx; swap32(a, b); mx = fmx(a, b); }
#pragma unroll
      for (int i = 0; i < 5; ++i) v[i] = __builtin_amdgcn_exp2f(v[i] - mx);
      float sum = ((v[0] + v[1]) + (v[2] + v[3])) + v[4];
      sum += ror8(sum);
      const float inv = __builtin_amdgcn_rcpf(xsum1632(sum));
      const u32x2 pt = {san(pk(v[0] * inv, v[1] * inv)), san(pk(v[2] * inv, v[3] * inv))};
#pragma unroll
      for (int cp = 0; cp < 4; ++cp) st64(pt, c.rx, po, pst + cp * XP_COPY);
      v[4] *= inv;
      const float ot = ror8(v[4]);
      const unsigned pm = san(pk(hf ? ot : v[4], hf ? v[4] : ot));
      st32(pm, c.rx, pmo, ps + 32768 + h * 128);
      st32(pm, c.rx, pmo, ps + 32768 + h * 128 + 2 * XP_COPY);
      WAITV(6);
#pragma unroll
      for (int i = 0; i < 4; ++i) u[i] = lds128(lds + CL_U0 + i * 1024 + l16);
    } else {
      WAITL(0);
      bar();
      if (CLS == 3) {
        const int ps = XP_OFF + ((s + 2) & 3) * XP_SLOT + c.fa;
        const u32x4 sv = sent4();
        rearm_m(c, s, l);
        st128(sv, c.rx, (l >> 5) * XP_COPY + (l & 31) * 16, ps + (h >> 4) * 16384 + (h & 15) * 512);
        st128(sv, c.rx, (l >> 5) * XP_COPY + (l & 31) * 16, ps + (h >> 4) * 16384 + (h & 15) * 512 + 2 * XP_COPY);
        st128(sv, c.rx, ((l >> 3) & 3) * XP_COPY + (l & 7) * 16, ps + 32768 + h * 128);
      }
      asm volatile("s_waitcnt vmcnt(0)" : "+v"(u[0]), "+v"(u[1]), "+v"(u[2]), "+v"(u[3]) : "v"(wd) : "memory");
    }
    STAMP(4);
    if (last) break;
    l = opaque(l);
    u32x4 y;
    if (!o_pass(p, c, s, lds, l, t_m, u, y ST_ARG)) return false;
    if (CLS == 1) {
#pragma unroll
      for (int ks = 0; ks < 4; ++ks) u[ks] = ldsc1(rqk, l * 16 + ks * 1024, ((2 * s + w - 1) * 256 + h * 4) * 1024);
    }
    t_r = o_finish<CLS == 4, (CLS >= 4)>(p, c, s, lds, l, y ST_ARG);
    if (CLS == 1) p_tok(rpt, s, h, w - 1, lds, opaque(l), u);
    if (CLS >= 4) {
      wait_until(t_r + DA);
      STAMP(0);
      unsigned mspins;
      if (!wait_m(c, s + 1, lds, l * 16, mspins, y)) return false;
      STAMP(8); STAMPV(9, mspins);
    }
  }
  if (CLS == 1) {
    u32x4 q[4];
#pragma unroll
    for (int ks = 0; ks < 4; ++ks) q[ks] = ldsc1(rqk, l * 16 + ks * 1024, ((2 * (NSEG - 1) + w - 1) * 256 + h * 4) * 1024);
    p_tok(rpt, NSEG - 1, h, w - 1, lds, l, q);
  }
  return true;
}

__device__ __forceinline__ bool role_qk(const Params& p, int h, LAS char* lds) {
  const Chain c = chain_init(p, h);
  const int w = c.w, l = threadIdx.x & 63;
  u32x8 W[NREG];
  {
    const rsrc_t rq = mkrsrc(p.Wq + h * DH), rk = mkrsrc(p.Wk + h * DH);
    LAS char* wl = lds + CL_W + w * (NLDS * 2048) + l * 16;
    load_weights(lds, w, l,
      [&](int i, LAS char* dst) {
        if (i < 24) { const int t = 2 * (i / 3) + (i % 3 == 2); wstage(i % 3 == 1 ? rk : rq, t * 64 * 8192, 8192, dst, w, l); }
        else wstage(rk, (2 * (i - 24) + 1) * 64 * 8192, 8192, dst, w, l);
      },
      [&](int i, u32x8 f) { if (i < 24) W[i < 24 ? i : 0] = f; else put_frag(wl, i - 24, f); });
  }
  stage_m0(p, lds, w, l);
  if (w == 0) return qk_loop<0>(p, c, h, lds, W);
  if (w < 3) return qk_loop<1>(p, c, h, lds, W);
  if (w == 3) return qk_loop<3>(p, c, h, lds, W);
  if (w == 4) return qk_loop<4>(p, c, h, lds, W);
  return qk_loop<5>(p, c, h, lds, W);
}

template <int CLS>
__device__ __forceinline__ bool vy_loop(const Params& p, const Chain& c, int h, LAS char* lds, const u32x8 (&W)[NREG]) {
  const int w = c.w;
  int l = threadIdx.x & 63;
  STAMP_DECL;
  unsigned t_m = 0, t_r = 0;
  for (int s = 0;; ++s) {
    l = opaque(l);
    const bool last = s + 1 == NSEG;
    const int l16 = l * 16;
    const int idx = opaque((l & 8) ? 0xEEEE : 0x4444);
    LAS const char* wl = lds + CL_W + w * (NLDS * 2048) + l16;
    const int jst = last ? 8192 : 16384;
    const rsrc_t ryo = last ? c.ry : c.rx;
    const int ys = (last ? s * (int)Y_STEP : XY_OFF + (s & 1) * XY_SLOT + c.fb) + w * jst + h * 256;
    f32x4 av = zero4();
    WAITL(0);
    bar();
    u32x4 A[6];
#pragma unroll
    for (int t = 0; t < 6; ++t) A[t] = lds128(lds + CL_M + t * 1024 + l16);
    t_m = now32();
    STAMP(1);
    SCHED();
    u32x4 u[4];
    const int uso = s * (int)U_STEP + c.j * 32768 + w * 4096;
    u32x8 B[2];
#pragma unroll
    for (int t = 0; t < 16; ++t) {
      av = smfma(A[t % 6], W[t], av, idx);
      SCHED();
      if (t + 6 < 16) A[t % 6] = lds128(lds + CL_M + (t + 6) * 1024 + l16);
      if (t == 11) B[0] = get_frag(wl, 0);
      if (t == 13) B[1] = get_frag(wl, 1);
      if (t == 1) load_u<0>(u[0], c, uso, l16);
      if (t == 5) load_u<1>(u[1], c, uso, l16);
      if (t == 9) load_u<2>(u[2], c, uso, l16);
      if (t == 13) load_u<3>(u[3], c, uso, l16);
      SCHED();
    }
    l = opaque(l);
    const int r16 = l & 15, g = l >> 4;
    {
      float r[4];
#pragma unroll
      for (int i = 0; i < 4; ++i) { float a = av[i], b = av[i]; swap32(a, b); r[i] = a + b; }
      *(LAS u32x2*)(lds + CL_IMG + w * 256 + r16 * 16 + (g & 1) * 8) = u32x2{pk(r[0], r[1]), pk(r[2], r[3])};
    }
    SCHED();
    LAS const char* fimg = lds + opaque(CL_IMG + (16 * g + (r16 >> 2) + 8 * ((r16 >> 1) & 1)) * 16 + (r16 & 1) * 8);
    const int yo = opaque((2 * (g & 1) + (g >> 1)) * 8 * jst + r16 * 16);
    f32x4 y0 = zero4(), y1 = zero4();
    WAITL(0);
    STAMP(2);
    bar();
    STAMP(3);
    u32x4 Av[2];
#pragma unroll
    for (int kb = 0; kb < 2; ++kb) {
      const u32x2 lo = trread(fimg + kb * 1024), hi = trread(fimg + kb * 1024 + 64);
      Av[kb] = u32x4{lo.x, lo.y, hi.x, hi.y};
    }
    SCHED();
    unsigned yp[2][2];
#pragma unroll
    for (int cp = 0; cp < 4; ++cp) {
      y0 = smfma(Av[0], W[16 + 2 * cp], y0, idx);
      y1 = smfma(Av[0], W[17 + 2 * cp], y1, idx);
      y0 = smfma(Av[1], B[0], y0, idx);
      y1 = smfma(Av[1], B[1], y1, idx);
      SCHED();
      if (cp < 3) { B[0] = get_frag(wl, 2 * cp + 2); B[1] = get_frag(wl, 2 * cp + 3); }
      SCHED();
#pragma unroll
      for (int i = 0; i < 4; ++i) { float a = y0[i], b = y1[i]; swap32(a, b); y0[i] = a + b; }
      yp[cp & 1][0] = san(pk(y0[0], y0[1])); yp[cp & 1][1] = san(pk(y0[2], y0[3]));
      SCHED();
      if (cp < 3) { y0 = zero4(); y1 = zero4(); }
      if (cp & 1) {
        swap16u(yp[0][0], yp[1][0]); swap16u(yp[0][1], yp[1][1]);
        st128(u32x4{yp[0][0], yp[0][1], yp[1][0], yp[1][1]}, ryo, yo, ys + 32 * (cp >> 1) * jst);
      }
      SCHED();
    }
    STAMP(4);
    asm volatile("s_waitcnt vmcnt(2)" : "+v"(u[0]), "+v"(u[1]), "+v"(u[2]), "+v"(u[3])::"memory");
    if (last) break;
    l = opaque(l);
    if (CLS == 0 && w == 3) rearm_m(c, s, l);
    u32x4 y;
    if (!o_pass(p, c, s, lds, l, t_m, u, y ST_ARG)) return false;
    t_r = o_finish<CLS == 4, (CLS >= 4)>(p, c, s, lds, l, y ST_ARG);
    if (CLS >= 4) {
      wait_until(t_r + DA);
      STAMP(0);
      unsigned mspins;
      if (!wait_m(c, s + 1, lds, l * 16, mspins, y)) return false;
      STAMP(8); STAMPV(9, mspins);
    }
  }
  return true;
}

__device__ __forceinline__ bool role_vy(const Params& p, int h, LAS char* lds) {
  const Chain c = chain_init(p, 32 + h);
  const int w = c.w, l = threadIdx.x & 63;
  u32x8 W[NREG];
  {
    const rsrc_t rv = mkrsrc(p.Wv + h * DH), ro = mkrsrc(p.Wo + (size_t)h * DH * DM);
    LAS char* wl = lds + CL_W + w * (NLDS * 2048) + l * 16;
    load_weights(lds, w, l,
      [&](int i, LAS char* dst) {
        if (i < 16) wstage(rv, i * 64 * 8192, 8192, dst, w, l);
        else if (i < 24) wstage(ro, (i - 16) * 256, 2048, dst, w, l);
        else wstage(ro, 64 * 2048 + (i - 24) * 256, 2048, dst, w, l);
      },
      [&](int i, u32x8 f) { if (i < 24) W[i < 24 ? i : 0] = f; else put_frag(wl, i - 24, f); });
  }
  stage_m0(p, lds, w, l);
  if (w < 4) return vy_loop<0>(p, c, h, lds, W);
  if (w == 4) return vy_loop<4>(p, c, h, lds, W);
  return vy_loop<5>(p, c, h, lds, W);
}

#define PLACE 4
__global__ __launch_bounds__(512, 2) void k_chain(Params p) {
  LAS char* lds = (LAS char*)smem;
  int b = blockIdx.x;
  if ((b & 7) >= PLACE || (b >> 3) >= 64 / PLACE) return;
  b = (b >> 3) * PLACE + (b & 7);
  const int h = (b / (2 * PLACE)) * PLACE + b % PLACE;
  const bool ok = ((b / PLACE) & 1) == 0 ? role_qk(p, h, lds) : role_vy(p, h, lds);
  if (!ok) __hip_atomic_store((unsigned*)(p.ws + OFF_CTRL), 1u, __ATOMIC_RELAXED, __HIP_MEMORY_SCOPE_AGENT);
}

extern "C" void kernel_launch(void* const* d_in, const int* in_sizes, int n_in, void* d_out, int out_size, void* d_ws, size_t ws_size,
                              hipStream_t stream) {
  Params p;
  p.x = (const bf16_t*)d_in[0]; p.mem0 = (const bf16_t*)d_in[1]; p.Wq = (const bf16_t*)d_in[2]; p.Wk = (const bf16_t*)d_in[3];
  p.Wv = (const bf16_t*)d_in[4]; p.Wo = (const bf16_t*)d_in[5]; p.w1 = (const bf16_t*)d_in[8]; p.w2 = (const bf16_t*)d_in[9];
  p.w3 = (const bf16_t*)d_in[10]; p.w4 = (const bf16_t*)d_in[11]; p.c = (const bf16_t*)d_in[12]; p.wmod = (const bf16_t*)d_in[13];
  p.out = (bf16_t*)d_out; p.ws = (unsigned char*)d_ws;
  auto big = [](const void* f, int bytes) { (void)hipFuncSetAttribute(f, hipFuncAttributeMaxDynamicSharedMemorySize, bytes); };
  auto k1 = k_mlp<1, 128, 64, 4, 2, 4>; auto k2 = k_mlp<2, 128, 64, 4, 2, 4>; auto k3 = k_mlp<3, 256, 128, 4, 2, 3>; auto k4 = k_mlp<4, 128, 64, 4, 2, 4>;
  big((const void*)k_qk, 131072); big((const void*)k_vu, 147456); big((const void*)k_chain, CL_END); big((const void*)k_pz, 81920);
  big((const void*)k1, 98304); big((const void*)k2, 98304); big((const void*)k3, 147456); big((const void*)k4, 98304);
  (void)hipMemsetAsync(p.ws + OFF_CTRL, 0, 65536, stream);
  (void)hipMemsetAsync(p.ws + OFF_X, 0xFF, X_BYTES, stream);
  k_mod<<<195, 512, 0, stream>>>(p);
  k_ln<0><<<256, 512, 0, stream>>>(p);
  k_qk<<<256, 512, 131072, stream>>>(p);
  k_vu<<<256, 512, 147456, stream>>>(p);
  k_chain<<<256, 512, CL_END, stream>>>(p);
  k_pz<<<256, 512, 81920, stream>>>(p);
  k_ln<1><<<256, 512, 0, stream>>>(p);
  k1<<<256, 512, 98304, stream>>>(p);
  k2<<<256, 512, 98304, stream>>>(p);
  k_ln<2><<<256, 512, 0, stream>>>(p);
  k3<<<224, 512, 147456, stream>>>(p);
  k4<<<256, 512, 98304, stream>>>(p);
}
```

```cpp
#include <hip/hip_runtime.h>
#include <stdint.h>

#define LAS __attribute__((address_space(3)))
typedef unsigned short bf16_t;
typedef __bf16 bfx8 __attribute__((ext_vector_type(8)));
typedef __bf16 bfx16 __attribute__((ext_vector_type(16)));
typedef __bf16 bfx2 __attribute__((ext_vector_type(2)));
typedef short s16x4 __attribute__((ext_vector_type(4)));
typedef float f32x4 __attribute__((ext_vector_type(4)));
typedef float f32x2 __attribute__((ext_vector_type(2)));
typedef unsigned u32x2 __attribute__((ext_vector_type(2)));
typedef unsigned u32x4 __attribute__((ext_vector_type(4)));
typedef unsigned u32x8 __attribute__((ext_vector_type(8)));
typedef __amdgpu_buffer_rsrc_t rsrc_t;

constexpr int SEQ = 2048, DM = 1024, NH = 32, DH = 128, NSEG = 64, FF = 3584;
constexpr float LN_EPS = 1e-5f;
constexpr unsigned SENT = 0xFFFFFFFFu;
constexpr int AUX_SC1 = 16;

constexpr int ACT_BYTES = SEQ * DM * 2, W_BYTES = DM * NH * DH * 2, WS_BYTES = 256 << 20;
constexpr int OFF_CTRL = 0;
constexpr int OFF_MOD = 65536;
constexpr int OFF_X = 131072, PG = 2097152;
constexpr int XM_SLOT = 131072, XM_COPY = 32768;
constexpr int XV_OFF = 524288, XV_SLOT = 131072;
constexpr int XO_OFF = PG, XO_SLOT = 524288, XO_COPY = 131072;
constexpr int OFF_QKV = OFF_X + 3 * PG;
constexpr int QKV_S = 24576, QKV_H = 64 * QKV_S + 4096;
constexpr int OFF_OT = OFF_QKV + 32 * QKV_H;
constexpr int OFF_H = OFF_OT + SEQ * 4096 * 2;
constexpr int OFF_X1 = OFF_H + ACT_BYTES;
constexpr int OFF_H2 = OFF_X1 + ACT_BYTES;
constexpr int OFF_N2 = OFF_H2 + ACT_BYTES;
constexpr int OFF_G = OFF_N2 + ACT_BYTES;
constexpr int OFF_Y1 = OFF_G + ACT_BYTES + 4 * DM * 2;
constexpr int OFF_N3 = OFF_Y1 + ACT_BYTES;
constexpr int OFF_G3 = OFF_N3 + ACT_BYTES;
constexpr int OFF_MDBG = OFF_G3 + SEQ * FF * 2;
constexpr int WS_END = OFF_MDBG + 65 * 16384;
static_assert(WS_END + (8 << 20) <= WS_BYTES, "workspace");

struct Params {
  const bf16_t *x, *mem0, *Wq, *Wk, *Wv, *Wo, *w1, *w2, *w3, *w4, *c, *wmod;
  bf16_t* out;
  unsigned char* ws;
};
typedef const __attribute__((address_space(4))) Params* KP;
__device__ __forceinline__ KP kparams() { KP k = (KP)__builtin_amdgcn_kernarg_segment_ptr(); asm volatile("" : "+s"(k)); return k; }

__device__ __forceinline__ float bf2f(unsigned short b) { return __builtin_bit_cast(float, (unsigned)b << 16); }
__device__ __forceinline__ float bflo(unsigned u) { return __builtin_bit_cast(float, u << 16); }
__device__ __forceinline__ float bfhi(unsigned u) { return __builtin_bit_cast(float, u & 0xFFFF0000u); }
__device__ __forceinline__ unsigned pk(float lo, float hi) { bfx2 v; v[0] = (__bf16)lo; v[1] = (__bf16)hi; return __builtin_bit_cast(unsigned, v); }
__device__ __forceinline__ float rbf(float x) { return bflo(pk(x, 0.f)); }
__device__ __forceinline__ unsigned san(unsigned u) { return u < 0xFFFFFFFEu ? u : 0xFFFFFFFEu; }
__device__ __forceinline__ f32x4 mfma(u32x4 a, u32x4 b, f32x4 c) {
  return __builtin_amdgcn_mfma_f32_16x16x32_bf16(__builtin_bit_cast(bfx8, a), __builtin_bit_cast(bfx8, b), c, 0, 0, 0);
}
__device__ __forceinline__ f32x4 mfma16(u32x2 a, u32x2 b, f32x4 c) {
  return __builtin_amdgcn_mfma_f32_16x16x16bf16_1k(__builtin_bit_cast(s16x4, a), __builtin_bit_cast(s16x4, b), c, 0, 0, 0);
}
__device__ __forceinline__ f32x4 smfma(u32x4 a, u32x8 b, f32x4 c, int idx) {
  return __builtin_amdgcn_smfmac_f32_16x16x64_bf16(__builtin_bit_cast(bfx8, a), __builtin_bit_cast(bfx16, b), c, idx, 0, 0);
}
__device__ __forceinline__ u32x2 trread(LAS const char* p) { return __builtin_bit_cast(u32x2, __builtin_amdgcn_ds_read_tr16_b64_v4i16((LAS s16x4*)p)); }
__device__ __forceinline__ u32x4 lds128(LAS const char* p) { return *(LAS const u32x4*)p; }
__device__ __forceinline__ u32x2 lds64(LAS const char* p) { return *(LAS const u32x2*)p; }
__device__ __forceinline__ rsrc_t mkrsrc(const void* p, int bytes) { return __builtin_amdgcn_make_buffer_rsrc((void*)p, 0, bytes, 0x00020000); }
__device__ __forceinline__ rsrc_t wsr(KP p) { return mkrsrc(p->ws, WS_BYTES); }
template <int AUX> __device__ __forceinline__ void glds(rsrc_t rs, LAS char* dst, int voff, int soff) {
  const unsigned d = (unsigned)(size_t)dst;
  if (AUX) asm volatile("s_mov_b32 m0, %0\n\ts_nop 0\n\tbuffer_load_dwordx4 %1, %2, %3 offen sc1 lds" ::"s"(d), "v"(voff), "s"(rs), "s"(soff) : "memory");
  else asm volatile("s_mov_b32 m0, %0\n\ts_nop 0\n\tbuffer_load_dwordx4 %1, %2, %3 offen lds" ::"s"(d), "v"(voff), "s"(rs), "s"(soff) : "memory");
}
__device__ __forceinline__ void swap16(float& a, float& b) {
  auto r = __builtin_amdgcn_permlane16_swap(__builtin_bit_cast(unsigned, a), __builtin_bit_cast(unsigned, b), false, false);
  a = __builtin_bit_cast(float, (unsigned)r[0]); b = __builtin_bit_cast(float, (unsigned)r[1]);
}
__device__ __forceinline__ void swap32(float& a, float& b) {
  auto r = __builtin_amdgcn_permlane32_swap(__builtin_bit_cast(unsigned, a), __builtin_bit_cast(unsigned, b), false, false);
  a = __builtin_bit_cast(float, (unsigned)r[0]); b = __builtin_bit_cast(float, (unsigned)r[1]);
}
__device__ __forceinline__ float xsum1632(float v) {
  float a = v, b = v; swap16(a, b); v = a + b;
  a = v; b = v; swap32(a, b); return a + b;
}
__device__ __forceinline__ float xmax1632(float v) {
  float a = v, b = v; swap16(a, b); v = __builtin_fmaxf(a, b);
  a = v; b = v; swap32(a, b); return __builtin_fmaxf(a, b);
}
template <int CTRL> __device__ __forceinline__ float dppf(float v) { return __builtin_bit_cast(float, __builtin_amdgcn_mov_dpp(__builtin_bit_cast(int, v), CTRL, 0xf, 0xf, true)); }
__device__ __forceinline__ float ror8(float v) { return dppf<0x128>(v); }
__device__ __forceinline__ float gelu_tanh(float x) {
  const float u = x * (1.0f + 0.044715f * x * x) * (-2.0f * 0.7978845608028654f * 1.4426950408889634f);
  return x * __builtin_amdgcn_rcpf(1.0f + __builtin_amdgcn_exp2f(u));
}
__device__ __forceinline__ int xoff(KP p) { return (int)((((size_t)(p->ws + OFF_X) + PG - 1) & ~(size_t)(PG - 1)) - (size_t)p->ws); }
__device__ __forceinline__ unsigned xcc_id() { unsigned x; asm volatile("s_getreg_b32 %0, hwreg(HW_REG_XCC_ID)" : "=s"(x)); return x & 7u; }
#define WAITV(n) asm volatile("s_waitcnt vmcnt(" #n ")" ::: "memory")
#define WAITL(n) asm volatile("s_waitcnt lgkmcnt(" #n ")" ::: "memory")
template <int N> __device__ __forceinline__ void waitv() { asm volatile("s_waitcnt vmcnt(%0)" ::"i"(N) : "memory"); }
__device__ __forceinline__ void bar() { asm volatile("" ::: "memory"); __builtin_amdgcn_s_barrier(); asm volatile("" ::: "memory"); }
#define SCHED() __builtin_amdgcn_sched_barrier(0)
__device__ __forceinline__ int opaque(int x) { asm volatile("" : "+v"(x)); return x; }
__device__ __forceinline__ int sgpr(int x) { int r; asm volatile("s_nop 0\n\tv_readfirstlane_b32 %0, %1\n\ts_nop 4" : "=s"(r) : "v"(x)); return r; }
__device__ __forceinline__ u32x4 ld128(rsrc_t rs, int voff, int soff) { return __builtin_amdgcn_raw_buffer_load_b128(rs, voff, soff, AUX_SC1); }
__device__ __forceinline__ u32x2 ld64(rsrc_t rs, int voff, int soff) { return __builtin_amdgcn_raw_buffer_load_b64(rs, voff, soff, AUX_SC1); }
__device__ __forceinline__ void st128(u32x4 v, rsrc_t rs, int voff, int soff) {
  __builtin_amdgcn_raw_buffer_store_b128(v, rs, voff, soff, AUX_SC1);
  asm volatile("s_nop 1" ::"v"(v) : "memory");
}
__device__ __forceinline__ void st64(u32x2 v, rsrc_t rs, int voff, int soff) { __builtin_amdgcn_raw_buffer_store_b64(v, rs, voff, soff, AUX_SC1); }
__device__ __forceinline__ u32x4 pk8(f32x4 a, f32x4 b) { return u32x4{pk(a[0], a[1]), pk(a[2], a[3]), pk(b[0], b[1]), pk(b[2], b[3])}; }
__device__ __forceinline__ void unpk8(u32x4 u, f32x4& a, f32x4& b) {
  a = f32x4{bflo(u[0]), bfhi(u[0]), bflo(u[1]), bfhi(u[1])}; b = f32x4{bflo(u[2]), bfhi(u[2]), bflo(u[3]), bfhi(u[3])};
}
__device__ __forceinline__ void rnd8(f32x4& a, f32x4& b) { unpk8(pk8(a, b), a, b); }
__device__ __forceinline__ void add8(f32x4& a, f32x4& b, u32x4 r) { f32x4 x, y; unpk8(r, x, y); a += x; b += y; }
#define RLX_AGENT __ATOMIC_RELAXED, __HIP_MEMORY_SCOPE_AGENT

constexpr int C_TMO = 0, C_TICKET = 1  , C_PROD = 9  , C_DONE = 25  , C_FIN = 89  , C_X1 = 121  , C_N2 = 153  , C_G = 185  ,
              C_Y1 = 217  , C_N3 = 249  , C_G3 = 281  , C_LINES = 313, C_PROBE = 320  ;
__device__ __forceinline__ unsigned* cword(KP p, int line) { return (unsigned*)(p->ws + OFF_CTRL) + line * 32; }
constexpr unsigned SPIN_CAP = 400000u;
#define WSLEEP 8

__global__ __launch_bounds__(512) void k_pre(Params) {
  const KP p = kparams();
  __shared__ float red[128][33];
  const int tid = threadIdx.x, b = blockIdx.x;
  if (b < 192) {
    const int cg = tid & 3, kr = tid >> 2;
    float acc[8];
#pragma unroll
    for (int i = 0; i < 8; ++i) acc[i] = 0.f;
    u32x4 w[8];
#pragma unroll
    for (int it = 0; it < 8; ++it) w[it] = *(const u32x4*)(p->wmod + (size_t)(it * 128 + kr) * 6144 + b * 32 + cg * 8);
#pragma unroll
    for (int it = 0; it < 8; ++it) {
      const float cv = bf2f(p->c[it * 128 + kr]);
      const float sv = rbf(cv / (1.0f + __expf(-cv)));
#pragma unroll
      for (int i = 0; i < 4; ++i) { acc[2 * i] += sv * bflo(w[it][i]); acc[2 * i + 1] += sv * bfhi(w[it][i]); }
    }
#pragma unroll
    for (int i = 0; i < 8; ++i) red[kr][cg * 8 + i] = acc[i];
    __syncthreads();
    if (tid < 128) {
      float sum = 0.f;
#pragma unroll
      for (int r = 0; r < 32; ++r) sum += red[(tid >> 5) * 32 + r][tid & 31];
      red[(tid >> 5) * 32][tid & 31] = sum;
    }
    __syncthreads();
    if (tid < 32) ((float*)(p->ws + OFF_MOD))[b * 32 + tid] = rbf((red[0][tid] + red[32][tid]) + (red[64][tid] + red[96][tid]));
  } else if (b < 202) {
    if (tid < 64) {
      const int v = b - 192;
      unsigned char* q = p->ws + xoff(p) + (v / 5) * PG + (v % 5) * 131072 + 1048576;
      if (tid < 10) __hip_atomic_store((unsigned*)(q + (tid & 1) * 8192 + (tid >> 1) * 16384), SENT, RLX_AGENT);
      WAITV(0);
      unsigned best[2] = {1u << 30, 1u << 30};
#pragma unroll
      for (int r = 0; r < 10; ++r) {
        unsigned d; unsigned long long t0, t1;
        asm volatile("s_memtime %0\n\ts_waitcnt lgkmcnt(0)" : "=s"(t0)::"memory");
        asm volatile("global_load_dword %0, %1, off sc1\n\ts_waitcnt vmcnt(0)" : "=v"(d) : "v"(q + (r & 1) * 8192 + (r >> 1) * 16384) : "memory");
        asm volatile("s_memtime %0\n\ts_waitcnt lgkmcnt(0)" : "=s"(t1)::"memory");
        best[r & 1] = min(best[r & 1], (unsigned)(t1 - t0));
      }
      if (tid == 0) cword(p, C_PROBE)[v] = (unsigned)(best[1] < best[0]) ^ (xcc_id() >> 2);
    }
  } else if (b == 202) {
    const u32x4 z = {0u, 0u, 0u, 0u};
    if (tid < 256) *(u32x4*)(p->ws + OFF_G + tid * 16) = z;
    else *(u32x4*)(p->ws + OFF_G + (SEQ + 2) * DM * 2 + (tid - 256) * 16) = z;
    if (tid < C_LINES) *cword(p, tid) = 0u;
  } else {
    const u32x4 sv = {SENT, SENT, SENT, SENT};
    unsigned char* x = p->ws + xoff(p);
    for (int i = (b - 203) * 512 + tid; i < (786432 + PG) / 16; i += 53 * 512) {
      const int o = i * 16;
      *(u32x4*)(x + (o < 786432 ? o : o + (PG - 786432))) = sv;
    }
  }
}

__device__ __forceinline__ float wsum(float v) {
  v += dppf<0xB1>(v);
  v += dppf<0x4E>(v);
  v += dppf<0x124>(v);
  v += dppf<0x128>(v);
  return xsum1632(v);
}
struct Row { f32x4 v[4]; };
__device__ __forceinline__ float hsum(f32x4 a) { return (a[0] + a[1]) + (a[2] + a[3]); }
__device__ __forceinline__ void norm_row(Row& r) {
  const float mean = wsum(hsum((r.v[0] + r.v[1]) + (r.v[2] + r.v[3]))) * (1.0f / DM);
  f32x4 q = {0.f, 0.f, 0.f, 0.f};
#pragma unroll
  for (int i = 0; i < 4; ++i) { const f32x4 d = r.v[i] - mean; q += d * d; }
  const float sd = rbf(sqrtf(rbf(rbf(wsum(hsum(q)) * (1.0f / DM)) + LN_EPS))), mb = rbf(mean), rs = 1.0f / sd;
#pragma unroll
  for (int i = 0; i < 4; ++i) r.v[i] -= mb;
  rnd8(r.v[0], r.v[1]); rnd8(r.v[2], r.v[3]);
#pragma unroll
  for (int i = 0; i < 4; ++i) { const f32x4 t = r.v[i] * rs; r.v[i] = __builtin_elementwise_fma(__builtin_elementwise_fma(-t, (f32x4)sd, r.v[i]), (f32x4)rs, t); }
}
template <int NR> __device__ __forceinline__ void ln_rows(KP p, int mode, int row0) {
  const int tid = opaque(threadIdx.x), l16 = (tid & 63) * 16, r0 = row0 + __builtin_amdgcn_readfirstlane(tid >> 6) * NR;
  const rsrc_t rw = wsr(p), rs = mode == 0 ? mkrsrc(p->x, ACT_BYTES) : rw;
  const int so = mode == 0 ? 0 : mode == 1 ? OFF_X1 : OFF_Y1;
  u32x4 a[NR], b[NR];
#pragma unroll
  for (int r = 0; r < NR; ++r) { a[r] = ld128(rs, l16, so + (r0 + r) * 2048); b[r] = ld128(rs, l16 + 1024, so + (r0 + r) * 2048); }
  if (mode < 2) {
    const float* sp = (const float*)(p->ws + OFF_MOD) + (mode == 0 ? 0 : 3) * DM + l16 / 2;
    Row sc, sh;
#pragma unroll
    for (int i = 0; i < 4; ++i) { sh.v[i] = *(const f32x4*)(sp + (i >> 1) * 512 + (i & 1) * 4); sc.v[i] = *(const f32x4*)(sp + DM + (i >> 1) * 512 + (i & 1) * 4) + 1.0f; }
    rnd8(sc.v[0], sc.v[1]); rnd8(sc.v[2], sc.v[3]);
#pragma unroll
    for (int r = 0; r < NR; ++r) {
      Row v;
      unpk8(a[r], v.v[0], v.v[1]); unpk8(b[r], v.v[2], v.v[3]);
      norm_row(v);
      rnd8(v.v[0], v.v[1]); rnd8(v.v[2], v.v[3]);
#pragma unroll
      for (int i = 0; i < 4; ++i) v.v[i] *= sc.v[i];
      rnd8(v.v[0], v.v[1]); rnd8(v.v[2], v.v[3]);
#pragma unroll
      for (int i = 0; i < 4; ++i) v.v[i] += sh.v[i];
      a[r] = pk8(v.v[0], v.v[1]); b[r] = pk8(v.v[2], v.v[3]);
    }
#pragma unroll
    for (int r = 0; r < NR; ++r) { const int o = (mode == 0 ? OFF_H : OFF_H2) + (r0 + r) * 2048; st128(a[r], rw, l16, o); st128(b[r], rw, l16 + 1024, o); }
  }
  if (mode > 0) {
#pragma unroll
    for (int r = 0; r < NR; ++r) {
      Row v;
      unpk8(a[r], v.v[0], v.v[1]); unpk8(b[r], v.v[2], v.v[3]);
      norm_row(v);
      a[r] = pk8(v.v[0], v.v[1]); b[r] = pk8(v.v[2], v.v[3]);
    }
#pragma unroll
    for (int r = 0; r < NR; ++r) { const int o = (mode == 1 ? OFF_N2 : OFF_N3) + (r0 + r) * 2048; st128(a[r], rw, l16, o); st128(b[r], rw, l16 + 1024, o); }
  }
}
__global__ __launch_bounds__(512) void k_ln1(Params) { ln_rows<1>(kparams(), 0, blockIdx.x * 8); }

struct GemmArgs { rsrc_t ra, rb; int a_off, lda, b_off, ldb, nk, shift; };

template <int BM, int BN, int WM, int WN, int NST> __device__ __forceinline__ void gemm_tile(LAS char* lds, const GemmArgs& ga) {
  constexpr int TM = BM / WM / 16, TN = BN / WN / 16;
  constexpr int A_BYTES = BM * 128, B_BYTES = BN * 128, ST_BYTES = A_BYTES + B_BYTES;
  constexpr int LA = BM / 64, LB = BN / 64, LD = LA + LB;
  constexpr int RB = BN * 2;
  constexpr int NMMA = TM * TN, NRD = TM + 2 * TN;
  static_assert(WM * WN == 8 && (BN == 64 || BN == 128 || BN == 256) && BM % 64 == 0 && NST * ST_BYTES <= 163840, "shape");
  const int tid = opaque(threadIdx.x), w = __builtin_amdgcn_readfirstlane(tid >> 6), l = tid & 63, r16 = l & 15, g = l >> 4;
  const int wm = w / WN, wn = w % WN, lda = ga.lda, ldb = ga.ldb, nk = ga.nk;
  f32x4 acc[TN][TM];
#pragma unroll
  for (int i = 0; i < TN; ++i)
#pragma unroll
    for (int j = 0; j < TM; ++j) acc[i][j] = f32x4{0.f, 0.f, 0.f, 0.f};
  const int va = (8 * w + (l >> 3)) * lda + (((l & 7) ^ ((4 * w + (l >> 4)) & 7)) << 4);
  int vb;
  if (BN == 256) { const int k = 2 * w + (l >> 5), f = (k & 3) | (((k >> 3) & 1) << 2); vb = k * ldb + (((l & 31) ^ (f << 1)) << 4); }
  else if (BN == 128) { const int k = 4 * w + (l >> 4), f = (k & 3) | (((k >> 3) & 1) << 2); vb = k * ldb + (((l & 15) ^ (f << 1)) << 4); }
  else { const int k = 8 * w + (l >> 3), f = ((k >> 1) & 1) | (((k >> 3) & 1) << 1); vb = k * ldb + (((l & 7) ^ (f << 1)) << 4); }
  constexpr int BROWS = 8192 / RB;
  auto piece = [&](int i, int sa, int sb, LAS char* dst) {
    if (i < LA) glds<AUX_SC1>(ga.ra, dst + i * 8192, va, sa + i * 64 * lda);
    else glds<0>(ga.rb, dst + A_BYTES + (i - LA) * 8192, vb, sb + (i - LA) * BROWS * ldb);
  };
  auto aoff = [&](int kt) { const int qd = kt >> 2; return ga.a_off + kt * 128 + ((qd & 1) ? 1 : -1) * ((qd >> 1) + 1) * ga.shift; };
  const int fa = (wm * TM * 16 + r16) * 128 + ((g ^ (r16 >> 1)) << 4);
  const int q = r16 >> 2, pp = r16 & 3;
  int fb[TN];
#pragma unroll
  for (int ni = 0; ni < TN; ++ni) {
    const int nt = wn * TN + ni;
    const int f = BN == 64 ? (((q >> 1) & 1) | ((g & 1) << 1)) : (q | ((g & 1) << 2));
    fb[ni] = A_BYTES + (8 * g + q) * RB + ((nt ^ f) << 5) + pp * 8;
  }
  u32x4 A0[TM], A1[TM];
  u32x2 B0[TN][2], B1[TN][2];
  auto rd = [&](int i, LAS const char* base, int ks, u32x4 (&Af)[TM], u32x2 (&Bf)[TN][2]) {
    if (i < 2 * TN) Bf[i >> 1][i & 1] = trread(base + fb[i >> 1] + (32 * ks + 4 * (i & 1)) * RB);
    else Af[i - 2 * TN] = lds128(base + (fa ^ (ks * 64)) + (i - 2 * TN) * 2048);
  };
  auto mm = [&](int i, const u32x4 (&Af)[TM], const u32x2 (&Bf)[TN][2]) {
    const int mi = i / TN, ni = i % TN;
    acc[ni][mi] = mfma(u32x4{Bf[ni][0].x, Bf[ni][0].y, Bf[ni][1].x, Bf[ni][1].y}, Af[mi], acc[ni][mi]);
  };
#pragma nounroll
  for (int s = 0; s < NST; ++s)
#pragma unroll
    for (int i = 0; i < LD; ++i) piece(i, aoff(s), ga.b_off + s * 64 * ldb, lds + s * ST_BYTES + w * 1024);
  waitv<(NST - 1) * LD>();
  bar();
#pragma unroll
  for (int i = 0; i < NRD; ++i) rd(i, lds, 0, A0, B0);
  int buf = 0;
#pragma nounroll
  for (int kt = 0; kt < nk; ++kt) {
    LAS const char* cur = lds + buf * ST_BYTES;
#pragma unroll
    for (int i = 0; i < NMMA; ++i) {
      mm(i, A0, B0);
#pragma unroll
      for (int r = 0; r < NRD; ++r) if (r * (NMMA * 3 / 4) / NRD == i) rd(r, cur, 1, A1, B1);
      SCHED();
    }
    const bool more = kt + NST < nk;
    WAITL(0);
    if (kt + NST <= nk) waitv<(NST - 2) * LD>(); else WAITV(0);
    bar();
    const int sa = aoff(kt + NST), sb = ga.b_off + (kt + NST) * 64 * ldb;
    LAS char* dst = lds + buf * ST_BYTES + w * 1024;
    buf = buf + 1 == NST ? 0 : buf + 1;
    LAS const char* nxt = lds + buf * ST_BYTES;
#pragma unroll
    for (int i = 0; i < NMMA; ++i) {
      mm(i, A1, B1);
#pragma unroll
      for (int r = 0; r < NRD; ++r) if (r * (NMMA * 3 / 4) / NRD == i) rd(r, nxt, 0, A0, B0);
#pragma unroll
      for (int r = 0; r < LD; ++r) if ((r * NMMA + NMMA / 2) / LD == i && more) piece(r, sa, sb, dst);
      SCHED();
    }
  }
  LAS char* tw = lds + (wm * TM * 16 + r16) * (2 * BN + 16) + (wn * TN * 16 + 4 * g) * 2;
#pragma unroll
  for (int mi = 0; mi < TM; ++mi)
#pragma unroll
    for (int ni = 0; ni < TN; ++ni)
      *(LAS u32x2*)(tw + mi * 16 * (2 * BN + 16) + ni * 32) = u32x2{pk(acc[ni][mi][0], acc[ni][mi][1]), pk(acc[ni][mi][2], acc[ni][mi][3])};
  WAITL(0);
  bar();
}

extern __shared__ __attribute__((aligned(16))) char smem[];

__device__ __forceinline__ void epi_qkv(KP p, LAS char* lds, int rb, int cb) {
  constexpr int PITCH = 528;
  const int tid = opaque(threadIdx.x), w = __builtin_amdgcn_readfirstlane(tid >> 6), l = tid & 63, r16 = l & 15, g = l >> 4;
  const int which = cb >> 4, sl = w >> 1, hh = w & 1;
  const rsrc_t rw = wsr(p);
  const int so = OFF_QKV + ((cb & 15) * 2 + hh) * QKV_H + (rb * 4 + sl) * QKV_S + which * 8192;
  LAS const char* t = lds + sl * 32 * PITCH + hh * 256;
  if (which < 2) {
    t += r16 * PITCH + g * 8;
#pragma unroll 2
    for (int f = 0; f < 8; ++f) {
      const u32x2 lo = lds64(t + (f >> 2) * 16 * PITCH + (f & 3) * 64), hi = lds64(t + (f >> 2) * 16 * PITCH + (f & 3) * 64 + 32);
      st128(u32x4{lo.x, lo.y, hi.x, hi.y}, rw, l * 16, so + f * 1024);
    }
  } else {
    t += (4 * g + (r16 >> 2)) * PITCH + (r16 & 3) * 8;
#pragma unroll 2
    for (int f = 0; f < 8; ++f) {
      const u32x2 lo = trread(t + f * 32), hi = trread(t + 16 * PITCH + f * 32);
      st128(u32x4{lo.x, lo.y, hi.x, hi.y}, rw, l * 16, so + f * 1024);
    }
  }
}

struct EpiArgs { rsrc_t r1, ro; int kind, nit, lgn, r1_off, r2_off, o_off, ldo, gate; };
__device__ __forceinline__ void epi_rows(KP p, LAS char* lds, const EpiArgs& e) {
  const int tid = opaque(threadIdx.x), cu = tid & ((1 << e.lgn) - 1), row = tid >> e.lgn, rstep = 512 >> e.lgn;
  const rsrc_t rw = wsr(p);
  const int pitch = (16 << e.lgn) + 16;
  LAS const char* t = lds + row * pitch + cu * 16;
  const int vi = row * 2048 + cu * 16, vo = row * e.ldo + cu * 16;
  f32x4 g0, g1;
  if (e.kind >= 2) { const float* gp = (const float*)(p->ws + OFF_MOD) + e.gate + cu * 8; g0 = *(const f32x4*)gp; g1 = *(const f32x4*)(gp + 4); }
  if (e.kind == 0) {
#pragma nounroll
    for (int it = 0; it < e.nit; ++it) {
      f32x4 a, b;
      unpk8(lds128(t + it * rstep * pitch), a, b);
#pragma unroll
      for (int i = 0; i < 4; ++i) { a[i] = gelu_tanh(a[i]); b[i] = gelu_tanh(b[i]); }
      st128(pk8(a, b), e.ro, vo, e.o_off + it * rstep * e.ldo);
    }
  } else {
    u32x4 r1[4], r2[4];
#pragma unroll
    for (int it = 0; it < 4; ++it)
      if (it < e.nit) {
        r1[it] = ld128(e.r1, vi, e.r1_off + it * rstep * 2048);
        if (e.kind == 3) r2[it] = ld128(rw, vi, e.r2_off + it * rstep * 2048);
      }
#pragma unroll
    for (int it = 0; it < 4; ++it)
      if (it < e.nit) {
        f32x4 a, b;
        unpk8(lds128(t + it * rstep * pitch), a, b);
        if (e.kind == 3) { add8(a, b, r1[it]); rnd8(a, b); r1[it] = r2[it]; }
        if (e.kind >= 2) { a *= g0; b *= g1; rnd8(a, b); }
        add8(a, b, r1[it]);
        st128(pk8(a, b), e.ro, vo, e.o_off + it * rstep * e.ldo);
      }
  }
}

constexpr int NREG = 24, NLDS = 8;
constexpr int CL_M = 0;
constexpr int CL_KX = 16384;
constexpr int CL_IMG = 24576;
constexpr int CL_PX = 28672;
constexpr int CL_W = 32768;
constexpr int LDS_BYTES = 163840;
#define STAMPS 0
#define MDBG 0
#define STAMP(k)
#define STAMPV(k, v)
#define STAMP_DECL
#define STAMP_FLUSH(blk, s)
#define DB 3500
#define DA 800
#define DQ 3700
#define DRETRY 3

__device__ __forceinline__ unsigned umax3(unsigned a, unsigned b, unsigned c) { return max(max(a, b), c); }
__device__ __forceinline__ unsigned umax4(u32x4 v) { return max(umax3(v.x, v.y, v.z), v.w); }
__device__ __forceinline__ bool none(bool x) { return __builtin_amdgcn_ballot_w64(x) == 0ull; }
__device__ __forceinline__ unsigned now32() { unsigned long long t; asm volatile("s_memtime %0\n\ts_waitcnt lgkmcnt(0)" : "=s"(t)::"memory"); return (unsigned)t; }
__device__ __forceinline__ void wait_until(unsigned t) {
#pragma nounroll
  while ((int)(now32() - t) < 0) {}
}
__device__ __forceinline__ f32x4 zero4() { f32x2 a, b; asm volatile("v_mov_b64 %0, 0\n\tv_mov_b64 %1, 0\n\ts_nop 1" : "=v"(a), "=v"(b)); return f32x4{a.x, a.y, b.x, b.y}; }
__device__ __forceinline__ u32x4 sent4() { u32x2 a, b; asm volatile("s_nop 1\n\tv_mov_b64 %0, -1\n\tv_mov_b64 %1, -1" : "=v"(a), "=v"(b)); return u32x4{a.x, a.y, b.x, b.y}; }

__device__ __forceinline__ void wstage(rsrc_t rw, int soff, LAS char* dst, int w, int l) {
  const int r = 4 * w + (l >> 4);
  const int f = (r & 1) | (((r >> 2) & 3) << 1);
  const int v = r * 8192 + (((l & 15) ^ (f << 1)) << 4);
  glds<0>(rw, dst + w * 1024, v, soff);
  glds<0>(rw, dst + 8192 + w * 1024, v, soff + 32 * 8192);
}
__device__ __forceinline__ u32x8 wfrag(LAS const char* img, int nt, int l) {
  const int g = l >> 4, q = (l & 15) >> 2, pp = l & 3;
  const int rx = 16 * (g >> 1) + 4 * (g & 1) + (q & 1) + 8 * (q >> 1);
  const int f = (rx & 1) | (((rx >> 2) & 3) << 1);
  LAS const char* a = img + rx * 256 + ((nt ^ f) << 5) + pp * 8;
  const u32x2 x0 = trread(a), x1 = trread(a + 2 * 256), x2 = trread(a + 32 * 256), x3 = trread(a + 34 * 256);
  return u32x8{x0.x, x0.y, x1.x, x1.y, x2.x, x2.y, x3.x, x3.y};
}
__device__ __forceinline__ void put_frag(LAS char* wl, int i, u32x8 f) {
  *(LAS u32x4*)(wl + i * 2048) = u32x4{f[0], f[1], f[2], f[3]};
  *(LAS u32x4*)(wl + i * 2048 + 1024) = u32x4{f[4], f[5], f[6], f[7]};
}
__device__ __forceinline__ u32x8 get_frag(LAS const char* wl, int i) {
  const u32x4 a = lds128(wl + i * 2048), b = lds128(wl + i * 2048 + 1024);
  return u32x8{a.x, a.y, a.z, a.w, b.x, b.y, b.z, b.w};
}
__device__ __forceinline__ u32x8 get_frag_tr(LAS const char* wl8, int i) {
  const u32x2 x0 = trread(wl8 + i * 2048), x1 = trread(wl8 + i * 2048 + 512), x2 = trread(wl8 + i * 2048 + 1024), x3 = trread(wl8 + i * 2048 + 1536);
  return u32x8{x0.x, x0.y, x1.x, x1.y, x2.x, x2.y, x3.x, x3.y};
}
template <class SRC> __device__ __forceinline__ void load8(LAS char* lds, int w, int l, SRC src, u32x8* out) {
#pragma unroll
  for (int i = 0; i < 8; ++i) src(i, lds + CL_W + i * 16384);
#pragma unroll
  for (int i = 0; i < 8; i += 2) {
    if (i == 0) WAITV(12); else if (i == 2) WAITV(8); else if (i == 4) WAITV(4); else WAITV(0);
    bar();
    out[i] = wfrag(lds + CL_W + i * 16384, w, l);
    out[i + 1] = wfrag(lds + CL_W + (i + 1) * 16384, w, l);
  }
  WAITL(0);
  bar();
}

struct Chain {
  rsrc_t rw;
  int xa, xb;
  int cp, h, w;
};
__device__ __forceinline__ Chain chain_init(KP p, int h) {
  Chain c;
  c.rw = wsr(p);
  const int l = threadIdx.x & 63;
  const unsigned long long votes = __builtin_amdgcn_ballot_w64(l < 10 && (cword(p, C_PROBE)[l < 10 ? l : 0] & 1));
  c.xa = xoff(p) + (__builtin_popcountll(votes & 31) >= 3 ? 8192 : 0);
  c.xb = xoff(p) + (__builtin_popcountll(votes >> 5) >= 3 ? 8192 : 0);
  c.cp = xcc_id() & 3; c.h = h; c.w = __builtin_amdgcn_readfirstlane(threadIdx.x >> 6);
  return c;
}
__device__ __forceinline__ void stage_m0(KP p, LAS char* lds, int w, int l) {
#pragma unroll
  for (int i = 0; i < 2; ++i)
    *(LAS u32x4*)(lds + CL_M + (2 * w + i) * 1024 + l * 16) = *(const u32x4*)(p->mem0 + (l & 7) * DM + 64 * (2 * w + i) + 16 * (l >> 4) + 8 * ((l >> 3) & 1));
}
__device__ __forceinline__ bool wait_m(const Chain& c, int s, LAS char* lds, int l16, unsigned& spins) {
  const int soff = c.xa + (s & 3) * XM_SLOT + c.cp * XM_COPY + (c.w >> 2) * 16384 + (c.w & 3) * 2048;
  LAS char* dst = lds + CL_M + c.w * 2048 + l16;
  bool ok;
  spins = 0;
#pragma nounroll
  do {
    asm volatile("" ::: "memory");
    const u32x4 a = ld128(c.rw, l16, soff), b = ld128(c.rw, l16 + 1024, soff);
    *(LAS u32x4*)dst = a;
    *(LAS u32x4*)(dst + 1024) = b;
    ok = none(max(umax4(a), umax4(b)) == SENT);
    if (!ok) __builtin_amdgcn_s_sleep(DRETRY);
  } while (!ok && ++spins < SPIN_CAP);
  return ok;
}
__device__ __forceinline__ bool wait_prod(KP p, int rb) {
  unsigned spins = 0;
#pragma nounroll
  while ((unsigned)__builtin_amdgcn_readfirstlane(__hip_atomic_load(cword(p, C_PROD + rb), RLX_AGENT)) < 48u) {
    __builtin_amdgcn_s_sleep(2);
    if (++spins > SPIN_CAP) return false;
  }
  return true;
}

constexpr float SM_SCALE = 0.08838834764831845f, L2E = 1.4426950408889634f;

__device__ __forceinline__ void p_tok(int qt, LAS char* lds, int l, const u32x4 (&Q)[4]) {
  const int r16 = l & 15, g = l >> 4, l16 = l * 16;
  LAS const char* fimg = lds + CL_IMG + l * 8;
  f32x4 s0 = {0.f, 0.f, 0.f, 0.f}, s1 = s0, sm = s0;
#pragma unroll
  for (int ks = 0; ks < 4; ++ks) {
    const u32x2 lo = trread(fimg + ks * 1024), hi = trread(fimg + ks * 1024 + 512);
    s0 = mfma(lds128(lds + CL_KX + ks * 1024 + l16), Q[ks], s0);
    s1 = mfma(lds128(lds + CL_KX + 4096 + ks * 1024 + l16), Q[ks], s1);
    sm = mfma(u32x4{lo.x, lo.y, hi.x, hi.y}, Q[ks], sm);
  }
  const float NEG = -__builtin_inff();
  const int qi = qt * 16 + r16 - 4 * g;
  rnd8(s0, s1); s0 *= SM_SCALE; s1 *= SM_SCALE; rnd8(s0, s1);
#pragma unroll
  for (int i = 0; i < 4; ++i) {
    sm[i] = g < 2 ? NEG : rbf(rbf(sm[i]) * SM_SCALE);
    if (i > qi) s0[i] = NEG;
    if (16 + i > qi) s1[i] = NEG;
  }
  float mx = fmaxf(fmaxf(fmaxf(s0[0], s0[1]), fmaxf(s0[2], s0[3])), fmaxf(fmaxf(s1[0], s1[1]), fmaxf(s1[2], s1[3])));
  mx = xmax1632(fmaxf(mx, fmaxf(fmaxf(sm[0], sm[1]), fmaxf(sm[2], sm[3])))) * L2E;
  float sum = 0.f;
#pragma unroll
  for (int i = 0; i < 4; ++i) {
    s0[i] = __builtin_amdgcn_exp2f(s0[i] * L2E - mx); s1[i] = __builtin_amdgcn_exp2f(s1[i] * L2E - mx); sm[i] = __builtin_amdgcn_exp2f(sm[i] * L2E - mx);
    sum += (s0[i] + s1[i]) + sm[i];
  }
  const float inv = __builtin_amdgcn_rcpf(xsum1632(sum));
  LAS char* px = lds + CL_PX + 1024 + qt * 1536;
  *(LAS u32x4*)(px + l16) = pk8(s0 * inv, s1 * inv);
  *(LAS u32x2*)(px + 1024 + (l ^ 32) * 8) = u32x2{pk(sm[0] * inv, sm[1] * inv), pk(sm[2] * inv, sm[3] * inv)};
}

template <int CLS>
__device__ __forceinline__ bool qk_loop(KP p, const Chain& c, LAS char* lds, const u32x8 (&W)[NREG]) {
  const int w = c.w, h = c.h;
  int l = threadIdx.x & 63;
  STAMP_DECL;
  glds<AUX_SC1>(c.rw, lds + CL_KX + w * 1024, l * 16, OFF_QKV + h * QKV_H + 8192 + w * 1024);
  u32x4 Vx = ld128(c.rw, l * 16, OFF_QKV + h * QKV_H + 16384 + w * 1024);
  for (int s = 0;; ++s) {
    l = opaque(l);
    const bool last = s + 1 == NSEG;
    int l16 = l * 16;
    const int idx = opaque((l & 8) ? 0xEEEE : 0x4444);
    LAS const char* wl = lds + CL_W + w * (NLDS * 2048) + l16;
    u32x8 B[2];
    B[0] = get_frag(wl, 0); B[1] = get_frag(wl, 1);
    f32x4 aq = zero4(), ak = zero4();
    WAITL(0);
    bar();
    u32x4 A[4];
#pragma unroll
    for (int t = 0; t < 4; ++t) A[t] = lds128(lds + CL_M + t * 1024 + l16);
    STAMP(1);
    SCHED();
    const int vs = c.xa + XV_OFF + (s & 1) * XV_SLOT + (h >> 2) * 16384 + (h & 3) * 2048 + w * 256;
    u32x2 vm;
#pragma unroll
    for (int t = 0; t < 16; ++t) {
      aq = smfma(A[t & 3], W[3 * (t >> 1) + 2 * (t & 1)], aq, idx);
      ak = smfma(A[t & 3], (t & 1) ? B[(t >> 1) & 1] : W[3 * (t >> 1) + 1], ak, idx);
      SCHED();
      if (t + 4 < 16) A[t & 3] = lds128(lds + CL_M + (t + 4) * 1024 + l16);
      if ((t & 1) && (t >> 1) + 2 < NLDS) B[(t >> 1) & 1] = get_frag(wl, (t >> 1) + 2);
      SCHED();
    }
    l = opaque(l);
    const int r16 = l & 15, g = l >> 4;
    l16 = l * 16;
    {
      float r[4];
#pragma unroll
      for (int i = 0; i < 4; ++i) { float a = aq[i], b = ak[i]; swap32(a, b); r[i] = a + b; }
      *(LAS u32x2*)(lds + CL_IMG + w * 512 + r16 * 32 + g * 8) = u32x2{pk(r[0], r[1]), pk(r[2], r[3])};
    }
    WAITV(0);
    WAITL(0);
    STAMP(2);
    bar();
    vm = ld64(c.rw, (l & 31) * 8, vs);
    u32x4 Q[4];
    if (CLS == 1) {
#pragma unroll
      for (int ks = 0; ks < 4; ++ks) Q[ks] = ld128(c.rw, l16 + ks * 1024, OFF_QKV + h * QKV_H + s * QKV_S + (w - 1) * 4096);
    }
    SCHED();
    if (CLS == 0) {
      LAS const char* fq = lds + CL_IMG + (l >> 2) * 32 + (l & 1) * 8;
      u32x4 Fq[4], Fk[4], KX[8];
      auto rdq = [&](int ks) { const u32x2 lo = trread(fq + ks * 1024), hi = trread(fq + ks * 1024 + 512); Fq[ks] = u32x4{lo.x, lo.y, hi.x, hi.y}; };
      auto rdk = [&](int ks) { const u32x2 lo = trread(fq + 16 + ks * 1024), hi = trread(fq + 16 + ks * 1024 + 512); Fk[ks] = u32x4{lo.x, lo.y, hi.x, hi.y}; };
      auto rdx = [&](int ks) { KX[ks] = lds128(lds + CL_KX + ks * 1024 + l16); KX[4 + ks] = lds128(lds + CL_KX + (4 + ks) * 1024 + l16); };
      rdq(0); rdx(0); rdk(0); rdq(1); rdx(1); rdk(1);
      SCHED();
      f32x4 s0 = {0.f, 0.f, 0.f, 0.f}, s1 = s0, sm = s0;
#pragma unroll
      for (int ks = 0; ks < 4; ++ks) {
        s0 = mfma(KX[ks], Fq[ks], s0);
        s1 = mfma(KX[4 + ks], Fq[ks], s1);
        sm = mfma(Fk[ks], Fq[ks], sm);
        SCHED();
        if (ks < 2) { rdq(ks + 2); rdx(ks + 2); rdk(ks + 2); }
        SCHED();
      }
      const bool hf = r16 >= 8;
      constexpr float SC = SM_SCALE * L2E;
      float v[5];
#pragma unroll
      for (int i = 0; i < 4; ++i) v[i] = (hf ? s1[i] : s0[i]) * SC;
      float mx = fmaxf(fmaxf(v[0], v[1]), fmaxf(v[2], v[3]));
      { const float a = hf ? sm[1] : sm[0], b = hf ? sm[3] : sm[2]; v[4] = (g >= 2 ? b : a) * SC; }
      mx = fmaxf(mx, v[4]);
      mx = xmax1632(fmaxf(mx, ror8(mx)));
#pragma unroll
      for (int i = 0; i < 5; ++i) v[i] = __builtin_amdgcn_exp2f(v[i] - mx);
      float sum = ((v[0] + v[1]) + (v[2] + v[3])) + v[4];
      sum += ror8(sum);
      const float inv = __builtin_amdgcn_rcpf(xsum1632(sum));
      *(LAS u32x2*)(lds + CL_PX + g * 128 + (r16 & 7) * 16 + (r16 >> 3) * 8) = u32x2{pk(v[0] * inv, v[1] * inv), pk(v[2] * inv, v[3] * inv)};
      *(LAS unsigned short*)(lds + CL_PX + 512 + (r16 & 7) * 16 + (g & 1) * 8 + (g >> 1) * 4 + (r16 >> 3) * 2) = (unsigned short)pk(v[4] * inv, 0.f);
    }
    if (!none(max(vm.x, vm.y) == SENT)) {
      unsigned spins = 0;
      bool ok;
#pragma nounroll
      do {
        __builtin_amdgcn_s_sleep(DRETRY);
        asm volatile("" ::: "memory");
        vm = ld64(c.rw, (l & 31) * 8, vs);
        ok = none(max(vm.x, vm.y) == SENT);
      } while (!ok && ++spins < SPIN_CAP);
      if (!ok) return false;
    }
    if (l >= 32) vm = u32x2{0u, 0u};
    const int oo = opaque((r16 >> 3) * XO_COPY + ((r16 & 7) + 8 * (g >> 1)) * 16 + (g & 1) * 8);
    const int os = c.xb + XO_OFF + (h >> 2) * 16384 + (h & 3) * 2048 + w * 256;
    WAITL(0);
    STAMP(3);
    bar();
    if (!last) {
      const u32x4 pt = lds128(lds + CL_PX + g * 128 + (r16 & 7) * 16);
      const u32x2 pm = lds64(lds + CL_PX + 512 + (r16 & 7) * 16 + (g & 1) * 8);
      const f32x4 o = mfma(Vx, pt, f32x4{0.f, 0.f, 0.f, 0.f}) + mfma16(vm, pm, f32x4{0.f, 0.f, 0.f, 0.f});
      const u32x2 ob = {san(pk(o[0], o[1])), san(pk(o[2], o[3]))};
      st64(ob, c.rw, oo, os + (s & 3) * XO_SLOT);
      st64(ob, c.rw, oo, os + (s & 3) * XO_SLOT + 2 * XO_COPY);
    }
    const unsigned t_p = now32();
    STAMP(4);
    if (CLS == 1) p_tok(w - 1, lds, l, Q);
    if (CLS == 3 && w == 3 && !last && ((s + 1) & 3) == 0 && !wait_prod(p, (s + 1) >> 2)) return false;
    {
      const u32x4 sv = sent4();
      if (l < 16) st128(sv, c.rw, l16, vs);
      st128(sv, c.rw, g * XO_COPY + r16 * 16, os + ((s + 2) & 3) * XO_SLOT);
    }
    WAITL(0);
    bar();
    if (!last) glds<AUX_SC1>(c.rw, lds + CL_KX + w * 1024, l16, OFF_QKV + h * QKV_H + (s + 1) * QKV_S + 8192 + w * 1024);
#pragma unroll
    for (int qt = 0; qt < 2; ++qt) {
      const f32x4 o = mfma(Vx, lds128(lds + CL_PX + 1024 + qt * 1536 + l16), f32x4{0.f, 0.f, 0.f, 0.f}) +
                      mfma16(vm, lds64(lds + CL_PX + 2048 + qt * 1536 + l * 8), f32x4{0.f, 0.f, 0.f, 0.f});
      st64(u32x2{pk(o[0], o[1]), pk(o[2], o[3])}, c.rw, r16 * 8192 + g * 8, OFF_OT + (s * 32 + qt * 16) * 8192 + h * 256 + w * 32);
    }
    if (last) break;
    SCHED();
    Vx = ld128(c.rw, l16, OFF_QKV + h * QKV_H + (s + 1) * QKV_S + 16384 + w * 1024);
    STAMP(5);
    STAMP_FLUSH(h, s);
    wait_until(t_p + DQ);
    STAMP(0);
    unsigned mspins;
    if (!wait_m(c, s + 1, lds, l16, mspins)) return false;
    STAMP(8); STAMPV(9, mspins);
  }
  WAITV(0);
  bar();
  if (w == 0 && l == 0) __hip_atomic_store(cword(p, C_FIN + h), 1u, RLX_AGENT);
  return true;
}

__device__ __forceinline__ bool role_qk(KP p, int h, LAS char* lds) {
  const Chain c = chain_init(p, h);
  const int w = c.w, l = threadIdx.x & 63;
  u32x8 W[NREG];
  {
    const rsrc_t rq = mkrsrc(p->Wq + h * DH, W_BYTES - h * DH * 2), rk = mkrsrc(p->Wk + h * DH, W_BYTES - h * DH * 2);
    load8(lds, w, l, [&](int i, LAS char* dst) { wstage(rk, (2 * i + 1) * 64 * 8192, dst, w, l); }, W);
#pragma unroll
    for (int r = 1; r < 3; ++r)
      load8(lds, w, l, [&](int i, LAS char* dst) { const int n = r * 8 + i; wstage(n % 3 == 1 ? rk : rq, (2 * (n / 3) + (n % 3 == 2)) * 64 * 8192, dst, w, l); }, W + r * 8);
#pragma unroll
    for (int i = 0; i < 8; ++i) put_frag(lds + CL_W + w * (NLDS * 2048) + l * 16, i, W[i]);
#pragma unroll
    for (int n = 0; n < 8; n += 2) {
      wstage(n % 3 == 1 ? rk : rq, (2 * (n / 3) + (n % 3 == 2)) * 64 * 8192, lds, w, l);
      wstage((n + 1) % 3 == 1 ? rk : rq, (2 * ((n + 1) / 3) + ((n + 1) % 3 == 2)) * 64 * 8192, lds + 16384, w, l);
      WAITV(0);
      bar();
      W[n] = wfrag(lds, w, l); W[n + 1] = wfrag(lds + 16384, w, l);
      WAITL(0);
      bar();
    }
  }
  stage_m0(p, lds, w, l);
  if (!wait_prod(p, 0)) return false;
  if (w == 0) return qk_loop<0>(p, c, lds, W);
  if (w < 3) return qk_loop<1>(p, c, lds, W);
  return qk_loop<3>(p, c, lds, W);
}

__device__ __forceinline__ bool role_vo(KP p, int h, LAS char* lds) {
  const Chain c = chain_init(p, h);
  const int w = c.w, rot = h >> 2;
  int l = threadIdx.x & 63;
  u32x8 W[NREG];
  {
    const rsrc_t rv = mkrsrc(p->Wv + h * DH, W_BYTES - h * DH * 2), ro = mkrsrc(p->Wo, W_BYTES);
#pragma unroll
    for (int r = 0; r < 2; ++r) load8(lds, w, l, [&](int i, LAS char* dst) { wstage(rv, (r * 8 + i) * 64 * 8192, dst, w, l); }, W + r * 8);
    const int gq = (l >> 3) & 3, qq = (l >> 1) & 3;
    const int vo = (16 * (gq >> 1) + 4 * (gq & 1) + (qq & 1) + 8 * (qq >> 1) + 2 * (l >> 5)) * 2048 + (l & 1) * 16;
    LAS char* wd = lds + CL_W + w * (NLDS * 2048);
#pragma unroll
    for (int nt = 0; nt < 2; ++nt) {
#pragma unroll
      for (int t = 0; t < 8; ++t) {
        const int so = (512 * w + 64 * ((t + rot) & 7)) * 2048 + h * 64 + nt * 32;
        glds<0>(ro, wd + t * 2048, vo, so);
        glds<0>(ro, wd + t * 2048 + 1024, vo, so + 32 * 2048);
      }
      WAITV(0);
      if (nt == 0) {
#pragma unroll
        for (int t = 0; t < 8; ++t) W[16 + t] = get_frag_tr(wd + l * 8, t);
        WAITL(0);
      }
    }
  }
  stage_m0(p, lds, w, l);
  STAMP_DECL;
  for (int s = 0;; ++s) {
    l = opaque(l);
    int l16 = l * 16;
    const int idx = opaque((l & 8) ? 0xEEEE : 0x4444);
    f32x4 av = zero4();
    WAITL(0);
    bar();
    u32x4 A[8];
#pragma unroll
    for (int t = 0; t < 8; ++t) A[t] = lds128(lds + CL_M + t * 1024 + l16);
    const unsigned t_m = now32();
    STAMP(1);
    SCHED();
#pragma unroll
    for (int t = 0; t < 16; ++t) {
      av = smfma(A[t & 7], W[t], av, idx);
      SCHED();
      if (t < 8) A[t] = lds128(lds + CL_M + (t + 8) * 1024 + l16);
      SCHED();
    }
    {
      float r[4];
#pragma unroll
      for (int i = 0; i < 4; ++i) { float a = av[i], b = av[i]; swap32(a, b); r[i] = a + b; }
      if (l < 32) st64(u32x2{san(pk(r[0], r[1])), san(pk(r[2], r[3]))}, c.rw, l * 8, c.xa + XV_OFF + (s & 1) * XV_SLOT + (h >> 2) * 16384 + (h & 3) * 2048 + w * 256);
    }
    STAMP(4);
    if (s + 1 == NSEG) break;
    if (w < 2) st128(sent4(), c.rw, (l >> 4) * XM_COPY + (l & 15) * 16, c.xa + ((s + 3) & 3) * XM_SLOT + (h >> 4) * 16384 + ((2 * h + w) & 31) * 256);
    STAMP_FLUSH(32 + h, s);
    l = opaque(l);
    l16 = l * 16;
    LAS const char* wl8 = lds + CL_W + w * (NLDS * 2048) + l * 8;
    const int os = c.xb + XO_OFF + (s & 3) * XO_SLOT + c.cp * XO_COPY + w * 16384;
    f32x4 a0, a1;
    unsigned spins = 0;
    bool ok;
    u32x8 B[2];
    B[0] = get_frag_tr(wl8, 0); B[1] = get_frag_tr(wl8, 1);
    wait_until(t_m + DB);
    STAMP(5);
#pragma nounroll
    do {
      asm volatile("" ::: "memory");
#pragma unroll
      for (int t = 0; t < 8; ++t) A[t] = ld128(c.rw, l16, os + ((t + rot) & 7) * 1024);
      a0 = zero4(); a1 = zero4();
      SCHED();
      unsigned mx = 0;
#pragma unroll
      for (int t = 0; t < 8; ++t) {
        a0 = smfma(A[t], W[16 + t], a0, idx);
        a1 = smfma(A[t], B[t & 1], a1, idx);
        mx = umax3(umax3(mx, A[t].x, A[t].y), A[t].z, A[t].w);
        SCHED();
        B[t & 1] = get_frag_tr(wl8, (t + 2) & 7);
        SCHED();
      }
      ok = none(mx == SENT);
      if (!ok) __builtin_amdgcn_s_sleep(DRETRY);
    } while (!ok && ++spins < SPIN_CAP);
    if (!ok) return false;
    STAMP(6); STAMPV(11, spins);
    l16 = opaque(l16);
    l = l16 >> 4;
    LAS char* redw = lds + CL_KX + w * 1024 + ((l >> 4) & 1) * 512 + (l >> 5) * 64 + (l & 15) * 4;
#pragma unroll
    for (int i = 0; i < 4; ++i) { float a = a0[i], b = a1[i]; swap32(a, b); *(LAS float*)(redw + i * 128) = a + b; }
    WAITL(0);
    bar();
    unsigned t_r;
    if (w < 4) {
      LAS const char* rr = lds + CL_KX + l16;
      f32x4 t = (*(LAS const f32x4*)rr + *(LAS const f32x4*)(rr + 1024)) + (*(LAS const f32x4*)(rr + 2048) + *(LAS const f32x4*)(rr + 3072));
      t += (*(LAS const f32x4*)(rr + 4096) + *(LAS const f32x4*)(rr + 5120)) + (*(LAS const f32x4*)(rr + 6144) + *(LAS const f32x4*)(rr + 7168));
      const u32x2 o = {san(pk(t[0], t[1])), san(pk(t[2], t[3]))};
      const int mo = ((l >> 2) & 1) * 256 + ((l >> 3) + 8 * ((l >> 1) & 1)) * 16 + (l & 1) * 8;
      st64(o, c.rw, mo, c.xa + ((s + 1) & 3) * XM_SLOT + w * XM_COPY + (h >> 4) * 16384 + ((2 * h) & 31) * 256);
      t_r = now32() - 200;
    } else {
      t_r = now32();
      if (w == 7 && s > 0 && h == (s & 31) && l == 0) __hip_atomic_store(cword(p, C_DONE + s - 1), 1u, RLX_AGENT);
    }
    STAMP(7);
    wait_until(t_r + DA);
    STAMP(0);
    unsigned mspins;
    if (!wait_m(c, s + 1, lds, l16, mspins)) return false;
    STAMP(8); STAMPV(9, mspins);
  }
  WAITV(0);
  bar();
  return true;
}

enum { T_QKV = 0, T_WO = 1, T_LN2 = 2, T_W1 = 3, T_W2 = 4, T_LN3 = 5, T_W3 = 6, T_W4 = 7, T_END = 15 };
constexpr int NTASK = 2184;
static __device__ const unsigned short QSTART[9] = {0,220,440,660,880,1220,1560,1872,2184};
static __device__ const unsigned TASKS[NTASK] = {
16,28,40,4,272,284,296,260,528,540,552,516,784,796,808,772,1040,1052,1064,1028,
1296,1308,1320,1284,1552,1564,1576,1540,1808,1820,1832,1796,2064,2076,2088,2052,2320,2332,2344,2308,
2576,2588,2600,2564,2832,2844,2856,2820,3088,3100,3112,3076,3344,3356,3368,3332,3600,3612,3624,3588,
3856,3868,3880,3844,65536,65792,66048,66304,66560,131072,133120,135168,196608,196864,197120,66816,137216,262144,262400,197376,
67072,139264,328704,330752,262656,197632,393220,393476,67328,332800,141312,262912,393732,197888,67584,458752,459008,334848,143360,263168,
393988,198144,67840,459264,336896,145408,263424,394244,198400,68096,459520,338944,147456,263680,394500,198656,68352,459776,340992,149504,
104448,104449,263936,394756,198912,104704,104705,460032,343040,151552,104960,104961,264192,395012,199168,155648,105216,105217,460288,235520,
345088,153600,105472,105473,264448,235776,395268,199424,157696,105728,105729,460544,236032,347136,105984,105985,264704,264960,301056,236288,
106240,106241,395524,301312,159744,460800,236544,301568,349184,351232,353280,432136,432137,236800,395780,396036,432392,432393,301824,161792,
461056,237056,497664,497665,432648,432649,302080,237312,497920,497921,355328,432904,432905,302336,498176,498177,461312,461568,302592,302848,
433160,433161,498432,498433,357376,359424,433416,433417,498688,498689,433672,433673,433928,433929,498944,498945,499200,499201,499456,499457,
17,29,41,5,273,285,297,261,529,541,553,517,785,797,809,773,1041,1053,1065,1029,
1297,1309,1321,1285,1553,1565,1577,1541,1809,1821,1833,1797,2065,2077,2089,2053,2321,2333,2345,2309,
2577,2589,2601,2565,2833,2845,2857,2821,3089,3101,3113,3077,3345,3357,3369,3333,3601,3613,3625,3589,
3857,3869,3881,3845,65537,65793,66049,66305,66561,131328,133376,135424,196609,196865,197121,66817,137472,262145,262401,197377,
67073,139520,328960,331008,262657,197633,393221,393477,67329,333056,141568,262913,393733,197889,67585,458753,459009,335104,143616,263169,
393989,198145,67841,459265,337152,145664,263425,394245,198401,68097,459521,339200,147712,263681,394501,198657,68353,459777,341248,149760,
104450,104451,263937,394757,198913,104706,104707,460033,343296,151808,104962,104963,264193,395013,199169,155904,105218,105219,460289,235521,
345344,153856,105474,105475,264449,235777,395269,199425,157952,105730,105731,460545,236033,347392,105986,105987,264705,264961,301057,236289,
106242,106243,395525,301313,160000,460801,236545,301569,349440,351488,353536,432138,432139,236801,395781,396037,432394,432395,301825,162048,
461057,237057,497666,497667,432650,432651,302081,237313,497922,497923,355584,432906,432907,302337,498178,498179,461313,461569,302593,302849,
433162,433163,498434,498435,357632,359680,433418,433419,498690,498691,433674,433675,433930,433931,498946,498947,499202,499203,499458,499459,
18,30,42,6,274,286,298,262,530,542,554,518,786,798,810,774,1042,1054,1066,1030,
1298,1310,1322,1286,1554,1566,1578,1542,1810,1822,1834,1798,2066,2078,2090,2054,2322,2334,2346,2310,
2578,2590,2602,2566,2834,2846,2858,2822,3090,3102,3114,3078,3346,3358,3370,3334,3602,3614,3626,3590,
3858,3870,3882,3846,65538,65794,66050,66306,66562,131584,133632,135680,196610,196866,197122,66818,137728,262146,262402,197378,
67074,139776,329216,331264,262658,197634,393222,393478,67330,333312,141824,262914,393734,197890,67586,458754,459010,335360,143872,263170,
393990,198146,67842,459266,337408,145920,263426,394246,198402,68098,459522,339456,147968,263682,394502,198658,68354,459778,341504,150016,
104452,104453,263938,394758,198914,104708,104709,460034,343552,152064,104964,104965,264194,395014,199170,156160,105220,105221,460290,235522,
345600,154112,105476,105477,264450,235778,395270,199426,158208,105732,105733,460546,236034,347648,105988,105989,264706,264962,301058,236290,
106244,106245,395526,301314,160256,460802,236546,301570,349696,351744,353792,432140,432141,236802,395782,396038,432396,432397,301826,162304,
461058,237058,497668,497669,432652,432653,302082,237314,497924,497925,355840,432908,432909,302338,498180,498181,461314,461570,302594,302850,
433164,433165,498436,498437,357888,359936,433420,433421,498692,498693,433676,433677,433932,433933,498948,498949,499204,499205,499460,499461,
19,31,43,7,275,287,299,263,531,543,555,519,787,799,811,775,1043,1055,1067,1031,
1299,1311,1323,1287,1555,1567,1579,1543,1811,1823,1835,1799,2067,2079,2091,2055,2323,2335,2347,2311,
2579,2591,2603,2567,2835,2847,2859,2823,3091,3103,3115,3079,3347,3359,3371,3335,3603,3615,3627,3591,
3859,3871,3883,3847,65539,65795,66051,66307,66563,131840,133888,135936,196611,196867,197123,66819,137984,262147,262403,197379,
67075,140032,329472,331520,262659,197635,393223,393479,67331,333568,142080,262915,393735,197891,67587,458755,459011,335616,144128,263171,
393991,198147,67843,459267,337664,146176,263427,394247,198403,68099,459523,339712,148224,263683,394503,198659,68355,459779,341760,150272,
104454,104455,263939,394759,198915,104710,104711,460035,343808,152320,104966,104967,264195,395015,199171,156416,105222,105223,460291,235523,
345856,154368,105478,105479,264451,235779,395271,199427,158464,105734,105735,460547,236035,347904,105990,105991,264707,264963,301059,236291,
106246,106247,395527,301315,160512,460803,236547,301571,349952,352000,354048,432142,432143,236803,395783,396039,432398,432399,301827,162560,
461059,237059,497670,497671,432654,432655,302083,237315,497926,497927,356096,432910,432911,302339,498182,498183,461315,461571,302595,302851,
433166,433167,498438,498439,358144,360192,433422,433423,498694,498695,433678,433679,433934,433935,498950,498951,499206,499207,499462,499463,
20,24,32,36,44,0,8,12,276,280,288,292,300,256,264,268,532,536,544,548,
556,512,520,524,788,792,800,804,812,768,776,780,1044,1048,1056,1060,1068,1024,1032,1036,
1300,1304,1312,1316,1324,1280,1288,1292,1556,1560,1568,1572,1580,1536,1544,1548,1812,1816,1824,1828,
1836,1792,1800,1804,2068,2072,2080,2084,2092,2048,2056,2060,2324,2328,2336,2340,2348,2304,2312,2316,
2580,2584,2592,2596,2604,2560,2568,2572,2836,2840,2848,2852,2860,2816,2824,2828,3092,3096,3104,3108,
3116,3072,3080,3084,3348,3352,3360,3364,3372,3328,3336,3340,3604,3608,3616,3620,3628,3584,3592,3596,
3860,3864,3872,3876,3884,3840,3848,3852,65540,65796,66052,66308,66564,132096,134144,136192,196612,196868,197124,66820,
138240,262148,262404,197380,67076,140288,327680,329728,262660,197636,393216,393224,393228,393472,393480,393484,67332,331776,142336,262916,
393728,393736,393740,197892,67588,458756,459012,333824,144384,263172,393984,393992,393996,198148,67844,459268,335872,146432,263428,394240,
394248,394252,198404,68100,459524,337920,148480,263684,394496,394504,394508,198660,68356,459780,339968,150528,104456,104457,263940,394752,
394760,394764,198916,104712,104713,460036,342016,152576,104968,104969,264196,395008,395016,395020,199172,105224,105225,460292,235524,156672,
344064,154624,105480,105481,264452,235780,395264,395272,395276,199428,105736,105737,460548,236036,158720,346112,105992,105993,264708,264964,
301060,236292,106248,106249,395520,395528,395532,301316,460804,236548,352256,301572,160768,348160,350208,432128,432129,432144,432145,432152,
432153,236804,395776,395784,395788,396032,396040,396044,432384,432385,432400,432401,432408,432409,301828,354304,461060,162816,237060,497672,
497673,432640,432641,432656,432657,432664,432665,302084,237316,497928,497929,432896,432897,432912,432913,432920,432921,302340,498184,498185,
356352,461316,461572,302596,302852,433152,433153,433168,433169,433176,433177,498440,498441,358400,433408,433409,433424,433425,433432,433433,
498696,498697,433664,433665,433680,433681,433688,433689,433920,433921,433936,433937,433944,433945,498952,498953,499208,499209,499464,499465,
21,25,33,37,45,1,9,13,277,281,289,293,301,257,265,269,533,537,545,549,
557,513,521,525,789,793,801,805,813,769,777,781,1045,1049,1057,1061,1069,1025,1033,1037,
1301,1305,1313,1317,1325,1281,1289,1293,1557,1561,1569,1573,1581,1537,1545,1549,1813,1817,1825,1829,
1837,1793,1801,1805,2069,2073,2081,2085,2093,2049,2057,2061,2325,2329,2337,2341,2349,2305,2313,2317,
2581,2585,2593,2597,2605,2561,2569,2573,2837,2841,2849,2853,2861,2817,2825,2829,3093,3097,3105,3109,
3117,3073,3081,3085,3349,3353,3361,3365,3373,3329,3337,3341,3605,3609,3617,3621,3629,3585,3593,3597,
3861,3865,3873,3877,3885,3841,3849,3853,65541,65797,66053,66309,66565,132352,134400,136448,196613,196869,197125,66821,
138496,262149,262405,197381,67077,140544,327936,329984,262661,197637,393217,393225,393229,393473,393481,393485,67333,332032,142592,262917,
393729,393737,393741,197893,67589,458757,459013,334080,144640,263173,393985,393993,393997,198149,67845,459269,336128,146688,263429,394241,
394249,394253,198405,68101,459525,338176,148736,263685,394497,394505,394509,198661,68357,459781,340224,150784,104458,104459,263941,394753,
394761,394765,198917,104714,104715,460037,342272,152832,104970,104971,264197,395009,395017,395021,199173,105226,105227,460293,235525,156928,
344320,154880,105482,105483,264453,235781,395265,395273,395277,199429,105738,105739,460549,236037,158976,346368,105994,105995,264709,264965,
301061,236293,106250,106251,395521,395529,395533,301317,460805,236549,352512,301573,161024,348416,350464,432130,432131,432146,432147,432154,
432155,236805,395777,395785,395789,396033,396041,396045,432386,432387,432402,432403,432410,432411,301829,354560,461061,163072,237061,497674,
497675,432642,432643,432658,432659,432666,432667,302085,237317,497930,497931,432898,432899,432914,432915,432922,432923,302341,498186,498187,
356608,461317,461573,302597,302853,433154,433155,433170,433171,433178,433179,498442,498443,358656,433410,433411,433426,433427,433434,433435,
498698,498699,433666,433667,433682,433683,433690,433691,433922,433923,433938,433939,433946,433947,498954,498955,499210,499211,499466,499467,
22,26,34,38,46,2,10,14,278,282,290,294,302,258,266,270,534,538,546,550,
558,514,522,526,790,794,802,806,814,770,778,782,1046,1050,1058,1062,1070,1026,1034,1038,
1302,1306,1314,1318,1326,1282,1290,1294,1558,1562,1570,1574,1582,1538,1546,1550,1814,1818,1826,1830,
1838,1794,1802,1806,2070,2074,2082,2086,2094,2050,2058,2062,2326,2330,2338,2342,2350,2306,2314,2318,
2582,2586,2594,2598,2606,2562,2570,2574,2838,2842,2850,2854,2862,2818,2826,2830,3094,3098,3106,3110,
3118,3074,3082,3086,3350,3354,3362,3366,3374,3330,3338,3342,3606,3610,3618,3622,3630,3586,3594,3598,
3862,3866,3874,3878,3886,3842,3850,3854,65542,65798,66054,66310,66566,132608,134656,136704,196614,196870,197126,66822,
138752,262150,262406,197382,67078,140800,328192,330240,262662,197638,393218,393226,393474,393482,67334,332288,142848,262918,393730,393738,
197894,67590,458758,459014,334336,144896,263174,393986,393994,198150,67846,459270,336384,146944,263430,394242,394250,198406,68102,459526,
338432,148992,263686,394498,394506,198662,68358,459782,340480,151040,104460,104461,263942,394754,394762,198918,104716,104717,460038,342528,
153088,104972,104973,264198,395010,395018,199174,105228,105229,460294,235526,157184,344576,155136,105484,105485,264454,235782,395266,395274,
199430,105740,105741,460550,236038,159232,346624,105996,105997,264710,264966,301062,236294,106252,106253,395522,395530,301318,460806,236550,
352768,301574,161280,348672,350720,432132,432133,432148,432149,236806,395778,395786,396034,396042,432388,432389,432404,432405,301830,354816,
461062,163328,237062,497676,497677,432644,432645,432660,432661,302086,237318,497932,497933,432900,432901,432916,432917,302342,498188,498189,
356864,461318,461574,302598,302854,433156,433157,433172,433173,498444,498445,358912,433412,433413,433428,433429,498700,498701,433668,433669,
433684,433685,433924,433925,433940,433941,498956,498957,499212,499213,499468,499469,23,27,35,39,47,3,11,15,
279,283,291,295,303,259,267,271,535,539,547,551,559,515,523,527,791,795,803,807,
815,771,779,783,1047,1051,1059,1063,1071,1027,1035,1039,1303,1307,1315,1319,1327,1283,1291,1295,
1559,1563,1571,1575,1583,1539,1547,1551,1815,1819,1827,1831,1839,1795,1803,1807,2071,2075,2083,2087,
2095,2051,2059,2063,2327,2331,2339,2343,2351,2307,2315,2319,2583,2587,2595,2599,2607,2563,2571,2575,
2839,2843,2851,2855,2863,2819,2827,2831,3095,3099,3107,3111,3119,3075,3083,3087,3351,3355,3363,3367,
3375,3331,3339,3343,3607,3611,3619,3623,3631,3587,3595,3599,3863,3867,3875,3879,3887,3843,3851,3855,
65543,65799,66055,66311,66567,132864,134912,136960,196615,196871,197127,66823,139008,262151,262407,197383,67079,141056,328448,330496,
262663,197639,393219,393227,393475,393483,67335,332544,143104,262919,393731,393739,197895,67591,458759,459015,334592,145152,263175,393987,
393995,198151,67847,459271,336640,147200,263431,394243,394251,198407,68103,459527,338688,149248,263687,394499,394507,198663,68359,459783,
340736,151296,104462,104463,263943,394755,394763,198919,104718,104719,460039,342784,153344,104974,104975,264199,395011,395019,199175,105230,
105231,460295,235527,157440,344832,155392,105486,105487,264455,235783,395267,395275,199431,105742,105743,460551,236039,159488,346880,105998,
105999,264711,264967,301063,236295,106254,106255,395523,395531,301319,460807,236551,353024,301575,161536,348928,350976,432134,432135,432150,
432151,236807,395779,395787,396035,396043,432390,432391,432406,432407,301831,355072,461063,163584,237063,497678,497679,432646,432647,432662,
432663,302087,237319,497934,497935,432902,432903,432918,432919,302343,498190,498191,357120,461319,461575,302599,302855,433158,433159,433174,
433175,498446,498447,359168,433414,433415,433430,433431,498702,498703,433670,433671,433686,433687,433926,433927,433942,433943,498958,498959,
499214,499215,499470,499471};

struct TypeInfo { int warg, wbytes, abase, lda, ldb, nk, shift, kind, r1base, obase, ldo, gate, sig, inc, dep, need, lgn, lgn_small; };
static __device__ const TypeInfo TYPES[8] = {
    {16, W_BYTES, OFF_H, 2048, 8192, 16, 0, 0, 0, 0, 2048, 0, C_PROD, 1, 0, 0, 5, 5},
    {40, W_BYTES, OFF_OT, 8192, 2048, 64, 0, 2, 0, OFF_X1, 2048, 2 * DM, C_X1, 2, 0, 1, 4, 3},
    {0, 0, 0, 0, 0, 0, 0, 0, 0, 0, 0, 0, C_N2, 1, C_X1, 16, 0, 0},
    {48, DM * DM * 2, OFF_N2, 2048, 2048, 16, 0, 0, 0, OFF_G + 4096, 2048, 0, C_G, 1, C_N2, 4, 4, 4},
    {56, DM * DM * 2, OFF_G + 4096, 2048, 2048, 16, 2048, 1, OFF_H2, OFF_Y1, 2048, 0, C_Y1, 1, C_G, 8, 4, 4},
    {0, 0, 0, 0, 0, 0, 0, 0, 0, 0, 0, 0, C_N3, 1, C_Y1, 8, 0, 0},
    {64, DM * FF * 2, OFF_N3, 2048, FF * 2, 16, 0, 0, 0, OFF_G3, FF * 2, 0, C_G3, 2, C_N3, 4, 5, 4},
    {72, DM * FF * 2, OFF_G3, FF * 2, 2048, FF / 64, 0, 3, OFF_Y1, 0, 2048, 5 * DM, 0, 1, C_G3, 28, 4, 3},
};
static_assert(__builtin_offsetof(Params, Wq) == 16 && __builtin_offsetof(Params, Wo) == 40 && __builtin_offsetof(Params, w4) == 72, "argument offsets");

#define WSTAMPS 0

__device__ __forceinline__ void worker(LAS char* lds) {
  int qn = 0;
#pragma nounroll
  for (;;) {
    const KP p = kparams();
    if (threadIdx.x < 64) {
      const int l = threadIdx.x;
      unsigned code = T_END << 16, t = 0;
      for (; qn < 8; ++qn) {
        const int q = (blockIdx.x + qn) & 7;
        if (l == 0) t = __hip_atomic_fetch_add(cword(p, C_TICKET + q), 1u, RLX_AGENT);
        t = __builtin_amdgcn_readfirstlane(t) + QSTART[q];
        if (t < QSTART[q + 1]) { code = TASKS[t]; break; }
      }
      const int ty = code >> 16, sm = (code >> 15) & 1, a = (code >> 8) & 127;
      const TypeInfo& ti = TYPES[ty & 7];
      const bool isln = ty == T_LN2 || ty == T_LN3;
      const int h0 = isln ? a >> 2 : sm ? a : 2 * a, h1 = isln || sm ? h0 : h0 + 1;
      const int halo = ty == T_W2;
      int lo = ti.dep + max(h0 - halo, 0), hi = ti.dep + min(h1 + halo, 31);
      const unsigned need = ti.need;
      lo = ty == T_WO ? (h1 < 31 ? C_DONE + 2 * h1 + 1 : C_FIN) : lo;
      hi = ty == T_WO ? (h1 < 31 ? lo : C_FIN + 31) : hi;
      hi = ty == T_QKV || ty == T_END ? -1 : hi;
      const unsigned* wp = cword(p, min(lo + l, max(hi, 0)));
      if (hi >= 0) {
        unsigned spins = 0;
#pragma nounroll
        while (!none(__hip_atomic_load(wp, RLX_AGENT) < need)) {
          __builtin_amdgcn_s_sleep(WSLEEP);
          if (++spins > SPIN_CAP || ((spins & 63u) == 0u && __builtin_amdgcn_readfirstlane(__hip_atomic_load(cword(p, C_TMO), RLX_AGENT)) != 0)) {
            __hip_atomic_store(cword(p, C_TMO), 1u, RLX_AGENT);
            code = T_END << 16;
            break;
          }
        }
      }
      if (l == 0) *(LAS unsigned*)lds = code;
    }
    WAITL(0);
    bar();
    const int code = __builtin_amdgcn_readfirstlane(*(LAS volatile unsigned*)lds);
    WAITL(0);
    bar();
    const int ty = code >> 16, sm = (code >> 15) & 1, a = (code >> 8) & 127, b = code & 255;
    if (ty == T_END) break;
    const TypeInfo& ti = TYPES[ty];
    const bool isln = ty == T_LN2 || ty == T_LN3;
    const int sig = ti.sig + (isln ? a >> 2 : sm || ty == T_QKV ? a : 2 * a), nsig = ty == T_W4 ? 0 : sm || isln || ty == T_QKV ? 1 : 2;
    const unsigned inc = sm ? 1 : ti.inc;
    if (isln) {
      ln_rows<2>(p, ty == T_LN2 ? 1 : 2, a * 16);
    } else {
      const int lgn = sm ? ti.lgn_small : ti.lgn;
      const int row0 = a * (sm ? 64 : 128), colb = (b & (ty == T_QKV ? 15 : 255)) << (lgn + 4);
      typedef const __attribute__((address_space(4))) char* KC;
      const bf16_t* wgt = *(const bf16_t* const __attribute__((address_space(4)))*)((KC)p + ti.warg + (ty == T_QKV ? (b >> 4) * 8 : 0));
      GemmArgs ga;
      ga.ra = wsr(p); ga.rb = mkrsrc(wgt, ti.wbytes);
      ga.a_off = sgpr(ti.abase + row0 * ti.lda); ga.b_off = sgpr(colb); ga.lda = sgpr(ti.lda); ga.ldb = sgpr(ti.ldb); ga.shift = sgpr(ti.shift); ga.nk = ti.nk;
      EpiArgs e;
      e.r1 = mkrsrc(ty == T_WO ? (const void*)p->x : (const void*)p->ws, ty == T_WO ? ACT_BYTES : WS_BYTES);
      e.ro = mkrsrc(ty == T_W4 ? (void*)p->out : (void*)p->ws, ty == T_W4 ? ACT_BYTES : WS_BYTES);
      e.kind = ti.kind; e.lgn = lgn; e.nit = (sm ? 64 : 128) >> (9 - lgn); e.ldo = ti.ldo; e.gate = ti.gate + (colb >> 1);
      e.r1_off = ti.r1base + row0 * 2048 + colb; e.r2_off = OFF_X1 + row0 * 2048 + colb; e.o_off = ti.obase + row0 * ti.ldo + colb;
      if (lgn == 5) gemm_tile<128, 256, 2, 4, 3>(lds, ga);
      else if (!sm) gemm_tile<128, 128, 2, 4, 4>(lds, ga);
      else if (lgn == 4) gemm_tile<64, 128, 2, 4, 6>(lds, ga);
      else gemm_tile<64, 64, 4, 2, 8>(lds, ga);
      if (ty == T_QKV) epi_qkv(p, lds, a, b); else epi_rows(p, lds, e);
    }
    WAITV(0);
    WAITL(0);
    bar();
    if ((int)threadIdx.x < nsig) __hip_atomic_fetch_add(cword(p, sig + threadIdx.x), inc, RLX_AGENT);
  }
}

__global__ __launch_bounds__(512, 2) void k_mega(Params) {
  LAS char* lds = (LAS char*)smem;
  const int b = blockIdx.x;
  if ((b & 7) < 4 && (b >> 3) < 16) {
    const int h = (b >> 4) * 4 + (b & 3);
    const bool ok = ((b >> 3) & 1) == 0 ? role_qk(kparams(), h, lds) : role_vo(kparams(), h, lds);
    if (!ok) { __hip_atomic_store(cword(kparams(), C_TMO), 1u, RLX_AGENT); return; }
  }
  worker(lds);
}

extern "C" void kernel_launch(void* const* d_in, const int* in_sizes, int n_in, void* d_out, int out_size, void* d_ws, size_t ws_size,
                              hipStream_t stream) {
  Params p;
  p.x = (const bf16_t*)d_in[0]; p.mem0 = (const bf16_t*)d_in[1]; p.Wq = (const bf16_t*)d_in[2]; p.Wk = (const bf16_t*)d_in[3];
  p.Wv = (const bf16_t*)d_in[4]; p.Wo = (const bf16_t*)d_in[5]; p.w1 = (const bf16_t*)d_in[8]; p.w2 = (const bf16_t*)d_in[9];
  p.w3 = (const bf16_t*)d_in[10]; p.w4 = (const bf16_t*)d_in[11]; p.c = (const bf16_t*)d_in[12]; p.wmod = (const bf16_t*)d_in[13];
  p.out = (bf16_t*)d_out; p.ws = (unsigned char*)d_ws;
  (void)hipFuncSetAttribute((const void*)k_mega, hipFuncAttributeMaxDynamicSharedMemorySize, LDS_BYTES);
  k_pre<<<256, 512, 0, stream>>>(p);
  k_ln1<<<256, 512, 0, stream>>>(p);
  k_mega<<<256, 512, LDS_BYTES, stream>>>(p);
}
```

```cpp
#include <hip/hip_runtime.h>
#include <stdint.h>

#define LAS __attribute__((address_space(3)))
typedef unsigned short bf16_t;
typedef __bf16 bfx8 __attribute__((ext_vector_type(8)));
typedef __bf16 bfx16 __attribute__((ext_vector_type(16)));
typedef __bf16 bfx2 __attribute__((ext_vector_type(2)));
typedef short s16x4 __attribute__((ext_vector_type(4)));
typedef float f32x4 __attribute__((ext_vector_type(4)));
typedef float f32x2 __attribute__((ext_vector_type(2)));
typedef unsigned u32x2 __attribute__((ext_vector_type(2)));
typedef unsigned u32x4 __attribute__((ext_vector_type(4)));
typedef unsigned u32x8 __attribute__((ext_vector_type(8)));
typedef __amdgpu_buffer_rsrc_t rsrc_t;

constexpr int SEQ = 2048, DM = 1024, NH = 32, DH = 128, NSEG = 64, FF = 3584;
constexpr float LN_EPS = 1e-5f;
constexpr unsigned SENT = 0xFFFFFFFFu;
constexpr int AUX_SC1 = 16;

constexpr int ACT_BYTES = SEQ * DM * 2, W_BYTES = DM * NH * DH * 2, WS_BYTES = 256 << 20;
constexpr int OFF_CTRL = 0;
constexpr int OFF_MOD = 65536;
constexpr int OFF_X = 131072, PG = 2097152;
constexpr int XM_SLOT = 131072, XM_COPY = 32768;
constexpr int XV_OFF = 524288, XV_SLOT = 131072;
constexpr int XT_OFF = 786432;
constexpr int XO_OFF = PG, XO_SLOT = 524288, XO_COPY = 131072;
constexpr int OFF_QKV = OFF_X + 3 * PG;
constexpr int QKV_S = 24576, QKV_H = 64 * QKV_S + 4096;
constexpr int OFF_OT = OFF_QKV + 32 * QKV_H;
constexpr int OFF_H = OFF_OT + SEQ * 4096 * 2;
constexpr int OFF_X1 = OFF_H + ACT_BYTES;
constexpr int OFF_H2 = OFF_X1 + ACT_BYTES;
constexpr int OFF_N2 = OFF_H2 + ACT_BYTES;
constexpr int OFF_G = OFF_N2 + ACT_BYTES;
constexpr int OFF_Y1 = OFF_G + 132 * 32768;
constexpr int OFF_N3 = OFF_Y1 + 2080 * 2048;
constexpr int OFF_G3 = OFF_N3 + 2080 * 2048;
constexpr int PSTR = 130 * 65536;
constexpr int OFF_P1 = OFF_G3 + 130 * FF * 32;
constexpr int OFF_P2 = OFF_P1 + 4 * PSTR;
constexpr int OFF_P4 = OFF_P2 + PSTR;
constexpr int OFF_FLAG = OFF_P4 + 4 * PSTR;
constexpr int OFF_MDBG = OFF_FLAG + 262144;
constexpr int WS_END = OFF_MDBG + 65 * 16384;
static_assert(WS_END + (8 << 20) <= WS_BYTES, "workspace");

struct Params {
  const bf16_t *x, *mem0, *Wq, *Wk, *Wv, *Wo, *w1, *w2, *w3, *w4, *c, *wmod;
  bf16_t* out;
  unsigned char* ws;
};
typedef const __attribute__((address_space(4))) Params* KP;
__device__ __forceinline__ KP kparams() { KP k = (KP)__builtin_amdgcn_kernarg_segment_ptr(); asm volatile("" : "+s"(k)); return k; }

__device__ __forceinline__ float bf2f(unsigned short b) { return __builtin_bit_cast(float, (unsigned)b << 16); }
__device__ __forceinline__ float bflo(unsigned u) { return __builtin_bit_cast(float, u << 16); }
__device__ __forceinline__ float bfhi(unsigned u) { return __builtin_bit_cast(float, u & 0xFFFF0000u); }
__device__ __forceinline__ unsigned pk(float lo, float hi) { bfx2 v; v[0] = (__bf16)lo; v[1] = (__bf16)hi; return __builtin_bit_cast(unsigned, v); }
__device__ __forceinline__ float rbf(float x) { return bflo(pk(x, 0.f)); }
__device__ __forceinline__ unsigned san(unsigned u) { return u < 0xFFFFFFFEu ? u : 0xFFFFFFFEu; }
__device__ __forceinline__ f32x4 mfma(u32x4 a, u32x4 b, f32x4 c) {
  return __builtin_amdgcn_mfma_f32_16x16x32_bf16(__builtin_bit_cast(bfx8, a), __builtin_bit_cast(bfx8, b), c, 0, 0, 0);
}
__device__ __forceinline__ f32x4 mfma16(u32x2 a, u32x2 b, f32x4 c) {
  return __builtin_amdgcn_mfma_f32_16x16x16bf16_1k(__builtin_bit_cast(s16x4, a), __builtin_bit_cast(s16x4, b), c, 0, 0, 0);
}
__device__ __forceinline__ f32x4 smfma(u32x4 a, u32x8 b, f32x4 c, int idx) {
  return __builtin_amdgcn_smfmac_f32_16x16x64_bf16(__builtin_bit_cast(bfx8, a), __builtin_bit_cast(bfx16, b), c, idx, 0, 0);
}
__device__ __forceinline__ u32x2 trread(LAS const char* p) { return __builtin_bit_cast(u32x2, __builtin_amdgcn_ds_read_tr16_b64_v4i16((LAS s16x4*)p)); }
__device__ __forceinline__ u32x4 lds128(LAS const char* p) { return *(LAS const u32x4*)p; }
__device__ __forceinline__ u32x2 lds64(LAS const char* p) { return *(LAS const u32x2*)p; }
__device__ __forceinline__ rsrc_t mkrsrc(const void* p, int bytes) { return __builtin_amdgcn_make_buffer_rsrc((void*)p, 0, bytes, 0x00020000); }
__device__ __forceinline__ rsrc_t wsr(KP p) { return mkrsrc(p->ws, WS_BYTES); }
template <int AUX> __device__ __forceinline__ void glds(rsrc_t rs, LAS char* dst, int voff, int soff) {
  const unsigned d = (unsigned)(size_t)dst;
  if (AUX) asm volatile("s_mov_b32 m0, %0\n\ts_nop 0\n\tbuffer_load_dwordx4 %1, %2, %3 offen sc1 lds" ::"s"(d), "v"(voff), "s"(rs), "s"(soff) : "memory");
  else asm volatile("s_mov_b32 m0, %0\n\ts_nop 0\n\tbuffer_load_dwordx4 %1, %2, %3 offen lds" ::"s"(d), "v"(voff), "s"(rs), "s"(soff) : "memory");
}
__device__ __forceinline__ void swap16(float& a, float& b) {
  auto r = __builtin_amdgcn_permlane16_swap(__builtin_bit_cast(unsigned, a), __builtin_bit_cast(unsigned, b), false, false);
  a = __builtin_bit_cast(float, (unsigned)r[0]); b = __builtin_bit_cast(float, (unsigned)r[1]);
}
__device__ __forceinline__ void swap32(float& a, float& b) {
  auto r = __builtin_amdgcn_permlane32_swap(__builtin_bit_cast(unsigned, a), __builtin_bit_cast(unsigned, b), false, false);
  a = __builtin_bit_cast(float, (unsigned)r[0]); b = __builtin_bit_cast(float, (unsigned)r[1]);
}
__device__ __forceinline__ float xsum1632(float v) {
  float a = v, b = v; swap16(a, b); v = a + b;
  a = v; b = v; swap32(a, b); return a + b;
}
__device__ __forceinline__ float xmax1632(float v) {
  float a = v, b = v; swap16(a, b); v = __builtin_fmaxf(a, b);
  a = v; b = v; swap32(a, b); return __builtin_fmaxf(a, b);
}
template <int CTRL> __device__ __forceinline__ float dppf(float v) { return __builtin_bit_cast(float, __builtin_amdgcn_mov_dpp(__builtin_bit_cast(int, v), CTRL, 0xf, 0xf, true)); }
__device__ __forceinline__ float ror8(float v) { return dppf<0x128>(v); }
__device__ __forceinline__ float gelu_tanh(float x) {
  const float u = x * (1.0f + 0.044715f * x * x) * (-2.0f * 0.7978845608028654f * 1.4426950408889634f);
  return x * __builtin_amdgcn_rcpf(1.0f + __builtin_amdgcn_exp2f(u));
}
__device__ __forceinline__ int xoff(KP p) { return (int)((((size_t)(p->ws + OFF_X) + PG - 1) & ~(size_t)(PG - 1)) - (size_t)p->ws); }
__device__ __forceinline__ unsigned xcc_id() { unsigned x; asm volatile("s_getreg_b32 %0, hwreg(HW_REG_XCC_ID)" : "=s"(x)); return x & 7u; }
#define WAITV(n) asm volatile("s_waitcnt vmcnt(" #n ")" ::: "memory")
#define WAITL(n) asm volatile("s_waitcnt lgkmcnt(" #n ")" ::: "memory")
template <int N> __device__ __forceinline__ void waitv() { asm volatile("s_waitcnt vmcnt(%0)" ::"i"(N) : "memory"); }
__device__ __forceinline__ void bar() { asm volatile("" ::: "memory"); __builtin_amdgcn_s_barrier(); asm volatile("" ::: "memory"); }
#define SCHED() __builtin_amdgcn_sched_barrier(0)
__device__ __forceinline__ int opaque(int x) { asm volatile("" : "+v"(x)); return x; }
__device__ __forceinline__ int sgpr(int x) { int r; asm volatile("s_nop 0\n\tv_readfirstlane_b32 %0, %1\n\ts_nop 4" : "=s"(r) : "v"(x)); return r; }
__device__ __forceinline__ u32x4 ld128(rsrc_t rs, int voff, int soff) { return __builtin_amdgcn_raw_buffer_load_b128(rs, voff, soff, AUX_SC1); }
__device__ __forceinline__ u32x2 ld64(rsrc_t rs, int voff, int soff) { return __builtin_amdgcn_raw_buffer_load_b64(rs, voff, soff, AUX_SC1); }
__device__ __forceinline__ void st128(u32x4 v, rsrc_t rs, int voff, int soff) {
  __builtin_amdgcn_raw_buffer_store_b128(v, rs, voff, soff, AUX_SC1);
  asm volatile("s_nop 1" ::"v"(v) : "memory");
}
__device__ __forceinline__ void st64(u32x2 v, rsrc_t rs, int voff, int soff) { __builtin_amdgcn_raw_buffer_store_b64(v, rs, voff, soff, AUX_SC1); }
__device__ __forceinline__ u32x4 pk8(f32x4 a, f32x4 b) { return u32x4{pk(a[0], a[1]), pk(a[2], a[3]), pk(b[0], b[1]), pk(b[2], b[3])}; }
__device__ __forceinline__ void unpk8(u32x4 u, f32x4& a, f32x4& b) {
  a = f32x4{bflo(u[0]), bfhi(u[0]), bflo(u[1]), bfhi(u[1])}; b = f32x4{bflo(u[2]), bfhi(u[2]), bflo(u[3]), bfhi(u[3])};
}
__device__ __forceinline__ void rnd8(f32x4& a, f32x4& b) { unpk8(pk8(a, b), a, b); }
__device__ __forceinline__ void add8(f32x4& a, f32x4& b, u32x4 r) { f32x4 x, y; unpk8(r, x, y); a += x; b += y; }
__device__ __forceinline__ int lane_id() { int l; asm volatile("v_mbcnt_lo_u32_b32 %0, -1, 0\n\tv_mbcnt_hi_u32_b32 %0, -1, %0" : "=v"(l)); return l; }
#define RLX_AGENT __ATOMIC_RELAXED, __HIP_MEMORY_SCOPE_AGENT

constexpr int C_TMO = 0, C_TICKET = 1  , C_PROD = 9  , C_LINES = 25, C_PROBE = 32  ;
constexpr int R_WO = 0, R_W4 = 32, R_W3 = 64, R_LN3 = 120, R_W1 = 128, R_W2 = 144, R_LN2 = 160, R_FIN = 176, R_CHAIN = 192, R_QKEND = 200;
__device__ __forceinline__ unsigned* cword(KP p, int line) { return (unsigned*)(p->ws + OFF_CTRL) + line * 32; }
__device__ __forceinline__ bool none(bool x) { return __builtin_amdgcn_ballot_w64(x) == 0ull; }
__device__ __forceinline__ void post(KP p, int role, unsigned v) {
  const int l = lane_id();
  if (l < 8) __hip_atomic_store((unsigned*)(p->ws + OFF_FLAG + (role * 8 + l) * 128), v, RLX_AGENT);
}
constexpr unsigned SPIN_CAP = 400000u;
#define WSLEEP 2

#define WSTAMPS 0
#define WSTAMP(role, u, k)
#define TS(k)
#define TS_DECL
#define TS_OUT(role, u)

__global__ __launch_bounds__(512) void k_pre(Params) {
  const KP p = kparams();
  __shared__ float red[128][33];
  const int tid = threadIdx.x, b = blockIdx.x;
  if (b < 192) {
    const int cg = tid & 3, kr = tid >> 2;
    float acc[8];
#pragma unroll
    for (int i = 0; i < 8; ++i) acc[i] = 0.f;
    u32x4 w[8];
#pragma unroll
    for (int it = 0; it < 8; ++it) w[it] = *(const u32x4*)(p->wmod + (size_t)(it * 128 + kr) * 6144 + b * 32 + cg * 8);
#pragma unroll
    for (int it = 0; it < 8; ++it) {
      const float cv = bf2f(p->c[it * 128 + kr]);
      const float sv = rbf(cv / (1.0f + __expf(-cv)));
#pragma unroll
      for (int i = 0; i < 4; ++i) { acc[2 * i] += sv * bflo(w[it][i]); acc[2 * i + 1] += sv * bfhi(w[it][i]); }
    }
#pragma unroll
    for (int i = 0; i < 8; ++i) red[kr][cg * 8 + i] = acc[i];
    __syncthreads();
    if (tid < 128) {
      float sum = 0.f;
#pragma unroll
      for (int r = 0; r < 32; ++r) sum += red[(tid >> 5) * 32 + r][tid & 31];
      red[(tid >> 5) * 32][tid & 31] = sum;
    }
    __syncthreads();
    if (tid < 32) ((float*)(p->ws + OFF_MOD))[b * 32 + tid] = rbf((red[0][tid] + red[32][tid]) + (red[64][tid] + red[96][tid]));
  } else if (b < 202) {
    if (tid < 64) {
      const int v = b - 192;
      unsigned char* q = p->ws + xoff(p) + (v / 5) * PG + (v % 5) * 131072 + 1048576;
      if (tid < 10) __hip_atomic_store((unsigned*)(q + (tid & 1) * 8192 + (tid >> 1) * 16384), SENT, RLX_AGENT);
      WAITV(0);
      unsigned best[2] = {1u << 30, 1u << 30};
#pragma unroll
      for (int r = 0; r < 10; ++r) {
        unsigned d; unsigned long long t0, t1;
        asm volatile("s_memtime %0\n\ts_waitcnt lgkmcnt(0)" : "=s"(t0)::"memory");
        asm volatile("global_load_dword %0, %1, off sc1\n\ts_waitcnt vmcnt(0)" : "=v"(d) : "v"(q + (r & 1) * 8192 + (r >> 1) * 16384) : "memory");
        asm volatile("s_memtime %0\n\ts_waitcnt lgkmcnt(0)" : "=s"(t1)::"memory");
        best[r & 1] = min(best[r & 1], (unsigned)(t1 - t0));
      }
      if (tid == 0) cword(p, C_PROBE)[v] = (unsigned)(best[1] < best[0]) ^ (xcc_id() >> 2);
    }
  } else if (b == 202) {
    const u32x4 z = {0u, 0u, 0u, 0u};
#pragma unroll
    for (int i = 0; i < 4; ++i) {
      *(u32x4*)(p->ws + OFF_G + (i * 512 + tid) * 16) = z;
      *(u32x4*)(p->ws + OFF_G + 129 * 32768 + (i * 512 + tid) * 16) = z;
      *(unsigned*)(p->ws + OFF_FLAG + (i * 512 + tid) * 128) = 0u;
    }
    if (tid < C_LINES) *cword(p, tid) = 0u;
  } else {
    const u32x4 sv = {SENT, SENT, SENT, SENT};
    unsigned char* x = p->ws + xoff(p);
    for (int i = (b - 203) * 512 + tid; i < (802816 + PG) / 16; i += 53 * 512) {
      const int o = i * 16;
      *(u32x4*)(x + (o < 802816 ? o : o + (PG - 802816))) = sv;
    }
  }
}

__device__ __forceinline__ float wsum(float v) {
  v += dppf<0xB1>(v);
  v += dppf<0x4E>(v);
  v += dppf<0x124>(v);
  v += dppf<0x128>(v);
  return xsum1632(v);
}
struct Row { f32x4 v[4]; };
__device__ __forceinline__ float hsum(f32x4 a) { return (a[0] + a[1]) + (a[2] + a[3]); }
__device__ __forceinline__ void norm_row(Row& r) {
  const float mean = wsum(hsum((r.v[0] + r.v[1]) + (r.v[2] + r.v[3]))) * (1.0f / DM);
  f32x4 q = {0.f, 0.f, 0.f, 0.f};
#pragma unroll
  for (int i = 0; i < 4; ++i) { const f32x4 d = r.v[i] - mean; q += d * d; }
  const float sd = rbf(sqrtf(rbf(rbf(wsum(hsum(q)) * (1.0f / DM)) + LN_EPS))), mb = rbf(mean), rs = 1.0f / sd;
#pragma unroll
  for (int i = 0; i < 4; ++i) r.v[i] -= mb;
  rnd8(r.v[0], r.v[1]); rnd8(r.v[2], r.v[3]);
#pragma unroll
  for (int i = 0; i < 4; ++i) { const f32x4 t = r.v[i] * rs; r.v[i] = __builtin_elementwise_fma(__builtin_elementwise_fma(-t, (f32x4)sd, r.v[i]), (f32x4)rs, t); }
}
__global__ __launch_bounds__(512) void k_ln1(Params) {
  const KP p = kparams();
  const int l16 = (threadIdx.x & 63) * 16, row = blockIdx.x * 8 + (threadIdx.x >> 6);
  const unsigned char* src = (const unsigned char*)p->x + row * 2048 + l16;
  const float* sp = (const float*)(p->ws + OFF_MOD) + l16 / 2;
  Row v, sc, sh;
  unpk8(*(const u32x4*)src, v.v[0], v.v[1]); unpk8(*(const u32x4*)(src + 1024), v.v[2], v.v[3]);
#pragma unroll
  for (int i = 0; i < 4; ++i) { sh.v[i] = *(const f32x4*)(sp + (i >> 1) * 512 + (i & 1) * 4); sc.v[i] = *(const f32x4*)(sp + DM + (i >> 1) * 512 + (i & 1) * 4) + 1.0f; }
  rnd8(sc.v[0], sc.v[1]); rnd8(sc.v[2], sc.v[3]);
  norm_row(v);
  rnd8(v.v[0], v.v[1]); rnd8(v.v[2], v.v[3]);
#pragma unroll
  for (int i = 0; i < 4; ++i) v.v[i] *= sc.v[i];
  rnd8(v.v[0], v.v[1]); rnd8(v.v[2], v.v[3]);
#pragma unroll
  for (int i = 0; i < 4; ++i) v.v[i] += sh.v[i];
  unsigned char* dst = p->ws + OFF_H + row * 2048 + l16;
  *(u32x4*)dst = pk8(v.v[0], v.v[1]); *(u32x4*)(dst + 1024) = pk8(v.v[2], v.v[3]);
}

struct GemmArgs { rsrc_t ra, rb; int a_off, lda, b_off, ldb, nk; };

template <int BM, int BN, int WM, int WN, int NST> __device__ __forceinline__ void gemm_tile(LAS char* lds, int w, const GemmArgs& ga) {
  constexpr int TM = BM / WM / 16, TN = BN / WN / 16;
  constexpr int A_BYTES = BM * 128, B_BYTES = BN * 128, ST_BYTES = A_BYTES + B_BYTES;
  constexpr int LA = BM / 64, LB = BN / 64, LD = LA + LB;
  constexpr int RB = BN * 2;
  constexpr int NMMA = TM * TN, NRD = TM + 2 * TN;
  static_assert(WM * WN == 8 && (BN == 64 || BN == 128 || BN == 256) && BM % 64 == 0 && NST * ST_BYTES <= 163840, "shape");
  const int l = lane_id(), r16 = l & 15, g = l >> 4;
  const int wm = w / WN, wn = w % WN, lda = ga.lda, ldb = ga.ldb, nk = ga.nk;
  f32x4 acc[TN][TM];
#pragma unroll
  for (int i = 0; i < TN; ++i)
#pragma unroll
    for (int j = 0; j < TM; ++j) acc[i][j] = f32x4{0.f, 0.f, 0.f, 0.f};
  const int va = (8 * w + (l >> 3)) * lda + (((l & 7) ^ ((4 * w + (l >> 4)) & 7)) << 4);
  int vb;
  if (BN == 256) { const int k = 2 * w + (l >> 5), f = (k & 3) | (((k >> 3) & 1) << 2); vb = k * ldb + (((l & 31) ^ (f << 1)) << 4); }
  else if (BN == 128) { const int k = 4 * w + (l >> 4), f = (k & 3) | (((k >> 3) & 1) << 2); vb = k * ldb + (((l & 15) ^ (f << 1)) << 4); }
  else { const int k = 8 * w + (l >> 3), f = ((k >> 1) & 1) | (((k >> 3) & 1) << 1); vb = k * ldb + (((l & 7) ^ (f << 1)) << 4); }
  constexpr int BROWS = 8192 / RB;
  auto piece = [&](int i, int sa, int sb, LAS char* dst) {
    if (i < LA) glds<0>(ga.ra, dst + i * 8192, va, sa + i * 64 * lda);
    else glds<0>(ga.rb, dst + A_BYTES + (i - LA) * 8192, vb, sb + (i - LA) * BROWS * ldb);
  };
  auto aoff = [&](int kt) { return ga.a_off + kt * 128; };
  const int fa = (wm * TM * 16 + r16) * 128 + ((g ^ (r16 >> 1)) << 4);
  const int q = r16 >> 2, pp = r16 & 3;
  int fb[TN];
#pragma unroll
  for (int ni = 0; ni < TN; ++ni) {
    const int nt = wn * TN + ni;
    const int f = BN == 64 ? (((q >> 1) & 1) | ((g & 1) << 1)) : (q | ((g & 1) << 2));
    fb[ni] = A_BYTES + (8 * g + q) * RB + ((nt ^ f) << 5) + pp * 8;
  }
  u32x4 A0[TM], A1[TM];
  u32x2 B0[TN][2], B1[TN][2];
  auto rd = [&](int i, LAS const char* base, int ks, u32x4 (&Af)[TM], u32x2 (&Bf)[TN][2]) {
    if (i < 2 * TN) Bf[i >> 1][i & 1] = trread(base + fb[i >> 1] + (32 * ks + 4 * (i & 1)) * RB);
    else Af[i - 2 * TN] = lds128(base + (fa ^ (ks * 64)) + (i - 2 * TN) * 2048);
  };
  auto mm = [&](int i, const u32x4 (&Af)[TM], const u32x2 (&Bf)[TN][2]) {
    const int mi = i / TN, ni = i % TN;
    acc[ni][mi] = mfma(u32x4{Bf[ni][0].x, Bf[ni][0].y, Bf[ni][1].x, Bf[ni][1].y}, Af[mi], acc[ni][mi]);
  };
#pragma nounroll
  for (int s = 0; s < NST; ++s)
#pragma unroll
    for (int i = 0; i < LD; ++i) piece(i, aoff(s), ga.b_off + s * 64 * ldb, lds + s * ST_BYTES + w * 1024);
  waitv<(NST - 1) * LD>();
  bar();
#pragma unroll
  for (int i = 0; i < NRD; ++i) rd(i, lds, 0, A0, B0);
  int buf = 0;
#pragma nounroll
  for (int kt = 0; kt < nk; ++kt) {
    LAS const char* cur = lds + buf * ST_BYTES;
#pragma unroll
    for (int i = 0; i < NMMA; ++i) {
      mm(i, A0, B0);
#pragma unroll
      for (int r = 0; r < NRD; ++r) if (r * (NMMA * 3 / 4) / NRD == i) rd(r, cur, 1, A1, B1);
      SCHED();
    }
    const bool more = kt + NST < nk;
    WAITL(0);
    if (kt + NST <= nk) waitv<(NST - 2) * LD>(); else WAITV(0);
    bar();
    const int sa = aoff(kt + NST), sb = ga.b_off + (kt + NST) * 64 * ldb;
    LAS char* dst = lds + buf * ST_BYTES + w * 1024;
    buf = buf + 1 == NST ? 0 : buf + 1;
    LAS const char* nxt = lds + buf * ST_BYTES;
#pragma unroll
    for (int i = 0; i < NMMA; ++i) {
      mm(i, A1, B1);
#pragma unroll
      for (int r = 0; r < NRD; ++r) if (r * (NMMA * 3 / 4) / NRD == i) rd(r, nxt, 0, A0, B0);
#pragma unroll
      for (int r = 0; r < LD; ++r) if ((r * NMMA + NMMA / 2) / LD == i && more) piece(r, sa, sb, dst);
      SCHED();
    }
  }
  LAS char* tw = lds + (wm * TM * 16 + r16) * (2 * BN + 16) + (wn * TN * 16 + 4 * g) * 2;
#pragma unroll
  for (int mi = 0; mi < TM; ++mi)
#pragma unroll
    for (int ni = 0; ni < TN; ++ni)
      *(LAS u32x2*)(tw + mi * 16 * (2 * BN + 16) + ni * 32) = u32x2{pk(acc[ni][mi][0], acc[ni][mi][1]), pk(acc[ni][mi][2], acc[ni][mi][3])};
  WAITL(0);
  bar();
}

extern __shared__ __attribute__((aligned(16))) char smem[];

__device__ __forceinline__ void epi_qkv(KP p, LAS char* lds, int w, int rb, int cb) {
  constexpr int PITCH = 528;
  const int l = lane_id(), r16 = l & 15, g = l >> 4;
  const int which = cb >> 4, sl = w >> 1, hh = w & 1;
  const rsrc_t rw = wsr(p);
  const int so = OFF_QKV + ((cb & 15) * 2 + hh) * QKV_H + (rb * 4 + sl) * QKV_S + which * 8192;
  LAS const char* t = lds + sl * 32 * PITCH + hh * 256;
  if (which < 2) {
    t += r16 * PITCH + g * 8;
#pragma unroll 2
    for (int f = 0; f < 8; ++f) {
      const u32x2 lo = lds64(t + (f >> 2) * 16 * PITCH + (f & 3) * 64), hi = lds64(t + (f >> 2) * 16 * PITCH + (f & 3) * 64 + 32);
      st128(u32x4{lo.x, lo.y, hi.x, hi.y}, rw, l * 16, so + f * 1024);
    }
  } else {
    t += (4 * g + (r16 >> 2)) * PITCH + (r16 & 3) * 8;
#pragma unroll 2
    for (int f = 0; f < 8; ++f) {
      const u32x2 lo = trread(t + f * 32), hi = trread(t + 16 * PITCH + f * 32);
      st128(u32x4{lo.x, lo.y, hi.x, hi.y}, rw, l * 16, so + f * 1024);
    }
  }
}

constexpr int NREG = 24, NLDS = 8;
constexpr int CL_M = 0;
constexpr int CL_KX = 16384;
constexpr int CL_IMG = 24576;
constexpr int CL_PX = 28672;
constexpr int CL_W = 32768;
constexpr int LDS_BYTES = 163840;

__device__ __forceinline__ void wstage(rsrc_t rw, int soff, LAS char* dst, int w, int l) {
  const int r = 4 * w + (l >> 4);
  const int f = (r & 1) | (((r >> 2) & 3) << 1);
  const int v = r * 8192 + (((l & 15) ^ (f << 1)) << 4);
  glds<0>(rw, dst + w * 1024, v, soff);
  glds<0>(rw, dst + 8192 + w * 1024, v, soff + 32 * 8192);
}
__device__ __forceinline__ u32x8 wfrag(LAS const char* img, int nt, int l) {
  const int g = l >> 4, q = (l & 15) >> 2, pp = l & 3;
  const int rx = 16 * (g >> 1) + 4 * (g & 1) + (q & 1) + 8 * (q >> 1);
  const int f = (rx & 1) | (((rx >> 2) & 3) << 1);
  LAS const char* a = img + rx * 256 + ((nt ^ f) << 5) + pp * 8;
  const u32x2 x0 = trread(a), x1 = trread(a + 2 * 256), x2 = trread(a + 32 * 256), x3 = trread(a + 34 * 256);
  return u32x8{x0.x, x0.y, x1.x, x1.y, x2.x, x2.y, x3.x, x3.y};
}
__device__ __forceinline__ u32x8 get_frag_tr(LAS const char* wl8, int i) {
  const u32x2 x0 = trread(wl8 + i * 2048), x1 = trread(wl8 + i * 2048 + 512), x2 = trread(wl8 + i * 2048 + 1024), x3 = trread(wl8 + i * 2048 + 1536);
  return u32x8{x0.x, x0.y, x1.x, x1.y, x2.x, x2.y, x3.x, x3.y};
}
template <class SRC> __device__ __forceinline__ void load8(LAS char* lds, int w, int l, SRC src, u32x8* out) {
#pragma unroll
  for (int i = 0; i < 8; ++i) src(i, lds + CL_W + i * 16384);
  WAITV(0);
  bar();
#pragma unroll
  for (int i = 0; i < 8; ++i) out[i] = wfrag(lds + CL_W + i * 16384, w, l);
  WAITL(0);
  bar();
}
template <class OFF> __device__ __forceinline__ void load8_image(LAS char* lds, int w, int l, rsrc_t r, int ld, OFF off) {
  const int gq = (l >> 3) & 3, qq = (l >> 1) & 3;
  const int vo = (16 * (gq >> 1) + 4 * (gq & 1) + (qq & 1) + 8 * (qq >> 1) + 2 * (l >> 5)) * ld + (l & 1) * 16;
  LAS char* wd = lds + CL_W + w * (NLDS * 2048);
#pragma unroll
  for (int t = 0; t < 8; ++t) {
    glds<0>(r, wd + t * 2048, vo, off(t));
    glds<0>(r, wd + t * 2048 + 1024, vo, off(t) + 32 * ld);
  }
  WAITV(0);
}
__device__ __forceinline__ void image_to_frags(LAS char* lds, int w, int l) {
  LAS char* wd = lds + CL_W + w * (NLDS * 2048);
#pragma nounroll
  for (int t = 0; t < 8; ++t) {
    const u32x8 f = get_frag_tr(wd + l * 8 + t * 2048, 0);
    WAITL(0);
    *(LAS u32x4*)(wd + t * 2048 + l * 16) = u32x4{f[0], f[1], f[2], f[3]};
    *(LAS u32x4*)(wd + t * 2048 + 1024 + l * 16) = u32x4{f[4], f[5], f[6], f[7]};
  }
}
__device__ __forceinline__ void stage_m0(KP p, LAS char* lds, int w, int l) {
#pragma unroll
  for (int i = 0; i < 2; ++i)
    *(LAS u32x4*)(lds + CL_M + (2 * w + i) * 1024 + l * 16) = *(const u32x4*)(p->mem0 + (l & 7) * DM + 64 * (2 * w + i) + 16 * (l >> 4) + 8 * ((l >> 3) & 1));
  WAITL(0);
}

constexpr int CHAIN_FLAG_OFF = 195067904;
__device__ __forceinline__ bool qk_loop(const u32x8 (&W)[NREG], rsrc_t rw, u32x8 sc) {
  int ok;
  asm volatile(
    "s_mov_b32 %0, 0\n"
    "s_mov_b32 s95, 0\n"
    "v_mbcnt_lo_u32_b32 v63, -1, 0\n"
    "v_mbcnt_hi_u32_b32 v63, -1, v63\n"
    "v_lshlrev_b32 v0, 4, v63\n"
    "v_and_b32 v62, 15, v63\n"
    "v_lshrrev_b32 v61, 4, v63\n"
    "v_and_b32 v60, 8, v63\n"
    "v_cmp_ne_u32 vcc, 0, v60\n"
    "v_mov_b32 v1, 0x4444\n"
    "v_mov_b32 v60, 0xeeee\n"
    "v_cndmask_b32 v1, v1, v60, vcc\n"
    "s_lshl_b32 s76, s68, 14\n"
    "s_add_u32 s76, s76, 32768\n"
    "v_add_u32 v2, s76, v0\n"
    "s_lshl_b32 s76, s68, 11\n"
    "v_add_u32 v3, s76, v0\n"
    "s_lshl_b32 s76, s68, 9\n"
    "s_add_u32 s76, s76, 24576\n"
    "v_lshlrev_b32 v10, 5, v62\n"
    "v_lshl_add_u32 v10, v61, 3, v10\n"
    "v_add_u32 v10, s76, v10\n"
    "v_and_b32 v11, 31, v63\n"
    "v_lshlrev_b32 v11, 3, v11\n"
    "v_and_b32 v60, 7, v63\n"
    "v_lshlrev_b32 v60, 4, v60\n"
    "v_lshl_add_u32 v12, v61, 7, v60\n"
    "v_add_u32 v12, 28672, v12\n"
    "v_and_b32 v59, 1, v61\n"
    "v_lshl_add_u32 v13, v59, 3, v60\n"
    "v_lshrrev_b32 v58, 1, v61\n"
    "v_lshl_add_u32 v14, v58, 7, v13\n"
    "v_add_u32 v13, 29184, v13\n"
    "v_lshrrev_b32 v57, 3, v62\n"
    "v_lshl_add_u32 v14, v57, 17, v14\n"
    "v_cmp_le_u32 s[78:79], 32, v63\n"
    "s_lshl_b32 s77, s68, 10\n"
    "s_add_u32 s98, s77, 16384\n"
    "s_sub_u32 s76, s68, 1\n"
    "s_lshl_b32 s76, s76, 4\n"
    "v_lshlrev_b32 v60, 2, v61\n"
    "v_sub_u32 v60, v62, v60\n"
    "v_add_u32 v60, s76, v60\n"
    "v_cmp_gt_i32 s[40:41], 0, v60\n"
    "v_cmp_gt_i32 s[48:49], 16, v60\n"
    "v_cmp_gt_i32 s[42:43], 1, v60\n"
    "v_cmp_gt_i32 s[50:51], 17, v60\n"
    "v_cmp_gt_i32 s[44:45], 2, v60\n"
    "v_cmp_gt_i32 s[52:53], 18, v60\n"
    "v_cmp_gt_i32 s[46:47], 3, v60\n"
    "v_cmp_gt_i32 s[54:55], 19, v60\n"
    "s_mov_b32 s80, 0\n"
    "s_mov_b32 s81, s73\n"
    "s_mov_b32 s82, s74\n"
    "s_mov_b32 s86, 0x3e0293ee\n"
    "s_mov_b32 s87, 0x3db504f3\n"
    "s_mov_b32 s88, 0x3fb8aa3b\n"
    "s_add_u32 s76, s81, s77\n"
    "s_add_u32 s76, s76, 8192\n"
    "s_mov_b32 m0, s98\n"
    "s_nop 0\n"
    "buffer_load_dwordx4 v0, s[64:67], s76 offen sc1 lds\n"
    "s_add_u32 s76, s76, 8192\n"
    "buffer_load_dwordx4 v[4:7], v0, s[64:67], s76 offen sc1\n"
    "ds_read_b128 v[56:59], v3\n"
    "ds_read_b128 v[60:63], v3 offset:1024\n"
    "s_waitcnt vmcnt(0)\n"
    "s_memrealtime s[96:97]\n"
    "v_mov_b32 v16, 29312\n"
    "s_waitcnt lgkmcnt(0)\n"
    "v_mov_b32 v18, s96\n"
    "v_mov_b32 v19, s96\n"
    "v_mov_b32 v20, 0\n"
    "ds_write_b64 v16, v[18:19]\n"
    "ds_write_b32 v16, v20 offset:16\n"
    "s_getpc_b64 s[56:57]\n"
    "pc0_%=:\n"
    "s_mul_i32 s76, s68, var1_%=-var0_%=\n"
    "s_add_u32 s76, s76, var0_%=-pc0_%=\n"
    "s_add_u32 s56, s56, s76\n"
    "s_addc_u32 s57, s57, 0\n"
    "s_and_b32 s76, s80, 1\n"
    "s_lshl_b32 s76, s76, 17\n"
    "s_add_u32 s83, s71, s76\n"
    "s_and_b32 s76, s80, 3\n"
    "s_lshl_b32 s76, s76, 19\n"
    "s_add_u32 s84, s72, s76\n"
    "s_add_u32 s85, s84, 262144\n"
    "s_mov_b32 s94, 0\n"
    "v_mov_b64 v[16:17], 0\n"
    "v_mov_b64 v[18:19], 0\n"
    "v_mov_b64 v[20:21], 0\n"
    "v_mov_b64 v[22:23], 0\n"
    "s_cmp_eq_u32 s80, s80\n"
    "s_nop 0\n"
    "s_setpc_b64 s[56:57]\n"
    "var0_%=:\n"
    "ds_read_b128 v[40:43], v2 offset:0\n"
    "ds_read_b128 v[44:47], v2 offset:1024\n"
    "ds_read_b128 v[48:51], v2 offset:2048\n"
    "ds_read_b128 v[52:55], v2 offset:3072\n"
    "s_cbranch_scc1 go0_%=\n"
    "s_nop 0\n"
    "mwait0_%=:\n"
    "s_memrealtime s[96:97]\n"
    "s_waitcnt lgkmcnt(0)\n"
    "s_sub_u32 s96, s92, s96\n"
    "s_sub_u32 s96, s96, 1\n"
    "s_cmp_lt_u32 s96, 1000\n"
    "s_cbranch_scc1 mwait0_%=\n"
    "mpoll0_%=:\n"
    "buffer_load_dwordx4 v[56:59], v0, s[64:67], s91 offen sc1\n"
    "buffer_load_dwordx4 v[60:63], v0, s[64:67], s91 offen offset:1024 sc1\n"
    "s_waitcnt vmcnt(1)\n"
    "ds_write_b128 v3, v[56:59]\n"
    "v_max3_u32 v24, v56, v57, v58\n"
    "s_waitcnt vmcnt(0)\n"
    "ds_write_b128 v3, v[60:63] offset:1024\n"
    "v_max3_u32 v24, v24, v59, v60\n"
    "v_max3_u32 v24, v24, v61, v62\n"
    "v_max_u32 v24, v24, v63\n"
    "v_cmp_eq_u32 vcc, -1, v24\n"
    "s_cbranch_vccnz mretry0_%=\n"
    "go0_%=:\n"
    "s_waitcnt lgkmcnt(0)\n"
    "s_barrier\n"
    "ds_read_b128 v[24:27], v0 offset:2048\n"
    "ds_read_b128 v[28:31], v0 offset:3072\n"
    "ds_read_b128 v[32:35], v0 offset:4096\n"
    "ds_read_b128 v[36:39], v0 offset:5120\n"
    "v_smfmac_f32_16x16x64_bf16 v[16:19], v[56:59], v[64:71], v1\n"
    "v_smfmac_f32_16x16x64_bf16 v[20:23], v[56:59], v[192:199], v1\n"
    "ds_read_b128 v[56:59], v0 offset:6144\n"
    "v_smfmac_f32_16x16x64_bf16 v[20:23], v[60:63], v[40:47], v1\n"
    "ds_read_b128 v[40:43], v2 offset:4096\n"
    "v_smfmac_f32_16x16x64_bf16 v[16:19], v[60:63], v[128:135], v1\n"
    "ds_read_b128 v[44:47], v2 offset:5120\n"
    "s_waitcnt lgkmcnt(6)\n"
    "v_smfmac_f32_16x16x64_bf16 v[16:19], v[24:27], v[72:79], v1\n"
    "ds_read_b128 v[60:63], v0 offset:7168\n"
    "v_smfmac_f32_16x16x64_bf16 v[20:23], v[24:27], v[200:207], v1\n"
    "ds_read_b128 v[24:27], v0 offset:8192\n"
    "s_waitcnt lgkmcnt(7)\n"
    "v_smfmac_f32_16x16x64_bf16 v[20:23], v[28:31], v[48:55], v1\n"
    "ds_read_b128 v[48:51], v2 offset:6144\n"
    "v_smfmac_f32_16x16x64_bf16 v[16:19], v[28:31], v[136:143], v1\n"
    "ds_read_b128 v[52:55], v2 offset:7168\n"
    "s_waitcnt lgkmcnt(8)\n"
    "v_smfmac_f32_16x16x64_bf16 v[16:19], v[32:35], v[80:87], v1\n"
    "ds_read_b128 v[28:31], v0 offset:9216\n"
    "v_smfmac_f32_16x16x64_bf16 v[20:23], v[32:35], v[208:215], v1\n"
    "ds_read_b128 v[32:35], v0 offset:10240\n"
    "s_waitcnt lgkmcnt(9)\n"
    "s_waitcnt lgkmcnt(6)\n"
    "v_smfmac_f32_16x16x64_bf16 v[20:23], v[36:39], v[40:47], v1\n"
    "ds_read_b128 v[40:43], v2 offset:8192\n"
    "v_smfmac_f32_16x16x64_bf16 v[16:19], v[36:39], v[144:151], v1\n"
    "ds_read_b128 v[44:47], v2 offset:9216\n"
    "v_smfmac_f32_16x16x64_bf16 v[16:19], v[56:59], v[88:95], v1\n"
    "ds_read_b128 v[36:39], v0 offset:11264\n"
    "v_smfmac_f32_16x16x64_bf16 v[20:23], v[56:59], v[216:223], v1\n"
    "ds_read_b128 v[56:59], v0 offset:12288\n"
    "s_waitcnt lgkmcnt(9)\n"
    "s_waitcnt lgkmcnt(6)\n"
    "v_smfmac_f32_16x16x64_bf16 v[20:23], v[60:63], v[48:55], v1\n"
    "ds_read_b128 v[48:51], v2 offset:10240\n"
    "v_smfmac_f32_16x16x64_bf16 v[16:19], v[60:63], v[152:159], v1\n"
    "ds_read_b128 v[52:55], v2 offset:11264\n"
    "v_smfmac_f32_16x16x64_bf16 v[16:19], v[24:27], v[96:103], v1\n"
    "ds_read_b128 v[60:63], v0 offset:13312\n"
    "v_smfmac_f32_16x16x64_bf16 v[20:23], v[24:27], v[224:231], v1\n"
    "ds_read_b128 v[24:27], v0 offset:14336\n"
    "s_waitcnt lgkmcnt(9)\n"
    "s_waitcnt lgkmcnt(6)\n"
    "v_smfmac_f32_16x16x64_bf16 v[20:23], v[28:31], v[40:47], v1\n"
    "ds_read_b128 v[40:43], v2 offset:12288\n"
    "v_smfmac_f32_16x16x64_bf16 v[16:19], v[28:31], v[160:167], v1\n"
    "ds_read_b128 v[44:47], v2 offset:13312\n"
    "v_smfmac_f32_16x16x64_bf16 v[16:19], v[32:35], v[104:111], v1\n"
    "ds_read_b128 v[28:31], v0 offset:15360\n"
    "v_smfmac_f32_16x16x64_bf16 v[20:23], v[32:35], v[232:239], v1\n"
    "s_waitcnt lgkmcnt(8)\n"
    "s_waitcnt lgkmcnt(5)\n"
    "v_smfmac_f32_16x16x64_bf16 v[20:23], v[36:39], v[48:55], v1\n"
    "ds_read_b128 v[48:51], v2 offset:14336\n"
    "v_smfmac_f32_16x16x64_bf16 v[16:19], v[36:39], v[168:175], v1\n"
    "ds_read_b128 v[52:55], v2 offset:15360\n"
    "v_smfmac_f32_16x16x64_bf16 v[16:19], v[56:59], v[112:119], v1\n"
    "v_smfmac_f32_16x16x64_bf16 v[20:23], v[56:59], v[240:247], v1\n"
    "s_waitcnt lgkmcnt(6)\n"
    "s_waitcnt lgkmcnt(3)\n"
    "v_smfmac_f32_16x16x64_bf16 v[20:23], v[60:63], v[40:47], v1\n"
    "v_smfmac_f32_16x16x64_bf16 v[16:19], v[60:63], v[176:183], v1\n"
    "v_smfmac_f32_16x16x64_bf16 v[16:19], v[24:27], v[120:127], v1\n"
    "v_smfmac_f32_16x16x64_bf16 v[20:23], v[24:27], v[248:255], v1\n"
    "s_waitcnt lgkmcnt(2)\n"
    "s_waitcnt lgkmcnt(0)\n"
    "v_smfmac_f32_16x16x64_bf16 v[20:23], v[28:31], v[48:55], v1\n"
    "v_smfmac_f32_16x16x64_bf16 v[16:19], v[28:31], v[184:191], v1\n"
    "s_branch gemvdone_%=\n"
    "mretry0_%=:\n"
    "s_nop 7\n"
    "s_sleep 3\n"
    "s_add_u32 s94, s94, 1\n"
    "s_cmp_lt_u32 s94, 400000\n"
    "s_cbranch_scc1 mpoll0_%=\n"
    "s_branch fail_%=\n"
    "var1_%=:\n"
    "ds_read_b128 v[40:43], v2 offset:2048\n"
    "ds_read_b128 v[44:47], v2 offset:3072\n"
    "ds_read_b128 v[48:51], v2 offset:0\n"
    "ds_read_b128 v[52:55], v2 offset:1024\n"
    "s_cbranch_scc1 go1_%=\n"
    "s_nop 0\n"
    "mwait1_%=:\n"
    "s_memrealtime s[96:97]\n"
    "s_waitcnt lgkmcnt(0)\n"
    "s_sub_u32 s96, s92, s96\n"
    "s_sub_u32 s96, s96, 1\n"
    "s_cmp_lt_u32 s96, 1000\n"
    "s_cbranch_scc1 mwait1_%=\n"
    "mpoll1_%=:\n"
    "buffer_load_dwordx4 v[56:59], v0, s[64:67], s91 offen sc1\n"
    "buffer_load_dwordx4 v[60:63], v0, s[64:67], s91 offen offset:1024 sc1\n"
    "s_waitcnt vmcnt(1)\n"
    "ds_write_b128 v3, v[56:59]\n"
    "v_max3_u32 v24, v56, v57, v58\n"
    "s_waitcnt vmcnt(0)\n"
    "ds_write_b128 v3, v[60:63] offset:1024\n"
    "v_max3_u32 v24, v24, v59, v60\n"
    "v_max3_u32 v24, v24, v61, v62\n"
    "v_max_u32 v24, v24, v63\n"
    "v_cmp_eq_u32 vcc, -1, v24\n"
    "s_cbranch_vccnz mretry1_%=\n"
    "go1_%=:\n"
    "s_waitcnt lgkmcnt(0)\n"
    "s_barrier\n"
    "ds_read_b128 v[24:27], v0 offset:0\n"
    "ds_read_b128 v[28:31], v0 offset:1024\n"
    "ds_read_b128 v[32:35], v0 offset:4096\n"
    "ds_read_b128 v[36:39], v0 offset:5120\n"
    "v_smfmac_f32_16x16x64_bf16 v[16:19], v[56:59], v[72:79], v1\n"
    "v_smfmac_f32_16x16x64_bf16 v[20:23], v[56:59], v[200:207], v1\n"
    "ds_read_b128 v[56:59], v0 offset:6144\n"
    "v_smfmac_f32_16x16x64_bf16 v[20:23], v[60:63], v[40:47], v1\n"
    "ds_read_b128 v[40:43], v2 offset:4096\n"
    "v_smfmac_f32_16x16x64_bf16 v[16:19], v[60:63], v[136:143], v1\n"
    "ds_read_b128 v[44:47], v2 offset:5120\n"
    "s_waitcnt lgkmcnt(6)\n"
    "v_smfmac_f32_16x16x64_bf16 v[16:19], v[24:27], v[64:71], v1\n"
    "ds_read_b128 v[60:63], v0 offset:7168\n"
    "v_smfmac_f32_16x16x64_bf16 v[20:23], v[24:27], v[192:199], v1\n"
    "ds_read_b128 v[24:27], v0 offset:8192\n"
    "s_waitcnt lgkmcnt(7)\n"
    "v_smfmac_f32_16x16x64_bf16 v[20:23], v[28:31], v[48:55], v1\n"
    "ds_read_b128 v[48:51], v2 offset:6144\n"
    "v_smfmac_f32_16x16x64_bf16 v[16:19], v[28:31], v[128:135], v1\n"
    "ds_read_b128 v[52:55], v2 offset:7168\n"
    "s_waitcnt lgkmcnt(8)\n"
    "v_smfmac_f32_16x16x64_bf16 v[16:19], v[32:35], v[80:87], v1\n"
    "ds_read_b128 v[28:31], v0 offset:9216\n"
    "v_smfmac_f32_16x16x64_bf16 v[20:23], v[32:35], v[208:215], v1\n"
    "ds_read_b128 v[32:35], v0 offset:10240\n"
    "s_waitcnt lgkmcnt(9)\n"
    "s_waitcnt lgkmcnt(6)\n"
    "v_smfmac_f32_16x16x64_bf16 v[20:23], v[36:39], v[40:47], v1\n"
    "ds_read_b128 v[40:43], v2 offset:8192\n"
    "v_smfmac_f32_16x16x64_bf16 v[16:19], v[36:39], v[144:151], v1\n"
    "ds_read_b128 v[44:47], v2 offset:9216\n"
    "v_smfmac_f32_16x16x64_bf16 v[16:19], v[56:59], v[88:95], v1\n"
    "ds_read_b128 v[36:39], v0 offset:11264\n"
    "v_smfmac_f32_16x16x64_bf16 v[20:23], v[56:59], v[216:223], v1\n"
    "ds_read_b128 v[56:59], v0 offset:12288\n"
    "s_waitcnt lgkmcnt(9)\n"
    "s_waitcnt lgkmcnt(6)\n"
    "v_smfmac_f32_16x16x64_bf16 v[20:23], v[60:63], v[48:55], v1\n"
    "ds_read_b128 v[48:51], v2 offset:10240\n"
    "v_smfmac_f32_16x16x64_bf16 v[16:19], v[60:63], v[152:159], v1\n"
    "ds_read_b128 v[52:55], v2 offset:11264\n"
    "v_smfmac_f32_16x16x64_bf16 v[16:19], v[24:27], v[96:103], v1\n"
    "ds_read_b128 v[60:63], v0 offset:13312\n"
    "v_smfmac_f32_16x16x64_bf16 v[20:23], v[24:27], v[224:231], v1\n"
    "ds_read_b128 v[24:27], v0 offset:14336\n"
    "s_waitcnt lgkmcnt(9)\n"
    "s_waitcnt lgkmcnt(6)\n"
    "v_smfmac_f32_16x16x64_bf16 v[20:23], v[28:31], v[40:47], v1\n"
    "ds_read_b128 v[40:43], v2 offset:12288\n"
    "v_smfmac_f32_16x16x64_bf16 v[16:19], v[28:31], v[160:167], v1\n"
    "ds_read_b128 v[44:47], v2 offset:13312\n"
    "v_smfmac_f32_16x16x64_bf16 v[16:19], v[32:35], v[104:111], v1\n"
    "ds_read_b128 v[28:31], v0 offset:15360\n"
    "v_smfmac_f32_16x16x64_bf16 v[20:23], v[32:35], v[232:239], v1\n"
    "s_waitcnt lgkmcnt(8)\n"
    "s_waitcnt lgkmcnt(5)\n"
    "v_smfmac_f32_16x16x64_bf16 v[20:23], v[36:39], v[48:55], v1\n"
    "ds_read_b128 v[48:51], v2 offset:14336\n"
    "v_smfmac_f32_16x16x64_bf16 v[16:19], v[36:39], v[168:175], v1\n"
    "ds_read_b128 v[52:55], v2 offset:15360\n"
    "v_smfmac_f32_16x16x64_bf16 v[16:19], v[56:59], v[112:119], v1\n"
    "v_smfmac_f32_16x16x64_bf16 v[20:23], v[56:59], v[240:247], v1\n"
    "s_waitcnt lgkmcnt(6)\n"
    "s_waitcnt lgkmcnt(3)\n"
    "v_smfmac_f32_16x16x64_bf16 v[20:23], v[60:63], v[40:47], v1\n"
    "v_smfmac_f32_16x16x64_bf16 v[16:19], v[60:63], v[176:183], v1\n"
    "v_smfmac_f32_16x16x64_bf16 v[16:19], v[24:27], v[120:127], v1\n"
    "v_smfmac_f32_16x16x64_bf16 v[20:23], v[24:27], v[248:255], v1\n"
    "s_waitcnt lgkmcnt(2)\n"
    "s_waitcnt lgkmcnt(0)\n"
    "v_smfmac_f32_16x16x64_bf16 v[20:23], v[28:31], v[48:55], v1\n"
    "v_smfmac_f32_16x16x64_bf16 v[16:19], v[28:31], v[184:191], v1\n"
    "s_branch gemvdone_%=\n"
    "mretry1_%=:\n"
    "s_nop 7\n"
    "s_sleep 3\n"
    "s_add_u32 s94, s94, 1\n"
    "s_cmp_lt_u32 s94, 400000\n"
    "s_cbranch_scc1 mpoll1_%=\n"
    "s_branch fail_%=\n"
    "var2_%=:\n"
    "ds_read_b128 v[40:43], v2 offset:4096\n"
    "ds_read_b128 v[44:47], v2 offset:5120\n"
    "ds_read_b128 v[48:51], v2 offset:0\n"
    "ds_read_b128 v[52:55], v2 offset:1024\n"
    "s_cbranch_scc1 go2_%=\n"
    "s_nop 0\n"
    "mwait2_%=:\n"
    "s_memrealtime s[96:97]\n"
    "s_waitcnt lgkmcnt(0)\n"
    "s_sub_u32 s96, s92, s96\n"
    "s_sub_u32 s96, s96, 1\n"
    "s_cmp_lt_u32 s96, 1000\n"
    "s_cbranch_scc1 mwait2_%=\n"
    "mpoll2_%=:\n"
    "buffer_load_dwordx4 v[56:59], v0, s[64:67], s91 offen sc1\n"
    "buffer_load_dwordx4 v[60:63], v0, s[64:67], s91 offen offset:1024 sc1\n"
    "s_waitcnt vmcnt(1)\n"
    "ds_write_b128 v3, v[56:59]\n"
    "v_max3_u32 v24, v56, v57, v58\n"
    "s_waitcnt vmcnt(0)\n"
    "ds_write_b128 v3, v[60:63] offset:1024\n"
    "v_max3_u32 v24, v24, v59, v60\n"
    "v_max3_u32 v24, v24, v61, v62\n"
    "v_max_u32 v24, v24, v63\n"
    "v_cmp_eq_u32 vcc, -1, v24\n"
    "s_cbranch_vccnz mretry2_%=\n"
    "go2_%=:\n"
    "s_waitcnt lgkmcnt(0)\n"
    "s_barrier\n"
    "ds_read_b128 v[24:27], v0 offset:0\n"
    "ds_read_b128 v[28:31], v0 offset:1024\n"
    "ds_read_b128 v[32:35], v0 offset:2048\n"
    "ds_read_b128 v[36:39], v0 offset:3072\n"
    "v_smfmac_f32_16x16x64_bf16 v[16:19], v[56:59], v[80:87], v1\n"
    "v_smfmac_f32_16x16x64_bf16 v[20:23], v[56:59], v[208:215], v1\n"
    "ds_read_b128 v[56:59], v0 offset:6144\n"
    "v_smfmac_f32_16x16x64_bf16 v[20:23], v[60:63], v[40:47], v1\n"
    "ds_read_b128 v[40:43], v2 offset:2048\n"
    "v_smfmac_f32_16x16x64_bf16 v[16:19], v[60:63], v[144:151], v1\n"
    "ds_read_b128 v[44:47], v2 offset:3072\n"
    "s_waitcnt lgkmcnt(6)\n"
    "v_smfmac_f32_16x16x64_bf16 v[16:19], v[24:27], v[64:71], v1\n"
    "ds_read_b128 v[60:63], v0 offset:7168\n"
    "v_smfmac_f32_16x16x64_bf16 v[20:23], v[24:27], v[192:199], v1\n"
    "ds_read_b128 v[24:27], v0 offset:8192\n"
    "s_waitcnt lgkmcnt(7)\n"
    "v_smfmac_f32_16x16x64_bf16 v[20:23], v[28:31], v[48:55], v1\n"
    "ds_read_b128 v[48:51], v2 offset:6144\n"
    "v_smfmac_f32_16x16x64_bf16 v[16:19], v[28:31], v[128:135], v1\n"
    "ds_read_b128 v[52:55], v2 offset:7168\n"
    "s_waitcnt lgkmcnt(8)\n"
    "v_smfmac_f32_16x16x64_bf16 v[16:19], v[32:35], v[72:79], v1\n"
    "ds_read_b128 v[28:31], v0 offset:9216\n"
    "v_smfmac_f32_16x16x64_bf16 v[20:23], v[32:35], v[200:207], v1\n"
    "ds_read_b128 v[32:35], v0 offset:10240\n"
    "s_waitcnt lgkmcnt(9)\n"
    "s_waitcnt lgkmcnt(6)\n"
    "v_smfmac_f32_16x16x64_bf16 v[20:23], v[36:39], v[40:47], v1\n"
    "ds_read_b128 v[40:43], v2 offset:8192\n"
    "v_smfmac_f32_16x16x64_bf16 v[16:19], v[36:39], v[136:143], v1\n"
    "ds_read_b128 v[44:47], v2 offset:9216\n"
    "v_smfmac_f32_16x16x64_bf16 v[16:19], v[56:59], v[88:95], v1\n"
    "ds_read_b128 v[36:39], v0 offset:11264\n"
    "v_smfmac_f32_16x16x64_bf16 v[20:23], v[56:59], v[216:223], v1\n"
    "ds_read_b128 v[56:59], v0 offset:12288\n"
    "s_waitcnt lgkmcnt(9)\n"
    "s_waitcnt lgkmcnt(6)\n"
    "v_smfmac_f32_16x16x64_bf16 v[20:23], v[60:63], v[48:55], v1\n"
    "ds_read_b128 v[48:51], v2 offset:10240\n"
    "v_smfmac_f32_16x16x64_bf16 v[16:19], v[60:63], v[152:159], v1\n"
    "ds_read_b128 v[52:55], v2 offset:11264\n"
    "v_smfmac_f32_16x16x64_bf16 v[16:19], v[24:27], v[96:103], v1\n"
    "ds_read_b128 v[60:63], v0 offset:13312\n"
    "v_smfmac_f32_16x16x64_bf16 v[20:23], v[24:27], v[224:231], v1\n"
    "ds_read_b128 v[24:27], v0 offset:14336\n"
    "s_waitcnt lgkmcnt(9)\n"
    "s_waitcnt lgkmcnt(6)\n"
    "v_smfmac_f32_16x16x64_bf16 v[20:23], v[28:31], v[40:47], v1\n"
    "ds_read_b128 v[40:43], v2 offset:12288\n"
    "v_smfmac_f32_16x16x64_bf16 v[16:19], v[28:31], v[160:167], v1\n"
    "ds_read_b128 v[44:47], v2 offset:13312\n"
    "v_smfmac_f32_16x16x64_bf16 v[16:19], v[32:35], v[104:111], v1\n"
    "ds_read_b128 v[28:31], v0 offset:15360\n"
    "v_smfmac_f32_16x16x64_bf16 v[20:23], v[32:35], v[232:239], v1\n"
    "s_waitcnt lgkmcnt(8)\n"
    "s_waitcnt lgkmcnt(5)\n"
    "v_smfmac_f32_16x16x64_bf16 v[20:23], v[36:39], v[48:55], v1\n"
    "ds_read_b128 v[48:51], v2 offset:14336\n"
    "v_smfmac_f32_16x16x64_bf16 v[16:19], v[36:39], v[168:175], v1\n"
    "ds_read_b128 v[52:55], v2 offset:15360\n"
    "v_smfmac_f32_16x16x64_bf16 v[16:19], v[56:59], v[112:119], v1\n"
    "v_smfmac_f32_16x16x64_bf16 v[20:23], v[56:59], v[240:247], v1\n"
    "s_waitcnt lgkmcnt(6)\n"
    "s_waitcnt lgkmcnt(3)\n"
    "v_smfmac_f32_16x16x64_bf16 v[20:23], v[60:63], v[40:47], v1\n"
    "v_smfmac_f32_16x16x64_bf16 v[16:19], v[60:63], v[176:183], v1\n"
    "v_smfmac_f32_16x16x64_bf16 v[16:19], v[24:27], v[120:127], v1\n"
    "v_smfmac_f32_16x16x64_bf16 v[20:23], v[24:27], v[248:255], v1\n"
    "s_waitcnt lgkmcnt(2)\n"
    "s_waitcnt lgkmcnt(0)\n"
    "v_smfmac_f32_16x16x64_bf16 v[20:23], v[28:31], v[48:55], v1\n"
    "v_smfmac_f32_16x16x64_bf16 v[16:19], v[28:31], v[184:191], v1\n"
    "s_branch gemvdone_%=\n"
    "mretry2_%=:\n"
    "s_nop 7\n"
    "s_sleep 3\n"
    "s_add_u32 s94, s94, 1\n"
    "s_cmp_lt_u32 s94, 400000\n"
    "s_cbranch_scc1 mpoll2_%=\n"
    "s_branch fail_%=\n"
    "var3_%=:\n"
    "ds_read_b128 v[40:43], v2 offset:6144\n"
    "ds_read_b128 v[44:47], v2 offset:7168\n"
    "ds_read_b128 v[48:51], v2 offset:0\n"
    "ds_read_b128 v[52:55], v2 offset:1024\n"
    "s_cbranch_scc1 go3_%=\n"
    "s_nop 0\n"
    "mwait3_%=:\n"
    "s_memrealtime s[96:97]\n"
    "s_waitcnt lgkmcnt(0)\n"
    "s_sub_u32 s96, s92, s96\n"
    "s_sub_u32 s96, s96, 1\n"
    "s_cmp_lt_u32 s96, 1000\n"
    "s_cbranch_scc1 mwait3_%=\n"
    "mpoll3_%=:\n"
    "buffer_load_dwordx4 v[56:59], v0, s[64:67], s91 offen sc1\n"
    "buffer_load_dwordx4 v[60:63], v0, s[64:67], s91 offen offset:1024 sc1\n"
    "s_waitcnt vmcnt(1)\n"
    "ds_write_b128 v3, v[56:59]\n"
    "v_max3_u32 v24, v56, v57, v58\n"
    "s_waitcnt vmcnt(0)\n"
    "ds_write_b128 v3, v[60:63] offset:1024\n"
    "v_max3_u32 v24, v24, v59, v60\n"
    "v_max3_u32 v24, v24, v61, v62\n"
    "v_max_u32 v24, v24, v63\n"
    "v_cmp_eq_u32 vcc, -1, v24\n"
    "s_cbranch_vccnz mretry3_%=\n"
    "go3_%=:\n"
    "s_waitcnt lgkmcnt(0)\n"
    "s_barrier\n"
    "ds_read_b128 v[24:27], v0 offset:0\n"
    "ds_read_b128 v[28:31], v0 offset:1024\n"
    "ds_read_b128 v[32:35], v0 offset:2048\n"
    "ds_read_b128 v[36:39], v0 offset:3072\n"
    "v_smfmac_f32_16x16x64_bf16 v[16:19], v[56:59], v[88:95], v1\n"
    "v_smfmac_f32_16x16x64_bf16 v[20:23], v[56:59], v[216:223], v1\n"
    "ds_read_b128 v[56:59], v0 offset:4096\n"
    "v_smfmac_f32_16x16x64_bf16 v[20:23], v[60:63], v[40:47], v1\n"
    "ds_read_b128 v[40:43], v2 offset:2048\n"
    "v_smfmac_f32_16x16x64_bf16 v[16:19], v[60:63], v[152:159], v1\n"
    "ds_read_b128 v[44:47], v2 offset:3072\n"
    "s_waitcnt lgkmcnt(6)\n"
    "v_smfmac_f32_16x16x64_bf16 v[16:19], v[24:27], v[64:71], v1\n"
    "ds_read_b128 v[60:63], v0 offset:5120\n"
    "v_smfmac_f32_16x16x64_bf16 v[20:23], v[24:27], v[192:199], v1\n"
    "ds_read_b128 v[24:27], v0 offset:8192\n"
    "s_waitcnt lgkmcnt(7)\n"
    "v_smfmac_f32_16x16x64_bf16 v[20:23], v[28:31], v[48:55], v1\n"
    "ds_read_b128 v[48:51], v2 offset:4096\n"
    "v_smfmac_f32_16x16x64_bf16 v[16:19], v[28:31], v[128:135], v1\n"
    "ds_read_b128 v[52:55], v2 offset:5120\n"
    "s_waitcnt lgkmcnt(8)\n"
    "v_smfmac_f32_16x16x64_bf16 v[16:19], v[32:35], v[72:79], v1\n"
    "ds_read_b128 v[28:31], v0 offset:9216\n"
    "v_smfmac_f32_16x16x64_bf16 v[20:23], v[32:35], v[200:207], v1\n"
    "ds_read_b128 v[32:35], v0 offset:10240\n"
    "s_waitcnt lgkmcnt(9)\n"
    "s_waitcnt lgkmcnt(6)\n"
    "v_smfmac_f32_16x16x64_bf16 v[20:23], v[36:39], v[40:47], v1\n"
    "ds_read_b128 v[40:43], v2 offset:8192\n"
    "v_smfmac_f32_16x16x64_bf16 v[16:19], v[36:39], v[136:143], v1\n"
    "ds_read_b128 v[44:47], v2 offset:9216\n"
    "v_smfmac_f32_16x16x64_bf16 v[16:19], v[56:59], v[80:87], v1\n"
    "ds_read_b128 v[36:39], v0 offset:11264\n"
    "v_smfmac_f32_16x16x64_bf16 v[20:23], v[56:59], v[208:215], v1\n"
    "ds_read_b128 v[56:59], v0 offset:12288\n"
    "s_waitcnt lgkmcnt(9)\n"
    "s_waitcnt lgkmcnt(6)\n"
    "v_smfmac_f32_16x16x64_bf16 v[20:23], v[60:63], v[48:55], v1\n"
    "ds_read_b128 v[48:51], v2 offset:10240\n"
    "v_smfmac_f32_16x16x64_bf16 v[16:19], v[60:63], v[144:151], v1\n"
    "ds_read_b128 v[52:55], v2 offset:11264\n"
    "v_smfmac_f32_16x16x64_bf16 v[16:19], v[24:27], v[96:103], v1\n"
    "ds_read_b128 v[60:63], v0 offset:13312\n"
    "v_smfmac_f32_16x16x64_bf16 v[20:23], v[24:27], v[224:231], v1\n"
    "ds_read_b128 v[24:27], v0 offset:14336\n"
    "s_waitcnt lgkmcnt(9)\n"
    "s_waitcnt lgkmcnt(6)\n"
    "v_smfmac_f32_16x16x64_bf16 v[20:23], v[28:31], v[40:47], v1\n"
    "ds_read_b128 v[40:43], v2 offset:12288\n"
    "v_smfmac_f32_16x16x64_bf16 v[16:19], v[28:31], v[160:167], v1\n"
    "ds_read_b128 v[44:47], v2 offset:13312\n"
    "v_smfmac_f32_16x16x64_bf16 v[16:19], v[32:35], v[104:111], v1\n"
    "ds_read_b128 v[28:31], v0 offset:15360\n"
    "v_smfmac_f32_16x16x64_bf16 v[20:23], v[32:35], v[232:239], v1\n"
    "s_waitcnt lgkmcnt(8)\n"
    "s_waitcnt lgkmcnt(5)\n"
    "v_smfmac_f32_16x16x64_bf16 v[20:23], v[36:39], v[48:55], v1\n"
    "ds_read_b128 v[48:51], v2 offset:14336\n"
    "v_smfmac_f32_16x16x64_bf16 v[16:19], v[36:39], v[168:175], v1\n"
    "ds_read_b128 v[52:55], v2 offset:15360\n"
    "v_smfmac_f32_16x16x64_bf16 v[16:19], v[56:59], v[112:119], v1\n"
    "v_smfmac_f32_16x16x64_bf16 v[20:23], v[56:59], v[240:247], v1\n"
    "s_waitcnt lgkmcnt(6)\n"
    "s_waitcnt lgkmcnt(3)\n"
    "v_smfmac_f32_16x16x64_bf16 v[20:23], v[60:63], v[40:47], v1\n"
    "v_smfmac_f32_16x16x64_bf16 v[16:19], v[60:63], v[176:183], v1\n"
    "v_smfmac_f32_16x16x64_bf16 v[16:19], v[24:27], v[120:127], v1\n"
    "v_smfmac_f32_16x16x64_bf16 v[20:23], v[24:27], v[248:255], v1\n"
    "s_waitcnt lgkmcnt(2)\n"
    "s_waitcnt lgkmcnt(0)\n"
    "v_smfmac_f32_16x16x64_bf16 v[20:23], v[28:31], v[48:55], v1\n"
    "v_smfmac_f32_16x16x64_bf16 v[16:19], v[28:31], v[184:191], v1\n"
    "s_branch gemvdone_%=\n"
    "mretry3_%=:\n"
    "s_nop 7\n"
    "s_sleep 3\n"
    "s_add_u32 s94, s94, 1\n"
    "s_cmp_lt_u32 s94, 400000\n"
    "s_cbranch_scc1 mpoll3_%=\n"
    "s_branch fail_%=\n"
    "var4_%=:\n"
    "ds_read_b128 v[40:43], v2 offset:8192\n"
    "ds_read_b128 v[44:47], v2 offset:9216\n"
    "ds_read_b128 v[48:51], v2 offset:0\n"
    "ds_read_b128 v[52:55], v2 offset:1024\n"
    "s_cbranch_scc1 go4_%=\n"
    "s_nop 0\n"
    "mwait4_%=:\n"
    "s_memrealtime s[96:97]\n"
    "s_waitcnt lgkmcnt(0)\n"
    "s_sub_u32 s96, s92, s96\n"
    "s_sub_u32 s96, s96, 1\n"
    "s_cmp_lt_u32 s96, 1000\n"
    "s_cbranch_scc1 mwait4_%=\n"
    "mpoll4_%=:\n"
    "buffer_load_dwordx4 v[56:59], v0, s[64:67], s91 offen sc1\n"
    "buffer_load_dwordx4 v[60:63], v0, s[64:67], s91 offen offset:1024 sc1\n"
    "s_waitcnt vmcnt(1)\n"
    "ds_write_b128 v3, v[56:59]\n"
    "v_max3_u32 v24, v56, v57, v58\n"
    "s_waitcnt vmcnt(0)\n"
    "ds_write_b128 v3, v[60:63] offset:1024\n"
    "v_max3_u32 v24, v24, v59, v60\n"
    "v_max3_u32 v24, v24, v61, v62\n"
    "v_max_u32 v24, v24, v63\n"
    "v_cmp_eq_u32 vcc, -1, v24\n"
    "s_cbranch_vccnz mretry4_%=\n"
    "go4_%=:\n"
    "s_waitcnt lgkmcnt(0)\n"
    "s_barrier\n"
    "ds_read_b128 v[24:27], v0 offset:0\n"
    "ds_read_b128 v[28:31], v0 offset:1024\n"
    "ds_read_b128 v[32:35], v0 offset:2048\n"
    "ds_read_b128 v[36:39], v0 offset:3072\n"
    "v_smfmac_f32_16x16x64_bf16 v[16:19], v[56:59], v[96:103], v1\n"
    "v_smfmac_f32_16x16x64_bf16 v[20:23], v[56:59], v[224:231], v1\n"
    "ds_read_b128 v[56:59], v0 offset:4096\n"
    "v_smfmac_f32_16x16x64_bf16 v[20:23], v[60:63], v[40:47], v1\n"
    "ds_read_b128 v[40:43], v2 offset:2048\n"
    "v_smfmac_f32_16x16x64_bf16 v[16:19], v[60:63], v[160:167], v1\n"
    "ds_read_b128 v[44:47], v2 offset:3072\n"
    "s_waitcnt lgkmcnt(6)\n"
    "v_smfmac_f32_16x16x64_bf16 v[16:19], v[24:27], v[64:71], v1\n"
    "ds_read_b128 v[60:63], v0 offset:5120\n"
    "v_smfmac_f32_16x16x64_bf16 v[20:23], v[24:27], v[192:199], v1\n"
    "ds_read_b128 v[24:27], v0 offset:6144\n"
    "s_waitcnt lgkmcnt(7)\n"
    "v_smfmac_f32_16x16x64_bf16 v[20:23], v[28:31], v[48:55], v1\n"
    "ds_read_b128 v[48:51], v2 offset:4096\n"
    "v_smfmac_f32_16x16x64_bf16 v[16:19], v[28:31], v[128:135], v1\n"
    "ds_read_b128 v[52:55], v2 offset:5120\n"
    "s_waitcnt lgkmcnt(8)\n"
    "v_smfmac_f32_16x16x64_bf16 v[16:19], v[32:35], v[72:79], v1\n"
    "ds_read_b128 v[28:31], v0 offset:7168\n"
    "v_smfmac_f32_16x16x64_bf16 v[20:23], v[32:35], v[200:207], v1\n"
    "ds_read_b128 v[32:35], v0 offset:10240\n"
    "s_waitcnt lgkmcnt(9)\n"
    "s_waitcnt lgkmcnt(6)\n"
    "v_smfmac_f32_16x16x64_bf16 v[20:23], v[36:39], v[40:47], v1\n"
    "ds_read_b128 v[40:43], v2 offset:6144\n"
    "v_smfmac_f32_16x16x64_bf16 v[16:19], v[36:39], v[136:143], v1\n"
    "ds_read_b128 v[44:47], v2 offset:7168\n"
    "v_smfmac_f32_16x16x64_bf16 v[16:19], v[56:59], v[80:87], v1\n"
    "ds_read_b128 v[36:39], v0 offset:11264\n"
    "v_smfmac_f32_16x16x64_bf16 v[20:23], v[56:59], v[208:215], v1\n"
    "ds_read_b128 v[56:59], v0 offset:12288\n"
    "s_waitcnt lgkmcnt(9)\n"
    "s_waitcnt lgkmcnt(6)\n"
    "v_smfmac_f32_16x16x64_bf16 v[20:23], v[60:63], v[48:55], v1\n"
    "ds_read_b128 v[48:51], v2 offset:10240\n"
    "v_smfmac_f32_16x16x64_bf16 v[16:19], v[60:63], v[144:151], v1\n"
    "ds_read_b128 v[52:55], v2 offset:11264\n"
    "v_smfmac_f32_16x16x64_bf16 v[16:19], v[24:27], v[88:95], v1\n"
    "ds_read_b128 v[60:63], v0 offset:13312\n"
    "v_smfmac_f32_16x16x64_bf16 v[20:23], v[24:27], v[216:223], v1\n"
    "ds_read_b128 v[24:27], v0 offset:14336\n"
    "s_waitcnt lgkmcnt(9)\n"
    "s_waitcnt lgkmcnt(6)\n"
    "v_smfmac_f32_16x16x64_bf16 v[20:23], v[28:31], v[40:47], v1\n"
    "ds_read_b128 v[40:43], v2 offset:12288\n"
    "v_smfmac_f32_16x16x64_bf16 v[16:19], v[28:31], v[152:159], v1\n"
    "ds_read_b128 v[44:47], v2 offset:13312\n"
    "v_smfmac_f32_16x16x64_bf16 v[16:19], v[32:35], v[104:111], v1\n"
    "ds_read_b128 v[28:31], v0 offset:15360\n"
    "v_smfmac_f32_16x16x64_bf16 v[20:23], v[32:35], v[232:239], v1\n"
    "s_waitcnt lgkmcnt(8)\n"
    "s_waitcnt lgkmcnt(5)\n"
    "v_smfmac_f32_16x16x64_bf16 v[20:23], v[36:39], v[48:55], v1\n"
    "ds_read_b128 v[48:51], v2 offset:14336\n"
    "v_smfmac_f32_16x16x64_bf16 v[16:19], v[36:39], v[168:175], v1\n"
    "ds_read_b128 v[52:55], v2 offset:15360\n"
    "v_smfmac_f32_16x16x64_bf16 v[16:19], v[56:59], v[112:119], v1\n"
    "v_smfmac_f32_16x16x64_bf16 v[20:23], v[56:59], v[240:247], v1\n"
    "s_waitcnt lgkmcnt(6)\n"
    "s_waitcnt lgkmcnt(3)\n"
    "v_smfmac_f32_16x16x64_bf16 v[20:23], v[60:63], v[40:47], v1\n"
    "v_smfmac_f32_16x16x64_bf16 v[16:19], v[60:63], v[176:183], v1\n"
    "v_smfmac_f32_16x16x64_bf16 v[16:19], v[24:27], v[120:127], v1\n"
    "v_smfmac_f32_16x16x64_bf16 v[20:23], v[24:27], v[248:255], v1\n"
    "s_waitcnt lgkmcnt(2)\n"
    "s_waitcnt lgkmcnt(0)\n"
    "v_smfmac_f32_16x16x64_bf16 v[20:23], v[28:31], v[48:55], v1\n"
    "v_smfmac_f32_16x16x64_bf16 v[16:19], v[28:31], v[184:191], v1\n"
    "s_branch gemvdone_%=\n"
    "mretry4_%=:\n"
    "s_nop 7\n"
    "s_sleep 3\n"
    "s_add_u32 s94, s94, 1\n"
    "s_cmp_lt_u32 s94, 400000\n"
    "s_cbranch_scc1 mpoll4_%=\n"
    "s_branch fail_%=\n"
    "var5_%=:\n"
    "ds_read_b128 v[40:43], v2 offset:10240\n"
    "ds_read_b128 v[44:47], v2 offset:11264\n"
    "ds_read_b128 v[48:51], v2 offset:0\n"
    "ds_read_b128 v[52:55], v2 offset:1024\n"
    "s_cbranch_scc1 go5_%=\n"
    "s_nop 0\n"
    "mwait5_%=:\n"
    "s_memrealtime s[96:97]\n"
    "s_waitcnt lgkmcnt(0)\n"
    "s_sub_u32 s96, s92, s96\n"
    "s_sub_u32 s96, s96, 1\n"
    "s_cmp_lt_u32 s96, 1000\n"
    "s_cbranch_scc1 mwait5_%=\n"
    "mpoll5_%=:\n"
    "buffer_load_dwordx4 v[56:59], v0, s[64:67], s91 offen sc1\n"
    "buffer_load_dwordx4 v[60:63], v0, s[64:67], s91 offen offset:1024 sc1\n"
    "s_waitcnt vmcnt(1)\n"
    "ds_write_b128 v3, v[56:59]\n"
    "v_max3_u32 v24, v56, v57, v58\n"
    "s_waitcnt vmcnt(0)\n"
    "ds_write_b128 v3, v[60:63] offset:1024\n"
    "v_max3_u32 v24, v24, v59, v60\n"
    "v_max3_u32 v24, v24, v61, v62\n"
    "v_max_u32 v24, v24, v63\n"
    "v_cmp_eq_u32 vcc, -1, v24\n"
    "s_cbranch_vccnz mretry5_%=\n"
    "go5_%=:\n"
    "s_waitcnt lgkmcnt(0)\n"
    "s_barrier\n"
    "ds_read_b128 v[24:27], v0 offset:0\n"
    "ds_read_b128 v[28:31], v0 offset:1024\n"
    "ds_read_b128 v[32:35], v0 offset:2048\n"
    "ds_read_b128 v[36:39], v0 offset:3072\n"
    "v_smfmac_f32_16x16x64_bf16 v[16:19], v[56:59], v[104:111], v1\n"
    "v_smfmac_f32_16x16x64_bf16 v[20:23], v[56:59], v[232:239], v1\n"
    "ds_read_b128 v[56:59], v0 offset:4096\n"
    "v_smfmac_f32_16x16x64_bf16 v[20:23], v[60:63], v[40:47], v1\n"
    "ds_read_b128 v[40:43], v2 offset:2048\n"
    "v_smfmac_f32_16x16x64_bf16 v[16:19], v[60:63], v[168:175], v1\n"
    "ds_read_b128 v[44:47], v2 offset:3072\n"
    "s_waitcnt lgkmcnt(6)\n"
    "v_smfmac_f32_16x16x64_bf16 v[16:19], v[24:27], v[64:71], v1\n"
    "ds_read_b128 v[60:63], v0 offset:5120\n"
    "v_smfmac_f32_16x16x64_bf16 v[20:23], v[24:27], v[192:199], v1\n"
    "ds_read_b128 v[24:27], v0 offset:6144\n"
    "s_waitcnt lgkmcnt(7)\n"
    "v_smfmac_f32_16x16x64_bf16 v[20:23], v[28:31], v[48:55], v1\n"
    "ds_read_b128 v[48:51], v2 offset:4096\n"
    "v_smfmac_f32_16x16x64_bf16 v[16:19], v[28:31], v[128:135], v1\n"
    "ds_read_b128 v[52:55], v2 offset:5120\n"
    "s_waitcnt lgkmcnt(8)\n"
    "v_smfmac_f32_16x16x64_bf16 v[16:19], v[32:35], v[72:79], v1\n"
    "ds_read_b128 v[28:31], v0 offset:7168\n"
    "v_smfmac_f32_16x16x64_bf16 v[20:23], v[32:35], v[200:207], v1\n"
    "ds_read_b128 v[32:35], v0 offset:8192\n"
    "s_waitcnt lgkmcnt(9)\n"
    "s_waitcnt lgkmcnt(6)\n"
    "v_smfmac_f32_16x16x64_bf16 v[20:23], v[36:39], v[40:47], v1\n"
    "ds_read_b128 v[40:43], v2 offset:6144\n"
    "v_smfmac_f32_16x16x64_bf16 v[16:19], v[36:39], v[136:143], v1\n"
    "ds_read_b128 v[44:47], v2 offset:7168\n"
    "v_smfmac_f32_16x16x64_bf16 v[16:19], v[56:59], v[80:87], v1\n"
    "ds_read_b128 v[36:39], v0 offset:9216\n"
    "v_smfmac_f32_16x16x64_bf16 v[20:23], v[56:59], v[208:215], v1\n"
    "ds_read_b128 v[56:59], v0 offset:12288\n"
    "s_waitcnt lgkmcnt(9)\n"
    "s_waitcnt lgkmcnt(6)\n"
    "v_smfmac_f32_16x16x64_bf16 v[20:23], v[60:63], v[48:55], v1\n"
    "ds_read_b128 v[48:51], v2 offset:8192\n"
    "v_smfmac_f32_16x16x64_bf16 v[16:19], v[60:63], v[144:151], v1\n"
    "ds_read_b128 v[52:55], v2 offset:9216\n"
    "v_smfmac_f32_16x16x64_bf16 v[16:19], v[24:27], v[88:95], v1\n"
    "ds_read_b128 v[60:63], v0 offset:13312\n"
    "v_smfmac_f32_16x16x64_bf16 v[20:23], v[24:27], v[216:223], v1\n"
    "ds_read_b128 v[24:27], v0 offset:14336\n"
    "s_waitcnt lgkmcnt(9)\n"
    "s_waitcnt lgkmcnt(6)\n"
    "v_smfmac_f32_16x16x64_bf16 v[20:23], v[28:31], v[40:47], v1\n"
    "ds_read_b128 v[40:43], v2 offset:12288\n"
    "v_smfmac_f32_16x16x64_bf16 v[16:19], v[28:31], v[152:159], v1\n"
    "ds_read_b128 v[44:47], v2 offset:13312\n"
    "v_smfmac_f32_16x16x64_bf16 v[16:19], v[32:35], v[96:103], v1\n"
    "ds_read_b128 v[28:31], v0 offset:15360\n"
    "v_smfmac_f32_16x16x64_bf16 v[20:23], v[32:35], v[224:231], v1\n"
    "s_waitcnt lgkmcnt(8)\n"
    "s_waitcnt lgkmcnt(5)\n"
    "v_smfmac_f32_16x16x64_bf16 v[20:23], v[36:39], v[48:55], v1\n"
    "ds_read_b128 v[48:51], v2 offset:14336\n"
    "v_smfmac_f32_16x16x64_bf16 v[16:19], v[36:39], v[160:167], v1\n"
    "ds_read_b128 v[52:55], v2 offset:15360\n"
    "v_smfmac_f32_16x16x64_bf16 v[16:19], v[56:59], v[112:119], v1\n"
    "v_smfmac_f32_16x16x64_bf16 v[20:23], v[56:59], v[240:247], v1\n"
    "s_waitcnt lgkmcnt(6)\n"
    "s_waitcnt lgkmcnt(3)\n"
    "v_smfmac_f32_16x16x64_bf16 v[20:23], v[60:63], v[40:47], v1\n"
    "v_smfmac_f32_16x16x64_bf16 v[16:19], v[60:63], v[176:183], v1\n"
    "v_smfmac_f32_16x16x64_bf16 v[16:19], v[24:27], v[120:127], v1\n"
    "v_smfmac_f32_16x16x64_bf16 v[20:23], v[24:27], v[248:255], v1\n"
    "s_waitcnt lgkmcnt(2)\n"
    "s_waitcnt lgkmcnt(0)\n"
    "v_smfmac_f32_16x16x64_bf16 v[20:23], v[28:31], v[48:55], v1\n"
    "v_smfmac_f32_16x16x64_bf16 v[16:19], v[28:31], v[184:191], v1\n"
    "s_branch gemvdone_%=\n"
    "mretry5_%=:\n"
    "s_nop 7\n"
    "s_sleep 3\n"
    "s_add_u32 s94, s94, 1\n"
    "s_cmp_lt_u32 s94, 400000\n"
    "s_cbranch_scc1 mpoll5_%=\n"
    "s_branch fail_%=\n"
    "var6_%=:\n"
    "ds_read_b128 v[40:43], v2 offset:12288\n"
    "ds_read_b128 v[44:47], v2 offset:13312\n"
    "ds_read_b128 v[48:51], v2 offset:0\n"
    "ds_read_b128 v[52:55], v2 offset:1024\n"
    "s_cbranch_scc1 go6_%=\n"
    "s_nop 0\n"
    "mwait6_%=:\n"
    "s_memrealtime s[96:97]\n"
    "s_waitcnt lgkmcnt(0)\n"
    "s_sub_u32 s96, s92, s96\n"
    "s_sub_u32 s96, s96, 1\n"
    "s_cmp_lt_u32 s96, 1000\n"
    "s_cbranch_scc1 mwait6_%=\n"
    "mpoll6_%=:\n"
    "buffer_load_dwordx4 v[56:59], v0, s[64:67], s91 offen sc1\n"
    "buffer_load_dwordx4 v[60:63], v0, s[64:67], s91 offen offset:1024 sc1\n"
    "s_waitcnt vmcnt(1)\n"
    "ds_write_b128 v3, v[56:59]\n"
    "v_max3_u32 v24, v56, v57, v58\n"
    "s_waitcnt vmcnt(0)\n"
    "ds_write_b128 v3, v[60:63] offset:1024\n"
    "v_max3_u32 v24, v24, v59, v60\n"
    "v_max3_u32 v24, v24, v61, v62\n"
    "v_max_u32 v24, v24, v63\n"
    "v_cmp_eq_u32 vcc, -1, v24\n"
    "s_cbranch_vccnz mretry6_%=\n"
    "go6_%=:\n"
    "s_waitcnt lgkmcnt(0)\n"
    "s_barrier\n"
    "ds_read_b128 v[24:27], v0 offset:0\n"
    "ds_read_b128 v[28:31], v0 offset:1024\n"
    "ds_read_b128 v[32:35], v0 offset:2048\n"
    "ds_read_b128 v[36:39], v0 offset:3072\n"
    "v_smfmac_f32_16x16x64_bf16 v[16:19], v[56:59], v[112:119], v1\n"
    "v_smfmac_f32_16x16x64_bf16 v[20:23], v[56:59], v[240:247], v1\n"
    "ds_read_b128 v[56:59], v0 offset:4096\n"
    "v_smfmac_f32_16x16x64_bf16 v[20:23], v[60:63], v[40:47], v1\n"
    "ds_read_b128 v[40:43], v2 offset:2048\n"
    "v_smfmac_f32_16x16x64_bf16 v[16:19], v[60:63], v[176:183], v1\n"
    "ds_read_b128 v[44:47], v2 offset:3072\n"
    "s_waitcnt lgkmcnt(6)\n"
    "v_smfmac_f32_16x16x64_bf16 v[16:19], v[24:27], v[64:71], v1\n"
    "ds_read_b128 v[60:63], v0 offset:5120\n"
    "v_smfmac_f32_16x16x64_bf16 v[20:23], v[24:27], v[192:199], v1\n"
    "ds_read_b128 v[24:27], v0 offset:6144\n"
    "s_waitcnt lgkmcnt(7)\n"
    "v_smfmac_f32_16x16x64_bf16 v[20:23], v[28:31], v[48:55], v1\n"
    "ds_read_b128 v[48:51], v2 offset:4096\n"
    "v_smfmac_f32_16x16x64_bf16 v[16:19], v[28:31], v[128:135], v1\n"
    "ds_read_b128 v[52:55], v2 offset:5120\n"
    "s_waitcnt lgkmcnt(8)\n"
    "v_smfmac_f32_16x16x64_bf16 v[16:19], v[32:35], v[72:79], v1\n"
    "ds_read_b128 v[28:31], v0 offset:7168\n"
    "v_smfmac_f32_16x16x64_bf16 v[20:23], v[32:35], v[200:207], v1\n"
    "ds_read_b128 v[32:35], v0 offset:8192\n"
    "s_waitcnt lgkmcnt(9)\n"
    "s_waitcnt lgkmcnt(6)\n"
    "v_smfmac_f32_16x16x64_bf16 v[20:23], v[36:39], v[40:47], v1\n"
    "ds_read_b128 v[40:43], v2 offset:6144\n"
    "v_smfmac_f32_16x16x64_bf16 v[16:19], v[36:39], v[136:143], v1\n"
    "ds_read_b128 v[44:47], v2 offset:7168\n"
    "v_smfmac_f32_16x16x64_bf16 v[16:19], v[56:59], v[80:87], v1\n"
    "ds_read_b128 v[36:39], v0 offset:9216\n"
    "v_smfmac_f32_16x16x64_bf16 v[20:23], v[56:59], v[208:215], v1\n"
    "ds_read_b128 v[56:59], v0 offset:10240\n"
    "s_waitcnt lgkmcnt(9)\n"
    "s_waitcnt lgkmcnt(6)\n"
    "v_smfmac_f32_16x16x64_bf16 v[20:23], v[60:63], v[48:55], v1\n"
    "ds_read_b128 v[48:51], v2 offset:8192\n"
    "v_smfmac_f32_16x16x64_bf16 v[16:19], v[60:63], v[144:151], v1\n"
    "ds_read_b128 v[52:55], v2 offset:9216\n"
    "v_smfmac_f32_16x16x64_bf16 v[16:19], v[24:27], v[88:95], v1\n"
    "ds_read_b128 v[60:63], v0 offset:11264\n"
    "v_smfmac_f32_16x16x64_bf16 v[20:23], v[24:27], v[216:223], v1\n"
    "ds_read_b128 v[24:27], v0 offset:14336\n"
    "s_waitcnt lgkmcnt(9)\n"
    "s_waitcnt lgkmcnt(6)\n"
    "v_smfmac_f32_16x16x64_bf16 v[20:23], v[28:31], v[40:47], v1\n"
    "ds_read_b128 v[40:43], v2 offset:10240\n"
    "v_smfmac_f32_16x16x64_bf16 v[16:19], v[28:31], v[152:159], v1\n"
    "ds_read_b128 v[44:47], v2 offset:11264\n"
    "v_smfmac_f32_16x16x64_bf16 v[16:19], v[32:35], v[96:103], v1\n"
    "ds_read_b128 v[28:31], v0 offset:15360\n"
    "v_smfmac_f32_16x16x64_bf16 v[20:23], v[32:35], v[224:231], v1\n"
    "s_waitcnt lgkmcnt(8)\n"
    "s_waitcnt lgkmcnt(5)\n"
    "v_smfmac_f32_16x16x64_bf16 v[20:23], v[36:39], v[48:55], v1\n"
    "ds_read_b128 v[48:51], v2 offset:14336\n"
    "v_smfmac_f32_16x16x64_bf16 v[16:19], v[36:39], v[160:167], v1\n"
    "ds_read_b128 v[52:55], v2 offset:15360\n"
    "v_smfmac_f32_16x16x64_bf16 v[16:19], v[56:59], v[104:111], v1\n"
    "v_smfmac_f32_16x16x64_bf16 v[20:23], v[56:59], v[232:239], v1\n"
    "s_waitcnt lgkmcnt(6)\n"
    "s_waitcnt lgkmcnt(3)\n"
    "v_smfmac_f32_16x16x64_bf16 v[20:23], v[60:63], v[40:47], v1\n"
    "v_smfmac_f32_16x16x64_bf16 v[16:19], v[60:63], v[168:175], v1\n"
    "v_smfmac_f32_16x16x64_bf16 v[16:19], v[24:27], v[120:127], v1\n"
    "v_smfmac_f32_16x16x64_bf16 v[20:23], v[24:27], v[248:255], v1\n"
    "s_waitcnt lgkmcnt(2)\n"
    "s_waitcnt lgkmcnt(0)\n"
    "v_smfmac_f32_16x16x64_bf16 v[20:23], v[28:31], v[48:55], v1\n"
    "v_smfmac_f32_16x16x64_bf16 v[16:19], v[28:31], v[184:191], v1\n"
    "s_branch gemvdone_%=\n"
    "mretry6_%=:\n"
    "s_nop 7\n"
    "s_sleep 3\n"
    "s_add_u32 s94, s94, 1\n"
    "s_cmp_lt_u32 s94, 400000\n"
    "s_cbranch_scc1 mpoll6_%=\n"
    "s_branch fail_%=\n"
    "var7_%=:\n"
    "ds_read_b128 v[40:43], v2 offset:14336\n"
    "ds_read_b128 v[44:47], v2 offset:15360\n"
    "ds_read_b128 v[48:51], v2 offset:0\n"
    "ds_read_b128 v[52:55], v2 offset:1024\n"
    "s_cbranch_scc1 go7_%=\n"
    "s_nop 0\n"
    "mwait7_%=:\n"
    "s_memrealtime s[96:97]\n"
    "s_waitcnt lgkmcnt(0)\n"
    "s_sub_u32 s96, s92, s96\n"
    "s_sub_u32 s96, s96, 1\n"
    "s_cmp_lt_u32 s96, 1000\n"
    "s_cbranch_scc1 mwait7_%=\n"
    "mpoll7_%=:\n"
    "buffer_load_dwordx4 v[56:59], v0, s[64:67], s91 offen sc1\n"
    "buffer_load_dwordx4 v[60:63], v0, s[64:67], s91 offen offset:1024 sc1\n"
    "buffer_load_dwordx2 v[26:27], off, s[64:67], s90 sc1\n"
    "v_mov_b32 v25, 29312\n"
    "s_waitcnt vmcnt(2)\n"
    "ds_write_b128 v3, v[56:59]\n"
    "v_max3_u32 v24, v56, v57, v58\n"
    "s_waitcnt vmcnt(1)\n"
    "ds_write_b128 v3, v[60:63] offset:1024\n"
    "v_max3_u32 v24, v24, v59, v60\n"
    "v_max3_u32 v24, v24, v61, v62\n"
    "v_max_u32 v24, v24, v63\n"
    "s_waitcnt vmcnt(0)\n"
    "ds_write_b64 v25, v[26:27]\n"
    "v_max3_u32 v24, v24, v26, v27\n"
    "v_cmp_eq_u32 vcc, -1, v24\n"
    "s_cbranch_vccnz mretry7_%=\n"
    "go7_%=:\n"
    "s_waitcnt lgkmcnt(0)\n"
    "s_barrier\n"
    "ds_read_b128 v[24:27], v0 offset:0\n"
    "ds_read_b128 v[28:31], v0 offset:1024\n"
    "ds_read_b128 v[32:35], v0 offset:2048\n"
    "ds_read_b128 v[36:39], v0 offset:3072\n"
    "v_smfmac_f32_16x16x64_bf16 v[16:19], v[56:59], v[120:127], v1\n"
    "v_smfmac_f32_16x16x64_bf16 v[20:23], v[56:59], v[248:255], v1\n"
    "ds_read_b128 v[56:59], v0 offset:4096\n"
    "v_smfmac_f32_16x16x64_bf16 v[20:23], v[60:63], v[40:47], v1\n"
    "ds_read_b128 v[40:43], v2 offset:2048\n"
    "v_smfmac_f32_16x16x64_bf16 v[16:19], v[60:63], v[184:191], v1\n"
    "ds_read_b128 v[44:47], v2 offset:3072\n"
    "s_waitcnt lgkmcnt(6)\n"
    "v_smfmac_f32_16x16x64_bf16 v[16:19], v[24:27], v[64:71], v1\n"
    "ds_read_b128 v[60:63], v0 offset:5120\n"
    "v_smfmac_f32_16x16x64_bf16 v[20:23], v[24:27], v[192:199], v1\n"
    "ds_read_b128 v[24:27], v0 offset:6144\n"
    "s_waitcnt lgkmcnt(7)\n"
    "v_smfmac_f32_16x16x64_bf16 v[20:23], v[28:31], v[48:55], v1\n"
    "ds_read_b128 v[48:51], v2 offset:4096\n"
    "v_smfmac_f32_16x16x64_bf16 v[16:19], v[28:31], v[128:135], v1\n"
    "ds_read_b128 v[52:55], v2 offset:5120\n"
    "s_waitcnt lgkmcnt(8)\n"
    "v_smfmac_f32_16x16x64_bf16 v[16:19], v[32:35], v[72:79], v1\n"
    "ds_read_b128 v[28:31], v0 offset:7168\n"
    "v_smfmac_f32_16x16x64_bf16 v[20:23], v[32:35], v[200:207], v1\n"
    "ds_read_b128 v[32:35], v0 offset:8192\n"
    "s_waitcnt lgkmcnt(9)\n"
    "s_waitcnt lgkmcnt(6)\n"
    "v_smfmac_f32_16x16x64_bf16 v[20:23], v[36:39], v[40:47], v1\n"
    "ds_read_b128 v[40:43], v2 offset:6144\n"
    "v_smfmac_f32_16x16x64_bf16 v[16:19], v[36:39], v[136:143], v1\n"
    "ds_read_b128 v[44:47], v2 offset:7168\n"
    "v_smfmac_f32_16x16x64_bf16 v[16:19], v[56:59], v[80:87], v1\n"
    "ds_read_b128 v[36:39], v0 offset:9216\n"
    "v_smfmac_f32_16x16x64_bf16 v[20:23], v[56:59], v[208:215], v1\n"
    "ds_read_b128 v[56:59], v0 offset:10240\n"
    "s_waitcnt lgkmcnt(9)\n"
    "s_waitcnt lgkmcnt(6)\n"
    "v_smfmac_f32_16x16x64_bf16 v[20:23], v[60:63], v[48:55], v1\n"
    "ds_read_b128 v[48:51], v2 offset:8192\n"
    "v_smfmac_f32_16x16x64_bf16 v[16:19], v[60:63], v[144:151], v1\n"
    "ds_read_b128 v[52:55], v2 offset:9216\n"
    "v_smfmac_f32_16x16x64_bf16 v[16:19], v[24:27], v[88:95], v1\n"
    "ds_read_b128 v[60:63], v0 offset:11264\n"
    "v_smfmac_f32_16x16x64_bf16 v[20:23], v[24:27], v[216:223], v1\n"
    "ds_read_b128 v[24:27], v0 offset:12288\n"
    "s_waitcnt lgkmcnt(9)\n"
    "s_waitcnt lgkmcnt(6)\n"
    "v_smfmac_f32_16x16x64_bf16 v[20:23], v[28:31], v[40:47], v1\n"
    "ds_read_b128 v[40:43], v2 offset:10240\n"
    "v_smfmac_f32_16x16x64_bf16 v[16:19], v[28:31], v[152:159], v1\n"
    "ds_read_b128 v[44:47], v2 offset:11264\n"
    "v_smfmac_f32_16x16x64_bf16 v[16:19], v[32:35], v[96:103], v1\n"
    "ds_read_b128 v[28:31], v0 offset:13312\n"
    "v_smfmac_f32_16x16x64_bf16 v[20:23], v[32:35], v[224:231], v1\n"
    "s_waitcnt lgkmcnt(8)\n"
    "s_waitcnt lgkmcnt(5)\n"
    "v_smfmac_f32_16x16x64_bf16 v[20:23], v[36:39], v[48:55], v1\n"
    "ds_read_b128 v[48:51], v2 offset:12288\n"
    "v_smfmac_f32_16x16x64_bf16 v[16:19], v[36:39], v[160:167], v1\n"
    "ds_read_b128 v[52:55], v2 offset:13312\n"
    "v_smfmac_f32_16x16x64_bf16 v[16:19], v[56:59], v[104:111], v1\n"
    "v_smfmac_f32_16x16x64_bf16 v[20:23], v[56:59], v[232:239], v1\n"
    "s_waitcnt lgkmcnt(6)\n"
    "s_waitcnt lgkmcnt(3)\n"
    "v_smfmac_f32_16x16x64_bf16 v[20:23], v[60:63], v[40:47], v1\n"
    "v_smfmac_f32_16x16x64_bf16 v[16:19], v[60:63], v[168:175], v1\n"
    "v_smfmac_f32_16x16x64_bf16 v[16:19], v[24:27], v[112:119], v1\n"
    "v_smfmac_f32_16x16x64_bf16 v[20:23], v[24:27], v[240:247], v1\n"
    "s_waitcnt lgkmcnt(2)\n"
    "s_waitcnt lgkmcnt(0)\n"
    "v_smfmac_f32_16x16x64_bf16 v[20:23], v[28:31], v[48:55], v1\n"
    "v_smfmac_f32_16x16x64_bf16 v[16:19], v[28:31], v[176:183], v1\n"
    "s_branch gemvdone_%=\n"
    "mretry7_%=:\n"
    "s_nop 7\n"
    "s_sleep 3\n"
    "s_add_u32 s94, s94, 1\n"
    "s_cmp_lt_u32 s94, 400000\n"
    "s_cbranch_scc1 mpoll7_%=\n"
    "s_branch fail_%=\n"
    "gemvdone_%=:\n"
    "s_nop 6\n"
    "v_permlane32_swap_b32 v16, v20\n"
    "v_permlane32_swap_b32 v17, v21\n"
    "v_permlane32_swap_b32 v18, v22\n"
    "v_permlane32_swap_b32 v19, v23\n"
    "v_pk_add_f32 v[16:17], v[16:17], v[20:21]\n"
    "v_pk_add_f32 v[18:19], v[18:19], v[22:23]\n"
    "v_cvt_pk_bf16_f32 v16, v16, v17\n"
    "v_cvt_pk_bf16_f32 v17, v18, v19\n"
    "ds_write_b64 v10, v[16:17]\n"
    "s_cmp_lg_u32 s68, 0\n"
    "s_cbranch_scc1 notw0_%=\n"
    "ds_read_b128 v[44:47], v0 offset:16384\n"
    "ds_read_b128 v[48:51], v0 offset:20480\n"
    "ds_read_b128 v[52:55], v0 offset:17408\n"
    "ds_read_b128 v[56:59], v0 offset:21504\n"
    "ds_read_b128 v[60:63], v0 offset:18432\n"
    "v_lshrrev_b32 v15, 1, v0\n"
    "s_waitcnt lgkmcnt(0)\n"
    "s_barrier\n"
    "ds_read_b64_tr_b16 v[28:29], v15 offset:24576\n"
    "ds_read_b64_tr_b16 v[30:31], v15 offset:25088\n"
    "ds_read_b64_tr_b16 v[32:33], v15 offset:25600\n"
    "ds_read_b64_tr_b16 v[34:35], v15 offset:26112\n"
    "ds_read_b64_tr_b16 v[36:37], v15 offset:26624\n"
    "ds_read_b64_tr_b16 v[38:39], v15 offset:27136\n"
    "ds_read_b64_tr_b16 v[40:41], v15 offset:27648\n"
    "ds_read_b64_tr_b16 v[42:43], v15 offset:28160\n"
    "s_waitcnt lgkmcnt(6)\n"
    "v_mfma_f32_16x16x32_bf16 v[16:19], v[44:47], v[28:31], 0\n"
    "v_mfma_f32_16x16x32_bf16 v[20:23], v[48:51], v[28:31], 0\n"
    "ds_read_b128 v[44:47], v0 offset:22528\n"
    "ds_read_b128 v[48:51], v0 offset:19456\n"
    "buffer_load_dwordx2 v[8:9], v11, s[64:67], s83 offen sc1\n"
    "v_mfma_f32_16x16x32_bf16 v[24:27], v[28:31], v[28:31], 0\n"
    "s_waitcnt lgkmcnt(6)\n"
    "v_mfma_f32_16x16x32_bf16 v[16:19], v[52:55], v[32:35], v[16:19]\n"
    "ds_read_b128 v[52:55], v0 offset:23552\n"
    "v_mfma_f32_16x16x32_bf16 v[20:23], v[56:59], v[32:35], v[20:23]\n"
    "v_mfma_f32_16x16x32_bf16 v[24:27], v[32:35], v[32:35], v[24:27]\n"
    "s_waitcnt lgkmcnt(5)\n"
    "v_mfma_f32_16x16x32_bf16 v[24:27], v[36:39], v[36:39], v[24:27]\n"
    "v_mfma_f32_16x16x32_bf16 v[16:19], v[60:63], v[36:39], v[16:19]\n"
    "s_waitcnt lgkmcnt(3)\n"
    "v_mfma_f32_16x16x32_bf16 v[24:27], v[40:43], v[40:43], v[24:27]\n"
    "s_waitcnt lgkmcnt(2)\n"
    "v_mfma_f32_16x16x32_bf16 v[20:23], v[44:47], v[36:39], v[20:23]\n"
    "s_waitcnt lgkmcnt(1)\n"
    "v_mfma_f32_16x16x32_bf16 v[16:19], v[48:51], v[40:43], v[16:19]\n"
    "s_waitcnt lgkmcnt(0)\n"
    "v_mfma_f32_16x16x32_bf16 v[20:23], v[52:55], v[40:43], v[20:23]\n"
    "v_bfe_u32 v58, v0, 7, 1\n"
    "v_lshl_add_u32 v56, v58, 3, v12\n"
    "v_lshl_add_u32 v57, v58, 1, v13\n"
    "v_lshrrev_b32 v58, 9, v0\n"
    "v_lshl_add_u32 v57, v58, 2, v57\n"
    "v_mov_b32_dpp v24, v25 row_ror:8 row_mask:0xf bank_mask:0xc\n"
    "v_mov_b32_dpp v26, v27 row_ror:8 row_mask:0xf bank_mask:0xc\n"
    "s_nop 1\n"
    "v_permlane32_swap_b32 v24, v26\n"
    "v_mov_b32_dpp v16, v20 row_ror:8 row_mask:0xf bank_mask:0xc\n"
    "v_mov_b32_dpp v17, v21 row_ror:8 row_mask:0xf bank_mask:0xc\n"
    "v_mov_b32_dpp v18, v22 row_ror:8 row_mask:0xf bank_mask:0xc\n"
    "v_mov_b32_dpp v19, v23 row_ror:8 row_mask:0xf bank_mask:0xc\n"
    "v_max3_f32 v20, v26, v16, v17\n"
    "v_max3_f32 v20, v20, v18, v19\n"
    "s_nop 1\n"
    "v_max_f32_dpp v20, v20, v20 row_ror:8 row_mask:0xf bank_mask:0xf\n"
    "v_mov_b32 v21, v20\n"
    "s_nop 1\n"
    "v_permlane16_swap_b32 v20, v21\n"
    "v_max_f32 v20, v20, v21\n"
    "v_mov_b32 v21, v20\n"
    "s_nop 1\n"
    "v_permlane32_swap_b32 v20, v21\n"
    "v_max_f32 v20, v20, v21\n"
    "v_mul_f32 v20, s86, v20\n"
    "v_fma_f32 v16, v16, s86, -v20\n"
    "v_fma_f32 v17, v17, s86, -v20\n"
    "v_fma_f32 v18, v18, s86, -v20\n"
    "v_fma_f32 v19, v19, s86, -v20\n"
    "v_fma_f32 v26, v26, s86, -v20\n"
    "v_exp_f32 v16, v16\n"
    "v_exp_f32 v17, v17\n"
    "v_exp_f32 v18, v18\n"
    "v_exp_f32 v19, v19\n"
    "v_exp_f32 v26, v26\n"
    "v_add_f32 v20, v16, v17\n"
    "v_add_f32 v21, v18, v19\n"
    "v_add_f32 v20, v20, v21\n"
    "v_add_f32 v20, v20, v26\n"
    "s_nop 1\n"
    "v_add_f32_dpp v20, v20, v20 row_ror:8 row_mask:0xf bank_mask:0xf\n"
    "v_mov_b32 v21, v20\n"
    "s_nop 1\n"
    "v_permlane16_swap_b32 v20, v21\n"
    "v_add_f32 v20, v20, v21\n"
    "v_mov_b32 v21, v20\n"
    "s_nop 1\n"
    "v_permlane32_swap_b32 v20, v21\n"
    "v_add_f32 v20, v20, v21\n"
    "v_rcp_f32 v20, v20\n"
    "s_nop 0\n"
    "v_mul_f32 v16, v16, v20\n"
    "v_mul_f32 v17, v17, v20\n"
    "v_mul_f32 v18, v18, v20\n"
    "v_mul_f32 v19, v19, v20\n"
    "v_mul_f32 v26, v26, v20\n"
    "v_cvt_pk_bf16_f32 v16, v16, v17\n"
    "v_cvt_pk_bf16_f32 v17, v18, v19\n"
    "v_cvt_pk_bf16_f32 v26, v26, v26\n"
    "ds_write_b64 v56, v[16:17]\n"
    "ds_write_b16 v57, v26\n"
    "vcheck_%=:\n"
    "s_waitcnt vmcnt(0)\n"
    "vcheck2_%=:\n"
    "v_max_u32 v20, v8, v9\n"
    "v_cmp_eq_u32 vcc, -1, v20\n"
    "s_cbranch_vccnz vretry_%=\n"
    "v_cndmask_b32 v8, v8, 0, s[78:79]\n"
    "v_cndmask_b32 v9, v9, 0, s[78:79]\n"
    "s_waitcnt lgkmcnt(0)\n"
    "s_barrier\n"
    "ds_read_b128 v[16:19], v12\n"
    "ds_read_b64 v[20:21], v13\n"
    "s_waitcnt lgkmcnt(1)\n"
    "v_mfma_f32_16x16x32_bf16 v[24:27], v[4:7], v[16:19], 0\n"
    "s_waitcnt lgkmcnt(0)\n"
    "v_mfma_f32_16x16x16_bf16 v[28:31], v[8:9], v[20:21], 0\n"
    "s_nop 7\n"
    "v_pk_add_f32 v[24:25], v[24:25], v[28:29]\n"
    "v_pk_add_f32 v[26:27], v[26:27], v[30:31]\n"
    "v_cvt_pk_bf16_f32 v24, v24, v25\n"
    "v_cvt_pk_bf16_f32 v25, v26, v27\n"
    "v_min_u32 v24, -2, v24\n"
    "v_min_u32 v25, -2, v25\n"
    "buffer_store_dwordx2 v[24:25], v14, s[64:67], s84 offen sc1\n"
    "buffer_store_dwordx2 v[24:25], v14, s[64:67], s85 offen sc1\n"
    "s_sub_u32 s76, s68, 1\n"
    "v_mov_b32 v16, 29312\n"
    "ds_read_b64 v[16:17], v16\n"
    "s_waitcnt lgkmcnt(0)\n"
    "v_readfirstlane_b32 s92, v17\n"
    "s_cmp_lt_u32 s76, 2\n"
    "s_nop 3\n"
    "s_cbranch_scc0 notok_%=\n"
    "v_lshrrev_b32 v15, 1, v0\n"
    "ds_read_b128 v[28:31], v0 offset:16384\n"
    "ds_read_b128 v[32:35], v0 offset:20480\n"
    "ds_read_b64_tr_b16 v[36:37], v15 offset:24576\n"
    "ds_read_b64_tr_b16 v[38:39], v15 offset:25088\n"
    "ds_read_b128 v[40:43], v0 offset:17408\n"
    "ds_read_b128 v[44:47], v0 offset:21504\n"
    "s_waitcnt vmcnt(2)\n"
    "s_waitcnt lgkmcnt(5)\n"
    "v_mfma_f32_16x16x32_bf16 v[16:19], v[28:31], v[48:51], 0\n"
    "ds_read_b64_tr_b16 v[28:29], v15 offset:25600\n"
    "ds_read_b64_tr_b16 v[30:31], v15 offset:26112\n"
    "s_waitcnt lgkmcnt(6)\n"
    "v_mfma_f32_16x16x32_bf16 v[20:23], v[32:35], v[48:51], 0\n"
    "ds_read_b128 v[32:35], v0 offset:18432\n"
    "s_waitcnt lgkmcnt(5)\n"
    "v_mfma_f32_16x16x32_bf16 v[24:27], v[36:39], v[48:51], 0\n"
    "ds_read_b128 v[36:39], v0 offset:22528\n"
    "s_waitcnt lgkmcnt(5)\n"
    "v_mfma_f32_16x16x32_bf16 v[16:19], v[40:43], v[52:55], v[16:19]\n"
    "ds_read_b64_tr_b16 v[40:41], v15 offset:26624\n"
    "ds_read_b64_tr_b16 v[42:43], v15 offset:27136\n"
    "s_waitcnt lgkmcnt(6)\n"
    "v_mfma_f32_16x16x32_bf16 v[20:23], v[44:47], v[52:55], v[20:23]\n"
    "ds_read_b128 v[44:47], v0 offset:19456\n"
    "s_waitcnt lgkmcnt(5)\n"
    "v_mfma_f32_16x16x32_bf16 v[24:27], v[28:31], v[52:55], v[24:27]\n"
    "ds_read_b128 v[28:31], v0 offset:23552\n"
    "s_waitcnt lgkmcnt(5)\n"
    "v_mfma_f32_16x16x32_bf16 v[16:19], v[32:35], v[56:59], v[16:19]\n"
    "ds_read_b64_tr_b16 v[32:33], v15 offset:27648\n"
    "ds_read_b64_tr_b16 v[34:35], v15 offset:28160\n"
    "s_waitcnt lgkmcnt(6)\n"
    "v_mfma_f32_16x16x32_bf16 v[20:23], v[36:39], v[56:59], v[20:23]\n"
    "s_waitcnt lgkmcnt(4)\n"
    "v_mfma_f32_16x16x32_bf16 v[24:27], v[40:43], v[56:59], v[24:27]\n"
    "s_waitcnt lgkmcnt(3)\n"
    "v_mfma_f32_16x16x32_bf16 v[16:19], v[44:47], v[60:63], v[16:19]\n"
    "s_waitcnt lgkmcnt(2)\n"
    "v_mfma_f32_16x16x32_bf16 v[20:23], v[28:31], v[60:63], v[20:23]\n"
    "s_waitcnt lgkmcnt(0)\n"
    "v_mfma_f32_16x16x32_bf16 v[24:27], v[32:35], v[60:63], v[24:27]\n"
    "s_nop 3\n"
    "v_cvt_pk_bf16_f32 v28, v16, v17\n"
    "v_lshlrev_b32 v16, 16, v28\n"
    "v_and_b32 v17, 0xffff0000, v28\n"
    "v_cvt_pk_bf16_f32 v28, v18, v19\n"
    "v_lshlrev_b32 v18, 16, v28\n"
    "v_and_b32 v19, 0xffff0000, v28\n"
    "v_cvt_pk_bf16_f32 v28, v20, v21\n"
    "v_lshlrev_b32 v20, 16, v28\n"
    "v_and_b32 v21, 0xffff0000, v28\n"
    "v_cvt_pk_bf16_f32 v28, v22, v23\n"
    "v_lshlrev_b32 v22, 16, v28\n"
    "v_and_b32 v23, 0xffff0000, v28\n"
    "v_cvt_pk_bf16_f32 v28, v24, v25\n"
    "v_lshlrev_b32 v24, 16, v28\n"
    "v_and_b32 v25, 0xffff0000, v28\n"
    "v_cvt_pk_bf16_f32 v28, v26, v27\n"
    "v_lshlrev_b32 v26, 16, v28\n"
    "v_and_b32 v27, 0xffff0000, v28\n"
    "v_mul_f32 v16, s87, v16\n"
    "v_mul_f32 v17, s87, v17\n"
    "v_mul_f32 v18, s87, v18\n"
    "v_mul_f32 v19, s87, v19\n"
    "v_mul_f32 v20, s87, v20\n"
    "v_mul_f32 v21, s87, v21\n"
    "v_mul_f32 v22, s87, v22\n"
    "v_mul_f32 v23, s87, v23\n"
    "v_mul_f32 v24, s87, v24\n"
    "v_mul_f32 v25, s87, v25\n"
    "v_mul_f32 v26, s87, v26\n"
    "v_mul_f32 v27, s87, v27\n"
    "v_cvt_pk_bf16_f32 v28, v16, v17\n"
    "v_lshlrev_b32 v16, 16, v28\n"
    "v_and_b32 v17, 0xffff0000, v28\n"
    "v_cvt_pk_bf16_f32 v28, v18, v19\n"
    "v_lshlrev_b32 v18, 16, v28\n"
    "v_and_b32 v19, 0xffff0000, v28\n"
    "v_cvt_pk_bf16_f32 v28, v20, v21\n"
    "v_lshlrev_b32 v20, 16, v28\n"
    "v_and_b32 v21, 0xffff0000, v28\n"
    "v_cvt_pk_bf16_f32 v28, v22, v23\n"
    "v_lshlrev_b32 v22, 16, v28\n"
    "v_and_b32 v23, 0xffff0000, v28\n"
    "v_cvt_pk_bf16_f32 v28, v24, v25\n"
    "v_lshlrev_b32 v24, 16, v28\n"
    "v_and_b32 v25, 0xffff0000, v28\n"
    "v_cvt_pk_bf16_f32 v28, v26, v27\n"
    "v_lshlrev_b32 v26, 16, v28\n"
    "v_and_b32 v27, 0xffff0000, v28\n"
    "v_mov_b32 v28, 0xff800000\n"
    "v_cndmask_b32 v16, v16, v28, s[40:41]\n"
    "v_cndmask_b32 v20, v20, v28, s[48:49]\n"
    "v_cndmask_b32 v24, v28, v24, s[78:79]\n"
    "v_cndmask_b32 v17, v17, v28, s[42:43]\n"
    "v_cndmask_b32 v21, v21, v28, s[50:51]\n"
    "v_cndmask_b32 v25, v28, v25, s[78:79]\n"
    "v_cndmask_b32 v18, v18, v28, s[44:45]\n"
    "v_cndmask_b32 v22, v22, v28, s[52:53]\n"
    "v_cndmask_b32 v26, v28, v26, s[78:79]\n"
    "v_cndmask_b32 v19, v19, v28, s[46:47]\n"
    "v_cndmask_b32 v23, v23, v28, s[54:55]\n"
    "v_cndmask_b32 v27, v28, v27, s[78:79]\n"
    "v_max3_f32 v29, v16, v17, v18\n"
    "v_max3_f32 v29, v29, v19, v20\n"
    "v_max3_f32 v29, v29, v21, v22\n"
    "v_max3_f32 v29, v29, v23, v24\n"
    "v_max3_f32 v29, v29, v25, v26\n"
    "v_max_f32 v29, v29, v27\n"
    "v_mov_b32 v30, v29\n"
    "s_nop 1\n"
    "v_permlane16_swap_b32 v29, v30\n"
    "v_max_f32 v29, v29, v30\n"
    "v_mov_b32 v30, v29\n"
    "s_nop 1\n"
    "v_permlane32_swap_b32 v29, v30\n"
    "v_max_f32 v29, v29, v30\n"
    "v_mul_f32 v29, s88, v29\n"
    "v_fma_f32 v16, v16, s88, -v29\n"
    "v_fma_f32 v17, v17, s88, -v29\n"
    "v_fma_f32 v18, v18, s88, -v29\n"
    "v_fma_f32 v19, v19, s88, -v29\n"
    "v_fma_f32 v20, v20, s88, -v29\n"
    "v_fma_f32 v21, v21, s88, -v29\n"
    "v_fma_f32 v22, v22, s88, -v29\n"
    "v_fma_f32 v23, v23, s88, -v29\n"
    "v_fma_f32 v24, v24, s88, -v29\n"
    "v_fma_f32 v25, v25, s88, -v29\n"
    "v_fma_f32 v26, v26, s88, -v29\n"
    "v_fma_f32 v27, v27, s88, -v29\n"
    "v_exp_f32 v16, v16\n"
    "v_exp_f32 v17, v17\n"
    "v_exp_f32 v18, v18\n"
    "v_exp_f32 v19, v19\n"
    "v_exp_f32 v20, v20\n"
    "v_exp_f32 v21, v21\n"
    "v_exp_f32 v22, v22\n"
    "v_exp_f32 v23, v23\n"
    "v_exp_f32 v24, v24\n"
    "v_exp_f32 v25, v25\n"
    "v_exp_f32 v26, v26\n"
    "v_exp_f32 v27, v27\n"
    "v_add_f32 v29, v16, v17\n"
    "v_add_f32 v29, v29, v18\n"
    "v_add_f32 v29, v29, v19\n"
    "v_add_f32 v29, v29, v20\n"
    "v_add_f32 v29, v29, v21\n"
    "v_add_f32 v29, v29, v22\n"
    "v_add_f32 v29, v29, v23\n"
    "v_add_f32 v29, v29, v24\n"
    "v_add_f32 v29, v29, v25\n"
    "v_add_f32 v29, v29, v26\n"
    "v_add_f32 v29, v29, v27\n"
    "v_mov_b32 v30, v29\n"
    "s_nop 1\n"
    "v_permlane16_swap_b32 v29, v30\n"
    "v_add_f32 v29, v29, v30\n"
    "v_mov_b32 v30, v29\n"
    "s_nop 1\n"
    "v_permlane32_swap_b32 v29, v30\n"
    "v_add_f32 v29, v29, v30\n"
    "v_rcp_f32 v29, v29\n"
    "s_nop 0\n"
    "v_mul_f32 v16, v16, v29\n"
    "v_mul_f32 v17, v17, v29\n"
    "v_mul_f32 v18, v18, v29\n"
    "v_mul_f32 v19, v19, v29\n"
    "v_mul_f32 v20, v20, v29\n"
    "v_mul_f32 v21, v21, v29\n"
    "v_mul_f32 v22, v22, v29\n"
    "v_mul_f32 v23, v23, v29\n"
    "v_mul_f32 v24, v24, v29\n"
    "v_mul_f32 v25, v25, v29\n"
    "v_mul_f32 v26, v26, v29\n"
    "v_mul_f32 v27, v27, v29\n"
    "v_cvt_pk_bf16_f32 v32, v16, v17\n"
    "v_cvt_pk_bf16_f32 v33, v18, v19\n"
    "v_cvt_pk_bf16_f32 v34, v20, v21\n"
    "v_cvt_pk_bf16_f32 v35, v22, v23\n"
    "v_cvt_pk_bf16_f32 v36, v24, v25\n"
    "v_cvt_pk_bf16_f32 v37, v26, v27\n"
    "s_sub_u32 s76, s68, 1\n"
    "s_mul_i32 s76, s76, 1536\n"
    "v_add_u32 v28, s76, v0\n"
    "v_xor_b32 v29, 0x100, v15\n"
    "v_add_u32 v29, s76, v29\n"
    "ds_write_b128 v28, v[32:35] offset:29696\n"
    "ds_write_b64 v29, v[36:37] offset:30720\n"
    "notok_%=:\n"
    "s_add_u32 s89, s80, 1\n"
    "s_cmp_eq_u32 s68, 3\n"
    "s_cbranch_scc1 wave3_%=\n"
    "wave3done_%=:\n"
    "v_mov_b64 v[16:17], -1\n"
    "v_mov_b64 v[18:19], -1\n"
    "v_lshlrev_b32 v20, 9, v0\n"
    "v_and_b32 v20, 0x60000, v20\n"
    "v_and_b32 v21, 0xf0, v0\n"
    "v_or_b32 v20, v20, v21\n"
    "s_add_u32 s76, s80, 2\n"
    "s_and_b32 s76, s76, 3\n"
    "s_lshl_b32 s76, s76, 19\n"
    "s_add_u32 s76, s76, s72\n"
    "buffer_store_dwordx4 v[16:19], v21, s[64:67], s83 offen sc1\n"
    "buffer_store_dwordx4 v[16:19], v20, s[64:67], s76 offen sc1\n"
    "v_lshrrev_b32 v15, 1, v0\n"
    "v_lshlrev_b32 v22, 1, v21\n"
    "v_bfe_u32 v23, v0, 8, 1\n"
    "v_lshl_add_u32 v22, v23, 17, v22\n"
    "v_lshrrev_b32 v23, 9, v0\n"
    "v_lshl_add_u32 v22, v23, 4, v22\n"
    "s_add_u32 s81, s81, 24576\n"
    "s_add_u32 s76, s81, s77\n"
    "s_add_u32 s76, s76, 8192\n"
    "s_mov_b32 m0, s98\n"
    "s_waitcnt lgkmcnt(0)\n"
    "s_barrier\n"
    "s_cmp_eq_u32 s89, 64\n"
    "s_cbranch_scc1 nokx_%=\n"
    "buffer_load_dwordx4 v0, s[64:67], s76 offen sc1 lds\n"
    "nokx_%=:\n"
    "ds_read_b128 v[24:27], v0 offset:29696\n"
    "ds_read_b64 v[28:29], v15 offset:30720\n"
    "ds_read_b128 v[32:35], v0 offset:31232\n"
    "ds_read_b64 v[30:31], v15 offset:32256\n"
    "s_waitcnt lgkmcnt(0)\n"
    "v_mfma_f32_16x16x32_bf16 v[36:39], v[4:7], v[24:27], 0\n"
    "v_mfma_f32_16x16x16_bf16 v[40:43], v[8:9], v[28:29], 0\n"
    "v_mfma_f32_16x16x32_bf16 v[44:47], v[4:7], v[32:35], 0\n"
    "v_mfma_f32_16x16x16_bf16 v[48:51], v[8:9], v[30:31], 0\n"
    "s_nop 5\n"
    "v_pk_add_f32 v[36:37], v[36:37], v[40:41]\n"
    "v_pk_add_f32 v[38:39], v[38:39], v[42:43]\n"
    "v_pk_add_f32 v[44:45], v[44:45], v[48:49]\n"
    "v_pk_add_f32 v[46:47], v[46:47], v[50:51]\n"
    "v_cvt_pk_bf16_f32 v36, v36, v37\n"
    "v_cvt_pk_bf16_f32 v37, v38, v39\n"
    "v_cvt_pk_bf16_f32 v38, v44, v45\n"
    "v_cvt_pk_bf16_f32 v39, v46, v47\n"
    "s_nop 0\n"
    "v_permlane16_swap_b32 v36, v38\n"
    "v_permlane16_swap_b32 v37, v39\n"
    "buffer_store_dwordx4 v[36:39], v22, s[64:67], s82 offen sc1\n"
    "s_cmp_eq_u32 s89, 64\n"
    "s_nop 0\n"
    "s_cbranch_scc1 done_%=\n"
    "s_add_u32 s76, s76, 8192\n"
    "buffer_load_dwordx4 v[4:7], v0, s[64:67], s76 offen sc1\n"
    "s_add_u32 s82, s82, 262144\n"
    "s_and_b32 s76, s89, 3\n"
    "s_lshl_b32 s90, s76, 9\n"
    "s_add_u32 s90, s90, s69\n"
    "s_lshl_b32 s76, s76, 17\n"
    "s_add_u32 s91, s70, s76\n"
    "s_mov_b32 s80, s89\n"
    "s_and_b32 s76, s80, 1\n"
    "s_lshl_b32 s76, s76, 17\n"
    "s_add_u32 s83, s71, s76\n"
    "s_and_b32 s76, s80, 3\n"
    "s_lshl_b32 s76, s76, 19\n"
    "s_add_u32 s84, s72, s76\n"
    "s_add_u32 s85, s84, 262144\n"
    "s_mov_b32 s94, 0\n"
    "v_mov_b64 v[16:17], 0\n"
    "v_mov_b64 v[18:19], 0\n"
    "v_mov_b64 v[20:21], 0\n"
    "v_mov_b64 v[22:23], 0\n"
    "s_cmp_lg_u32 s80, s80\n"
    "s_nop 0\n"
    "s_setpc_b64 s[56:57]\n"
    "notw0_%=:\n"
    "s_waitcnt lgkmcnt(0)\n"
    "s_barrier\n"
    "s_sleep 0\n"
    "buffer_load_dwordx2 v[8:9], v11, s[64:67], s83 offen sc1\n"
    "s_sub_u32 s76, s68, 1\n"
    "s_cmp_lt_u32 s76, 2\n"
    "s_cbranch_scc0 vcheck_%=\n"
    "s_sleep 2\n"
    "s_lshl_b32 s76, s76, 12\n"
    "s_add_u32 s76, s76, s81\n"
    "buffer_load_dwordx4 v[48:51], v0, s[64:67], s76 offen offset:0 sc1\n"
    "buffer_load_dwordx4 v[52:55], v0, s[64:67], s76 offen offset:1024 sc1\n"
    "buffer_load_dwordx4 v[56:59], v0, s[64:67], s76 offen offset:2048 sc1\n"
    "buffer_load_dwordx4 v[60:63], v0, s[64:67], s76 offen offset:3072 sc1\n"
    "s_waitcnt vmcnt(4)\n"
    "s_branch vcheck2_%=\n"
    "vretry_%=:\n"
    "s_nop 7\n"
    "s_sleep 3\n"
    "buffer_load_dwordx2 v[8:9], v11, s[64:67], s83 offen sc1\n"
    "s_add_u32 s95, s95, 1\n"
    "s_cmp_lt_u32 s95, 400000\n"
    "s_cbranch_scc1 vcheck_%=\n"
    "s_branch fail_%=\n"
    "wave3_%=:\n"
    "s_and_b32 s76, s89, 0x43\n"
    "s_cmp_eq_u32 s76, 0\n"
    "s_cbranch_scc1 prodwait_%=\n"
    "proddone_%=:\n"
    "v_lshlrev_b32 v16, 3, v0\n"
    "s_add_u32 s76, s81, 32768\n"
    "buffer_load_dword v61, v16, s[64:67], s76 offen\n"
    "s_add_u32 s76, s76, 8192\n"
    "buffer_load_dword v62, v16, s[64:67], s76 offen\n"
    "s_add_u32 s76, s81, 24576\n"
    "buffer_load_dword v63, v16, s[64:67], s76 offen\n"
    "s_branch wave3done_%=\n"
    "prodwait_%=:\n"
    "s_lshr_b32 s76, s89, 2\n"
    "s_lshl_b32 s76, s76, 7\n"
    "s_add_u32 s76, s76, 1152\n"
    "v_mov_b32 v16, 0\n"
    "prodloop_%=:\n"
    "buffer_load_dword v17, v16, s[64:67], s76 offen sc1\n"
    "s_waitcnt vmcnt(0)\n"
    "v_cmp_le_u32 vcc, 48, v17\n"
    "s_nop 4\n"
    "s_cbranch_vccnz proddone_%=\n"
    "s_sleep 2\n"
    "s_add_u32 s95, s95, 1\n"
    "s_cmp_lt_u32 s95, 400000\n"
    "s_cbranch_scc1 prodloop_%=\n"
    "s_branch fail_%=\n"
    "done_%=:\n"
    "s_mov_b32 %0, 1\n"
    "fail_%=:\n"
    "s_waitcnt vmcnt(0) lgkmcnt(0)\n"
    "s_nop 7\n"
    : "=&s"(ok)
    : "{v[64:71]}"(W[0]), "{v[72:79]}"(W[1]), "{v[80:87]}"(W[2]), "{v[88:95]}"(W[3]), "{v[96:103]}"(W[4]), "{v[104:111]}"(W[5]), "{v[112:119]}"(W[6]), "{v[120:127]}"(W[7]), "{v[128:135]}"(W[8]), "{v[136:143]}"(W[9]), "{v[144:151]}"(W[10]), "{v[152:159]}"(W[11]), "{v[160:167]}"(W[12]), "{v[168:175]}"(W[13]), "{v[176:183]}"(W[14]), "{v[184:191]}"(W[15]), "{v[192:199]}"(W[16]), "{v[200:207]}"(W[17]), "{v[208:215]}"(W[18]), "{v[216:223]}"(W[19]), "{v[224:231]}"(W[20]), "{v[232:239]}"(W[21]), "{v[240:247]}"(W[22]), "{v[248:255]}"(W[23]),
      "{s[64:67]}"(rw), "{s[68:75]}"(sc)
    : "memory", "vcc", "scc", "v0", "v1", "v2", "v3", "v4", "v5", "v6", "v7", "v8", "v9", "v10", "v11", "v12", "v13", "v14", "v15", "v16", "v17", "v18", "v19", "v20", "v21", "v22", "v23", "v24", "v25", "v26", "v27", "v28", "v29", "v30", "v31", "v32", "v33", "v34", "v35", "v36", "v37", "v38", "v39", "v40", "v41", "v42", "v43", "v44", "v45", "v46", "v47", "v48", "v49", "v50", "v51", "v52", "v53", "v54", "v55", "v56", "v57", "v58", "v59", "v60", "v61", "v62", "v63", "s40", "s41", "s42", "s43", "s44", "s45", "s46", "s47", "s48", "s49", "s50", "s51", "s52", "s53", "s54", "s55", "s56", "s57", "s76", "s77", "s78", "s79", "s80", "s81", "s82", "s83", "s84", "s85", "s86", "s87", "s88", "s89", "s90", "s91", "s92", "s93", "s94", "s95", "s96", "s97", "s98");
  return ok != 0;
}

__device__ __forceinline__ bool vo_loop(const u32x8 (&W)[NREG], rsrc_t rw, u32x8 sc) {
  int ok;
  asm volatile(
    "s_mov_b32 %0, 0\n"
    "v_mbcnt_lo_u32_b32 v63, -1, 0\n"
    "v_mbcnt_hi_u32_b32 v63, -1, v63\n"
    "v_lshlrev_b32 v0, 4, v63\n"
    "v_and_b32 v62, 15, v63\n"
    "v_lshrrev_b32 v61, 4, v63\n"
    "v_and_b32 v60, 8, v63\n"
    "v_cmp_ne_u32 vcc, 0, v60\n"
    "v_mov_b32 v1, 0x4444\n"
    "v_mov_b32 v60, 0xeeee\n"
    "v_cndmask_b32 v1, v1, v60, vcc\n"
    "s_lshl_b32 s76, s68, 14\n"
    "s_add_u32 s76, s76, 32768\n"
    "v_add_u32 v2, s76, v0\n"
    "s_lshl_b32 s76, s68, 11\n"
    "v_add_u32 v3, s76, v0\n"
    "v_and_b32 v60, 1, v61\n"
    "v_lshlrev_b32 v4, 9, v60\n"
    "v_lshrrev_b32 v60, 1, v61\n"
    "v_lshl_add_u32 v4, v60, 6, v4\n"
    "v_lshl_add_u32 v4, v62, 2, v4\n"
    "s_lshl_b32 s76, s68, 10\n"
    "s_add_u32 s76, s76, 16384\n"
    "v_add_u32 v4, s76, v4\n"
    "v_bfe_u32 v60, v63, 2, 1\n"
    "v_lshlrev_b32 v5, 8, v60\n"
    "v_lshrrev_b32 v60, 3, v63\n"
    "v_lshl_add_u32 v5, v60, 4, v5\n"
    "v_bfe_u32 v60, v63, 1, 1\n"
    "v_lshl_add_u32 v5, v60, 7, v5\n"
    "v_and_b32 v60, 1, v63\n"
    "v_lshl_add_u32 v5, v60, 3, v5\n"
    "s_mov_b32 s80, 0\n"
    "s_lshl_b32 s77, s68, 15\n"
    "s_add_u32 s77, s77, s73\n"
    "s_memrealtime s[96:97]\n"
    "v_mov_b32 v16, 29312\n"
    "s_waitcnt lgkmcnt(0)\n"
    "v_mov_b32 v18, s96\n"
    "v_mov_b32 v19, s96\n"
    "v_mov_b32 v20, 0\n"
    "ds_write_b64 v16, v[18:19]\n"
    "ds_write_b32 v16, v20 offset:16\n"
    "v_mov_b64 v[8:9], 0\n"
    "v_mov_b64 v[10:11], 0\n"
    "s_mov_b32 s86, 3040\n"
    "s_mov_b32 s87, 2000\n"
    "s_nop 0\n"
    "s_branch step_%=\n"
    "next7_%=:\n"
    "s_nop 0\n"
    "mwait7_%=:\n"
    "s_memrealtime s[96:97]\n"
    "s_waitcnt lgkmcnt(0)\n"
    "s_sub_u32 s96, s93, s96\n"
    "s_sub_u32 s96, s96, 1\n"
    "s_cmp_lt_u32 s96, 1000\n"
    "s_cbranch_scc1 mwait7_%=\n"
    "mpoll7_%=:\n"
    "buffer_load_dwordx4 v[16:19], v0, s[64:67], s91 offen sc1\n"
    "buffer_load_dwordx4 v[20:23], v0, s[64:67], s91 offen offset:1024 sc1\n"
    "buffer_load_dwordx2 v[26:27], off, s[64:67], s90 sc1\n"
    "v_mov_b32 v25, 29312\n"
    "s_waitcnt vmcnt(2)\n"
    "ds_write_b128 v3, v[16:19]\n"
    "v_max3_u32 v24, v16, v17, v18\n"
    "s_waitcnt vmcnt(1)\n"
    "ds_write_b128 v3, v[20:23] offset:1024\n"
    "v_max3_u32 v24, v24, v19, v20\n"
    "v_max3_u32 v24, v24, v21, v22\n"
    "v_max_u32 v24, v24, v23\n"
    "s_waitcnt vmcnt(0)\n"
    "ds_write_b64 v25, v[26:27]\n"
    "v_max3_u32 v24, v24, v26, v27\n"
    "v_cmp_eq_u32 vcc, -1, v24\n"
    "s_cbranch_vccnz mretry7_%=\n"
    "s_branch step_%=\n"
    "next_%=:\n"
    "s_nop 2\n"
    "mwait_%=:\n"
    "s_memrealtime s[96:97]\n"
    "s_waitcnt lgkmcnt(0)\n"
    "s_sub_u32 s96, s93, s96\n"
    "s_sub_u32 s96, s96, 1\n"
    "s_cmp_lt_u32 s96, 1000\n"
    "s_cbranch_scc1 mwait_%=\n"
    "mpoll_%=:\n"
    "buffer_load_dwordx4 v[16:19], v0, s[64:67], s91 offen sc1\n"
    "buffer_load_dwordx4 v[20:23], v0, s[64:67], s91 offen offset:1024 sc1\n"
    "s_waitcnt vmcnt(1)\n"
    "ds_write_b128 v3, v[16:19]\n"
    "v_max3_u32 v24, v16, v17, v18\n"
    "s_waitcnt vmcnt(0)\n"
    "ds_write_b128 v3, v[20:23] offset:1024\n"
    "v_max3_u32 v24, v24, v19, v20\n"
    "v_max3_u32 v24, v24, v21, v22\n"
    "v_max_u32 v24, v24, v23\n"
    "v_cmp_eq_u32 vcc, -1, v24\n"
    "s_cbranch_vccnz mretry_%=\n"
    "step_%=:\n"
    "s_waitcnt lgkmcnt(0)\n"
    "s_barrier\n"
    "ds_read_b128 v[16:19], v0 offset:0\n"
    "ds_read_b128 v[20:23], v0 offset:1024\n"
    "ds_read_b128 v[24:27], v0 offset:2048\n"
    "ds_read_b128 v[28:31], v0 offset:3072\n"
    "ds_read_b128 v[32:35], v0 offset:4096\n"
    "ds_read_b128 v[36:39], v0 offset:5120\n"
    "ds_read_b128 v[40:43], v0 offset:6144\n"
    "ds_read_b128 v[44:47], v0 offset:7168\n"
    "s_and_b32 s76, s80, 1\n"
    "s_lshl_b32 s76, s76, 17\n"
    "s_add_u32 s83, s71, s76\n"
    "s_and_b32 s76, s80, 3\n"
    "s_lshl_b32 s76, s76, 19\n"
    "s_add_u32 s84, s72, s76\n"
    "s_add_u32 s85, s84, 4096\n"
    "s_add_u32 s89, s80, 1\n"
    "s_waitcnt lgkmcnt(7)\n"
    "v_smfmac_f32_16x16x64_bf16 v[8:11], v[16:19], v[64:71], v1\n"
    "ds_read_b128 v[16:19], v0 offset:8192\n"
    "s_waitcnt lgkmcnt(7)\n"
    "v_smfmac_f32_16x16x64_bf16 v[8:11], v[20:23], v[72:79], v1\n"
    "ds_read_b128 v[20:23], v0 offset:9216\n"
    "s_waitcnt lgkmcnt(7)\n"
    "v_smfmac_f32_16x16x64_bf16 v[8:11], v[24:27], v[80:87], v1\n"
    "ds_read_b128 v[24:27], v0 offset:10240\n"
    "s_waitcnt lgkmcnt(7)\n"
    "v_smfmac_f32_16x16x64_bf16 v[8:11], v[28:31], v[88:95], v1\n"
    "ds_read_b128 v[28:31], v0 offset:11264\n"
    "s_waitcnt lgkmcnt(7)\n"
    "v_smfmac_f32_16x16x64_bf16 v[8:11], v[32:35], v[96:103], v1\n"
    "ds_read_b128 v[32:35], v0 offset:12288\n"
    "s_waitcnt lgkmcnt(7)\n"
    "v_smfmac_f32_16x16x64_bf16 v[8:11], v[36:39], v[104:111], v1\n"
    "ds_read_b128 v[36:39], v0 offset:13312\n"
    "s_waitcnt lgkmcnt(7)\n"
    "v_smfmac_f32_16x16x64_bf16 v[8:11], v[40:43], v[112:119], v1\n"
    "ds_read_b128 v[40:43], v0 offset:14336\n"
    "s_waitcnt lgkmcnt(7)\n"
    "v_smfmac_f32_16x16x64_bf16 v[8:11], v[44:47], v[120:127], v1\n"
    "ds_read_b128 v[44:47], v0 offset:15360\n"
    "s_waitcnt lgkmcnt(7)\n"
    "v_smfmac_f32_16x16x64_bf16 v[8:11], v[16:19], v[128:135], v1\n"
    "s_waitcnt lgkmcnt(6)\n"
    "v_smfmac_f32_16x16x64_bf16 v[8:11], v[20:23], v[136:143], v1\n"
    "s_waitcnt lgkmcnt(5)\n"
    "v_smfmac_f32_16x16x64_bf16 v[8:11], v[24:27], v[144:151], v1\n"
    "s_waitcnt lgkmcnt(4)\n"
    "v_smfmac_f32_16x16x64_bf16 v[8:11], v[28:31], v[152:159], v1\n"
    "s_waitcnt lgkmcnt(3)\n"
    "v_smfmac_f32_16x16x64_bf16 v[8:11], v[32:35], v[160:167], v1\n"
    "s_waitcnt lgkmcnt(2)\n"
    "v_smfmac_f32_16x16x64_bf16 v[8:11], v[36:39], v[168:175], v1\n"
    "s_waitcnt lgkmcnt(1)\n"
    "v_smfmac_f32_16x16x64_bf16 v[8:11], v[40:43], v[176:183], v1\n"
    "s_waitcnt lgkmcnt(0)\n"
    "v_smfmac_f32_16x16x64_bf16 v[8:11], v[44:47], v[184:191], v1\n"
    "v_lshrrev_b32 v6, 1, v0\n"
    "s_nop 6\n"
    "v_mov_b32 v12, v8\n"
    "v_mov_b32 v13, v9\n"
    "v_mov_b32 v14, v10\n"
    "v_mov_b32 v15, v11\n"
    "v_permlane32_swap_b32 v8, v12\n"
    "v_permlane32_swap_b32 v9, v13\n"
    "v_permlane32_swap_b32 v10, v14\n"
    "v_permlane32_swap_b32 v11, v15\n"
    "v_pk_add_f32 v[8:9], v[8:9], v[12:13]\n"
    "v_pk_add_f32 v[10:11], v[10:11], v[14:15]\n"
    "v_cvt_pk_bf16_f32 v8, v8, v9\n"
    "v_cvt_pk_bf16_f32 v9, v10, v11\n"
    "v_min_u32 v8, -2, v8\n"
    "v_min_u32 v9, -2, v9\n"
    "s_mov_b64 exec, 0xffffffff\n"
    "buffer_store_dwordx2 v[8:9], v6, s[64:67], s83 offen sc1\n"
    "s_mov_b64 exec, -1\n"
    "s_cmp_eq_u32 s89, 64\n"
    "s_nop 0\n"
    "s_cbranch_scc1 done_%=\n"
    "s_cmp_lt_u32 s68, 2\n"
    "s_cbranch_scc0 norearm_%=\n"
    "v_mov_b64 v[16:17], -1\n"
    "v_mov_b64 v[18:19], -1\n"
    "v_lshlrev_b32 v20, 7, v0\n"
    "v_and_b32 v20, 0x18000, v20\n"
    "v_and_b32 v21, 0xf0, v0\n"
    "v_or_b32 v20, v20, v21\n"
    "s_add_u32 s76, s80, 3\n"
    "s_and_b32 s76, s76, 3\n"
    "s_lshl_b32 s76, s76, 17\n"
    "s_add_u32 s76, s76, s73\n"
    "s_lshl_b32 s90, s68, 8\n"
    "s_add_u32 s76, s76, s90\n"
    "buffer_store_dwordx4 v[16:19], v20, s[64:67], s76 offen sc1\n"
    "s_nop 1\n"
    "norearm_%=:\n"
    "v_mov_b32 v16, 29312\n"
    "ds_read_b64 v[16:17], v16\n"
    "s_waitcnt lgkmcnt(0)\n"
    "v_readfirstlane_b32 s92, v16\n"
    "v_readfirstlane_b32 s93, v17\n"
    "s_and_b32 s76, s89, 3\n"
    "s_lshl_b32 s90, s76, 9\n"
    "s_add_u32 s90, s90, s74\n"
    "s_lshl_b32 s76, s76, 17\n"
    "s_add_u32 s91, s70, s76\n"
    "s_add_u32 s82, s77, s76\n"
    "s_mov_b32 s94, 0\n"
    "ds_read_b128 v[48:51], v2 offset:0\n"
    "ds_read_b128 v[52:55], v2 offset:1024\n"
    "ds_read_b128 v[56:59], v2 offset:2048\n"
    "ds_read_b128 v[60:63], v2 offset:3072\n"
    "v_mov_b64 v[8:9], 0\n"
    "v_mov_b64 v[10:11], 0\n"
    "v_mov_b64 v[12:13], 0\n"
    "v_mov_b64 v[14:15], 0\n"
    "s_nop 1\n"
    "owait_%=:\n"
    "s_memrealtime s[96:97]\n"
    "s_waitcnt lgkmcnt(0)\n"
    "s_sub_u32 s96, s92, s96\n"
    "s_sub_u32 s96, s96, 1\n"
    "s_cmp_lt_u32 s96, 1000\n"
    "s_cbranch_scc1 owait_%=\n"
    "buffer_load_dwordx4 v[16:19], v0, s[64:67], s84 offen offset:0 sc1\n"
    "s_sleep 1\n"
    "buffer_load_dwordx4 v[20:23], v0, s[64:67], s84 offen offset:1024 sc1\n"
    "s_sleep 1\n"
    "buffer_load_dwordx4 v[24:27], v0, s[64:67], s84 offen offset:2048 sc1\n"
    "s_sleep 1\n"
    "buffer_load_dwordx4 v[28:31], v0, s[64:67], s84 offen offset:3072 sc1\n"
    "s_waitcnt vmcnt(3)\n"
    "buffer_load_dwordx4 v[32:35], v0, s[64:67], s85 offen offset:0 sc1\n"
    "ochk0_%=:\n"
    "v_max3_u32 v6, v16, v17, v18\n"
    "v_max_u32 v6, v6, v19\n"
    "v_cmp_eq_u32 vcc, -1, v6\n"
    "s_cbranch_vccnz oretry0_%=\n"
    "v_smfmac_f32_16x16x64_bf16 v[8:11], v[16:19], v[192:199], v1\n"
    "v_smfmac_f32_16x16x64_bf16 v[12:15], v[16:19], v[48:55], v1\n"
    "ds_read_b128 v[48:51], v2 offset:4096\n"
    "ds_read_b128 v[52:55], v2 offset:5120\n"
    "s_waitcnt vmcnt(3)\n"
    "buffer_load_dwordx4 v[36:39], v0, s[64:67], s85 offen offset:1024 sc1\n"
    "ochk1_%=:\n"
    "v_max3_u32 v6, v20, v21, v22\n"
    "v_max_u32 v6, v6, v23\n"
    "v_cmp_eq_u32 vcc, -1, v6\n"
    "s_cbranch_vccnz oretry1_%=\n"
    "v_smfmac_f32_16x16x64_bf16 v[8:11], v[20:23], v[200:207], v1\n"
    "v_smfmac_f32_16x16x64_bf16 v[12:15], v[20:23], v[56:63], v1\n"
    "ds_read_b128 v[56:59], v2 offset:6144\n"
    "ds_read_b128 v[60:63], v2 offset:7168\n"
    "s_waitcnt vmcnt(3)\n"
    "buffer_load_dwordx4 v[40:43], v0, s[64:67], s85 offen offset:2048 sc1\n"
    "ochk2_%=:\n"
    "v_max3_u32 v6, v24, v25, v26\n"
    "v_max_u32 v6, v6, v27\n"
    "v_cmp_eq_u32 vcc, -1, v6\n"
    "s_cbranch_vccnz oretry2_%=\n"
    "v_smfmac_f32_16x16x64_bf16 v[8:11], v[24:27], v[208:215], v1\n"
    "s_waitcnt lgkmcnt(2)\n"
    "v_smfmac_f32_16x16x64_bf16 v[12:15], v[24:27], v[48:55], v1\n"
    "ds_read_b128 v[48:51], v2 offset:8192\n"
    "ds_read_b128 v[52:55], v2 offset:9216\n"
    "s_waitcnt vmcnt(3)\n"
    "buffer_load_dwordx4 v[44:47], v0, s[64:67], s85 offen offset:3072 sc1\n"
    "ochk3_%=:\n"
    "v_max3_u32 v6, v28, v29, v30\n"
    "v_max_u32 v6, v6, v31\n"
    "v_cmp_eq_u32 vcc, -1, v6\n"
    "s_cbranch_vccnz oretry3_%=\n"
    "v_smfmac_f32_16x16x64_bf16 v[8:11], v[28:31], v[216:223], v1\n"
    "s_waitcnt lgkmcnt(2)\n"
    "v_smfmac_f32_16x16x64_bf16 v[12:15], v[28:31], v[56:63], v1\n"
    "ds_read_b128 v[56:59], v2 offset:10240\n"
    "ds_read_b128 v[60:63], v2 offset:11264\n"
    "s_waitcnt vmcnt(3)\n"
    "ochk4_%=:\n"
    "v_max3_u32 v6, v32, v33, v34\n"
    "v_max_u32 v6, v6, v35\n"
    "v_cmp_eq_u32 vcc, -1, v6\n"
    "s_cbranch_vccnz oretry4_%=\n"
    "v_smfmac_f32_16x16x64_bf16 v[8:11], v[32:35], v[224:231], v1\n"
    "s_waitcnt lgkmcnt(2)\n"
    "v_smfmac_f32_16x16x64_bf16 v[12:15], v[32:35], v[48:55], v1\n"
    "ds_read_b128 v[48:51], v2 offset:12288\n"
    "ds_read_b128 v[52:55], v2 offset:13312\n"
    "s_waitcnt vmcnt(2)\n"
    "ochk5_%=:\n"
    "v_max3_u32 v6, v36, v37, v38\n"
    "v_max_u32 v6, v6, v39\n"
    "v_cmp_eq_u32 vcc, -1, v6\n"
    "s_cbranch_vccnz oretry5_%=\n"
    "v_smfmac_f32_16x16x64_bf16 v[8:11], v[36:39], v[232:239], v1\n"
    "s_waitcnt lgkmcnt(2)\n"
    "v_smfmac_f32_16x16x64_bf16 v[12:15], v[36:39], v[56:63], v1\n"
    "ds_read_b128 v[56:59], v2 offset:14336\n"
    "ds_read_b128 v[60:63], v2 offset:15360\n"
    "s_waitcnt vmcnt(1)\n"
    "ochk6_%=:\n"
    "v_max3_u32 v6, v40, v41, v42\n"
    "v_max_u32 v6, v6, v43\n"
    "v_cmp_eq_u32 vcc, -1, v6\n"
    "s_cbranch_vccnz oretry6_%=\n"
    "v_smfmac_f32_16x16x64_bf16 v[8:11], v[40:43], v[240:247], v1\n"
    "s_waitcnt lgkmcnt(2)\n"
    "v_smfmac_f32_16x16x64_bf16 v[12:15], v[40:43], v[48:55], v1\n"
    "s_waitcnt vmcnt(0)\n"
    "ochk7_%=:\n"
    "v_max3_u32 v6, v44, v45, v46\n"
    "v_max_u32 v6, v6, v47\n"
    "v_cmp_eq_u32 vcc, -1, v6\n"
    "s_cbranch_vccnz oretry7_%=\n"
    "v_smfmac_f32_16x16x64_bf16 v[8:11], v[44:47], v[248:255], v1\n"
    "s_waitcnt lgkmcnt(0)\n"
    "v_smfmac_f32_16x16x64_bf16 v[12:15], v[44:47], v[56:63], v1\n"
    "s_nop 7\n"
    "v_permlane32_swap_b32 v8, v12\n"
    "v_permlane32_swap_b32 v9, v13\n"
    "v_permlane32_swap_b32 v10, v14\n"
    "v_permlane32_swap_b32 v11, v15\n"
    "v_pk_add_f32 v[8:9], v[8:9], v[12:13]\n"
    "v_pk_add_f32 v[10:11], v[10:11], v[14:15]\n"
    "ds_write2_b32 v4, v8, v9 offset1:32\n"
    "ds_write2_b32 v4, v10, v11 offset0:64 offset1:96\n"
    "s_cmp_lt_u32 s68, 4\n"
    "s_waitcnt lgkmcnt(0)\n"
    "s_barrier\n"
    "s_cbranch_scc0 nored_%=\n"
    "ds_read_b128 v[16:19], v0 offset:16384\n"
    "ds_read_b128 v[20:23], v0 offset:17408\n"
    "ds_read_b128 v[24:27], v0 offset:18432\n"
    "ds_read_b128 v[28:31], v0 offset:19456\n"
    "ds_read_b128 v[32:35], v0 offset:20480\n"
    "ds_read_b128 v[36:39], v0 offset:21504\n"
    "ds_read_b128 v[40:43], v0 offset:22528\n"
    "ds_read_b128 v[44:47], v0 offset:23552\n"
    "s_waitcnt lgkmcnt(6)\n"
    "v_pk_add_f32 v[16:17], v[16:17], v[20:21]\n"
    "v_pk_add_f32 v[18:19], v[18:19], v[22:23]\n"
    "s_waitcnt lgkmcnt(4)\n"
    "v_pk_add_f32 v[24:25], v[24:25], v[28:29]\n"
    "v_pk_add_f32 v[26:27], v[26:27], v[30:31]\n"
    "v_pk_add_f32 v[16:17], v[16:17], v[24:25]\n"
    "v_pk_add_f32 v[18:19], v[18:19], v[26:27]\n"
    "s_waitcnt lgkmcnt(2)\n"
    "v_pk_add_f32 v[32:33], v[32:33], v[36:37]\n"
    "v_pk_add_f32 v[34:35], v[34:35], v[38:39]\n"
    "s_waitcnt lgkmcnt(0)\n"
    "v_pk_add_f32 v[40:41], v[40:41], v[44:45]\n"
    "v_pk_add_f32 v[42:43], v[42:43], v[46:47]\n"
    "v_pk_add_f32 v[32:33], v[32:33], v[40:41]\n"
    "v_pk_add_f32 v[34:35], v[34:35], v[42:43]\n"
    "v_pk_add_f32 v[16:17], v[16:17], v[32:33]\n"
    "v_pk_add_f32 v[18:19], v[18:19], v[34:35]\n"
    "v_cvt_pk_bf16_f32 v16, v16, v17\n"
    "v_cvt_pk_bf16_f32 v17, v18, v19\n"
    "v_min_u32 v16, -2, v16\n"
    "v_min_u32 v17, -2, v17\n"
    "buffer_store_dwordx2 v[16:17], v5, s[64:67], s82 offen sc1\n"
    "s_memrealtime s[96:97]\n"
    "s_waitcnt lgkmcnt(0)\n"
    "s_add_u32 s96, s96, 22\n"
    "redjoin_%=:\n"
    "s_sub_i32 s76, s96, s93\n"
    "s_cmp_gt_i32 s76, 0\n"
    "s_cselect_b32 s93, s96, s93\n"
    "v_mov_b64 v[8:9], 0\n"
    "v_mov_b64 v[10:11], 0\n"
    "s_mov_b32 s80, s89\n"
    "s_mov_b32 s94, 0\n"
    "s_cmp_eq_u32 s68, 7\n"
    "s_nop 0\n"
    "s_cbranch_scc1 next7_%=\n"
    "s_branch next_%=\n"
    "nored_%=:\n"
    "s_memrealtime s[96:97]\n"
    "s_lshl_b32 s76, s69, 3\n"
    "s_or_b32 s76, s76, s68\n"
    "s_cmp_eq_u32 s76, 5\n"
    "s_waitcnt lgkmcnt(0)\n"
    "s_cbranch_scc1 times_%=\n"
    "timesdone_%=:\n"
    "s_add_u32 s96, s96, 31\n"
    "s_lshl_b32 s76, s69, 3\n"
    "s_or_b32 s76, s76, s68\n"
    "s_cmp_lg_u32 s76, 14\n"
    "s_cbranch_scc1 redjoin_%=\n"
    "s_mov_b32 s76, 195067904\n"
    "v_mov_b32 v17, s80\n"
    "v_lshlrev_b32 v16, 3, v0\n"
    "s_mov_b64 exec, 0xff\n"
    "buffer_store_dword v17, v16, s[64:67], s76 offen sc1\n"
    "s_mov_b64 exec, -1\n"
    "s_nop 0\n"
    "s_branch redjoin_%=\n"
    "times_%=:\n"
    "v_mov_b32 v16, 29312\n"
    "ds_read_b32 v17, v16 offset:16\n"
    "v_mov_b32 v18, 0\n"
    "s_sub_u32 s88, s96, s92\n"
    "s_waitcnt lgkmcnt(0)\n"
    "ds_write_b32 v16, v18 offset:16\n"
    "v_readfirstlane_b32 s76, v17\n"
    "s_min_u32 s88, s88, 150\n"
    "s_add_u32 s88, s88, 39\n"
    "s_lshl_b32 s88, s88, 4\n"
    "s_sub_i32 s88, s88, s87\n"
    "s_ashr_i32 s88, s88, 2\n"
    "s_cmp_eq_u32 s76, 0\n"
    "s_cselect_b32 s88, s88, 0\n"
    "s_add_i32 s87, s87, s88\n"
    "s_cmp_eq_u32 s76, 0\n"
    "s_cselect_b32 s88, -8, 64\n"
    "s_cmp_eq_u32 s80, 0\n"
    "s_cselect_b32 s88, 0, s88\n"
    "s_add_i32 s86, s86, s88\n"
    "s_lshr_b32 s88, s86, 4\n"
    "s_add_u32 s88, s88, s96\n"
    "v_mov_b32 v18, s88\n"
    "s_lshr_b32 s76, s87, 4\n"
    "s_add_u32 s88, s88, s76\n"
    "v_mov_b32 v19, s88\n"
    "v_min_u32 v18, -2, v18\n"
    "v_min_u32 v19, -2, v19\n"
    "v_lshlrev_b32 v16, 3, v0\n"
    "s_and_b32 s76, s89, 3\n"
    "s_lshl_b32 s76, s76, 9\n"
    "s_add_u32 s88, s73, 786432\n"
    "s_add_u32 s76, s76, s88\n"
    "s_mov_b64 exec, 15\n"
    "buffer_store_dwordx2 v[18:19], v16, s[64:67], s76 offen sc1\n"
    "v_mov_b64 v[20:21], -1\n"
    "s_add_u32 s76, s80, 3\n"
    "s_and_b32 s76, s76, 3\n"
    "s_lshl_b32 s76, s76, 9\n"
    "s_add_u32 s76, s76, s88\n"
    "buffer_store_dwordx2 v[20:21], v16, s[64:67], s76 offen sc1\n"
    "s_mov_b64 exec, -1\n"
    "s_nop 0\n"
    "s_branch timesdone_%=\n"
    "oretry0_%=:\n"
    "v_mov_b32 v6, 29312\n"
    "v_mov_b32 v7, 1\n"
    "ds_write_b32 v6, v7 offset:16\n"
    "s_sleep 3\n"
    "s_waitcnt lgkmcnt(0)\n"
    "buffer_load_dwordx4 v[16:19], v0, s[64:67], s84 offen offset:0 sc1\n"
    "buffer_load_dwordx4 v[20:23], v0, s[64:67], s84 offen offset:1024 sc1\n"
    "buffer_load_dwordx4 v[24:27], v0, s[64:67], s84 offen offset:2048 sc1\n"
    "buffer_load_dwordx4 v[28:31], v0, s[64:67], s84 offen offset:3072 sc1\n"
    "buffer_load_dwordx4 v[32:35], v0, s[64:67], s85 offen offset:0 sc1\n"
    "s_add_u32 s94, s94, 1\n"
    "s_cmp_lt_u32 s94, 400000\n"
    "s_waitcnt vmcnt(0)\n"
    "s_cbranch_scc1 ochk0_%=\n"
    "s_branch fail_%=\n"
    "oretry1_%=:\n"
    "v_mov_b32 v6, 29312\n"
    "v_mov_b32 v7, 1\n"
    "ds_write_b32 v6, v7 offset:16\n"
    "s_sleep 3\n"
    "s_waitcnt lgkmcnt(0)\n"
    "buffer_load_dwordx4 v[20:23], v0, s[64:67], s84 offen offset:1024 sc1\n"
    "buffer_load_dwordx4 v[24:27], v0, s[64:67], s84 offen offset:2048 sc1\n"
    "buffer_load_dwordx4 v[28:31], v0, s[64:67], s84 offen offset:3072 sc1\n"
    "buffer_load_dwordx4 v[32:35], v0, s[64:67], s85 offen offset:0 sc1\n"
    "buffer_load_dwordx4 v[36:39], v0, s[64:67], s85 offen offset:1024 sc1\n"
    "s_add_u32 s94, s94, 1\n"
    "s_cmp_lt_u32 s94, 400000\n"
    "s_waitcnt vmcnt(0)\n"
    "s_cbranch_scc1 ochk1_%=\n"
    "s_branch fail_%=\n"
    "oretry2_%=:\n"
    "v_mov_b32 v6, 29312\n"
    "v_mov_b32 v7, 1\n"
    "ds_write_b32 v6, v7 offset:16\n"
    "s_sleep 3\n"
    "s_waitcnt lgkmcnt(0)\n"
    "buffer_load_dwordx4 v[24:27], v0, s[64:67], s84 offen offset:2048 sc1\n"
    "buffer_load_dwordx4 v[28:31], v0, s[64:67], s84 offen offset:3072 sc1\n"
    "buffer_load_dwordx4 v[32:35], v0, s[64:67], s85 offen offset:0 sc1\n"
    "buffer_load_dwordx4 v[36:39], v0, s[64:67], s85 offen offset:1024 sc1\n"
    "buffer_load_dwordx4 v[40:43], v0, s[64:67], s85 offen offset:2048 sc1\n"
    "s_add_u32 s94, s94, 1\n"
    "s_cmp_lt_u32 s94, 400000\n"
    "s_waitcnt vmcnt(0)\n"
    "s_cbranch_scc1 ochk2_%=\n"
    "s_branch fail_%=\n"
    "oretry3_%=:\n"
    "v_mov_b32 v6, 29312\n"
    "v_mov_b32 v7, 1\n"
    "ds_write_b32 v6, v7 offset:16\n"
    "s_sleep 3\n"
    "s_waitcnt lgkmcnt(0)\n"
    "buffer_load_dwordx4 v[28:31], v0, s[64:67], s84 offen offset:3072 sc1\n"
    "buffer_load_dwordx4 v[32:35], v0, s[64:67], s85 offen offset:0 sc1\n"
    "buffer_load_dwordx4 v[36:39], v0, s[64:67], s85 offen offset:1024 sc1\n"
    "buffer_load_dwordx4 v[40:43], v0, s[64:67], s85 offen offset:2048 sc1\n"
    "buffer_load_dwordx4 v[44:47], v0, s[64:67], s85 offen offset:3072 sc1\n"
    "s_add_u32 s94, s94, 1\n"
    "s_cmp_lt_u32 s94, 400000\n"
    "s_waitcnt vmcnt(0)\n"
    "s_cbranch_scc1 ochk3_%=\n"
    "s_branch fail_%=\n"
    "oretry4_%=:\n"
    "v_mov_b32 v6, 29312\n"
    "v_mov_b32 v7, 1\n"
    "ds_write_b32 v6, v7 offset:16\n"
    "s_sleep 3\n"
    "s_waitcnt lgkmcnt(0)\n"
    "buffer_load_dwordx4 v[32:35], v0, s[64:67], s85 offen offset:0 sc1\n"
    "buffer_load_dwordx4 v[36:39], v0, s[64:67], s85 offen offset:1024 sc1\n"
    "buffer_load_dwordx4 v[40:43], v0, s[64:67], s85 offen offset:2048 sc1\n"
    "buffer_load_dwordx4 v[44:47], v0, s[64:67], s85 offen offset:3072 sc1\n"
    "s_add_u32 s94, s94, 1\n"
    "s_cmp_lt_u32 s94, 400000\n"
    "s_waitcnt vmcnt(0)\n"
    "s_cbranch_scc1 ochk4_%=\n"
    "s_branch fail_%=\n"
    "oretry5_%=:\n"
    "v_mov_b32 v6, 29312\n"
    "v_mov_b32 v7, 1\n"
    "ds_write_b32 v6, v7 offset:16\n"
    "s_sleep 3\n"
    "s_waitcnt lgkmcnt(0)\n"
    "buffer_load_dwordx4 v[36:39], v0, s[64:67], s85 offen offset:1024 sc1\n"
    "buffer_load_dwordx4 v[40:43], v0, s[64:67], s85 offen offset:2048 sc1\n"
    "buffer_load_dwordx4 v[44:47], v0, s[64:67], s85 offen offset:3072 sc1\n"
    "s_add_u32 s94, s94, 1\n"
    "s_cmp_lt_u32 s94, 400000\n"
    "s_waitcnt vmcnt(0)\n"
    "s_cbranch_scc1 ochk5_%=\n"
    "s_branch fail_%=\n"
    "oretry6_%=:\n"
    "v_mov_b32 v6, 29312\n"
    "v_mov_b32 v7, 1\n"
    "ds_write_b32 v6, v7 offset:16\n"
    "s_sleep 3\n"
    "s_waitcnt lgkmcnt(0)\n"
    "buffer_load_dwordx4 v[40:43], v0, s[64:67], s85 offen offset:2048 sc1\n"
    "buffer_load_dwordx4 v[44:47], v0, s[64:67], s85 offen offset:3072 sc1\n"
    "s_add_u32 s94, s94, 1\n"
    "s_cmp_lt_u32 s94, 400000\n"
    "s_waitcnt vmcnt(0)\n"
    "s_cbranch_scc1 ochk6_%=\n"
    "s_branch fail_%=\n"
    "oretry7_%=:\n"
    "v_mov_b32 v6, 29312\n"
    "v_mov_b32 v7, 1\n"
    "ds_write_b32 v6, v7 offset:16\n"
    "s_sleep 3\n"
    "s_waitcnt lgkmcnt(0)\n"
    "buffer_load_dwordx4 v[44:47], v0, s[64:67], s85 offen offset:3072 sc1\n"
    "s_add_u32 s94, s94, 1\n"
    "s_cmp_lt_u32 s94, 400000\n"
    "s_waitcnt vmcnt(0)\n"
    "s_cbranch_scc1 ochk7_%=\n"
    "s_branch fail_%=\n"
    "mretry_%=:\n"
    "s_nop 7\n"
    "s_sleep 3\n"
    "s_add_u32 s94, s94, 1\n"
    "s_cmp_lt_u32 s94, 400000\n"
    "s_cbranch_scc1 mpoll_%=\n"
    "s_branch fail_%=\n"
    "mretry7_%=:\n"
    "s_nop 7\n"
    "s_sleep 3\n"
    "s_add_u32 s94, s94, 1\n"
    "s_cmp_lt_u32 s94, 400000\n"
    "s_cbranch_scc1 mpoll7_%=\n"
    "s_branch fail_%=\n"
    "done_%=:\n"
    "s_mov_b32 %0, 1\n"
    "fail_%=:\n"
    "s_waitcnt vmcnt(0) lgkmcnt(0)\n"
    "s_nop 7\n"
    : "=&s"(ok)
    : "{v[64:71]}"(W[0]), "{v[72:79]}"(W[1]), "{v[80:87]}"(W[2]), "{v[88:95]}"(W[3]), "{v[96:103]}"(W[4]), "{v[104:111]}"(W[5]), "{v[112:119]}"(W[6]), "{v[120:127]}"(W[7]), "{v[128:135]}"(W[8]), "{v[136:143]}"(W[9]), "{v[144:151]}"(W[10]), "{v[152:159]}"(W[11]), "{v[160:167]}"(W[12]), "{v[168:175]}"(W[13]), "{v[176:183]}"(W[14]), "{v[184:191]}"(W[15]), "{v[192:199]}"(W[16]), "{v[200:207]}"(W[17]), "{v[208:215]}"(W[18]), "{v[216:223]}"(W[19]), "{v[224:231]}"(W[20]), "{v[232:239]}"(W[21]), "{v[240:247]}"(W[22]), "{v[248:255]}"(W[23]),
      "{s[64:67]}"(rw), "{s[68:75]}"(sc)
    : "memory", "vcc", "scc", "v0", "v1", "v2", "v3", "v4", "v5", "v6", "v7", "v8", "v9", "v10", "v11", "v12", "v13", "v14", "v15", "v16", "v17", "v18", "v19", "v20", "v21", "v22", "v23", "v24", "v25", "v26", "v27", "v28", "v29", "v30", "v31", "v32", "v33", "v34", "v35", "v36", "v37", "v38", "v39", "v40", "v41", "v42", "v43", "v44", "v45", "v46", "v47", "v48", "v49", "v50", "v51", "v52", "v53", "v54", "v55", "v56", "v57", "v58", "v59", "v60", "v61", "v62", "v63", "s40", "s41", "s42", "s43", "s44", "s45", "s46", "s47", "s48", "s49", "s50", "s51", "s52", "s53", "s54", "s55", "s56", "s57", "s76", "s77", "s78", "s79", "s80", "s81", "s82", "s83", "s84", "s85", "s86", "s87", "s88", "s89", "s90", "s91", "s92", "s93", "s94", "s95", "s96", "s97", "s98");
  return ok != 0;
}

__device__ __forceinline__ bool chain_role(KP p, int h, bool vo, int w, LAS char* lds) {
  const int l0 = lane_id();
  const unsigned long long votes = __builtin_amdgcn_ballot_w64(l0 < 10 && (cword(p, C_PROBE)[l0 < 10 ? l0 : 0] & 1));
  const int xa = xoff(p) + (__builtin_popcountll(votes & 31) >= 3 ? 8192 : 0), xb = xoff(p) + (__builtin_popcountll(votes >> 5) >= 3 ? 8192 : 0);
  const int cp = xcc_id() & 3, piece = (h >> 2) * 16384 + (h & 3) * 2048 + w * 256;
  u32x8 sc;
  sc[0] = w;
  sc[2] = xa + cp * XM_COPY + (w >> 2) * 16384 + (w & 3) * 2048;
  sc[3] = xa + XV_OFF + piece;
  sc[7] = WS_END + ((vo ? 32 : 0) + h) * 8 * 64 * 48 + w * 64 * 48;
  u32x8 W[NREG];
  if (!vo) {
    const int l = lane_id();
    const rsrc_t rq = mkrsrc(p->Wq + h * DH, W_BYTES - h * DH * 2), rk = mkrsrc(p->Wk + h * DH, W_BYTES - h * DH * 2);
    load8(lds, w, l, [&](int i, LAS char* dst) { wstage(rq, (2 * i) * 64 * 8192, dst, w, l); }, W);
    load8(lds, w, l, [&](int i, LAS char* dst) { wstage(rq, (2 * i + 1) * 64 * 8192, dst, w, l); }, W + 8);
    load8(lds, w, l, [&](int i, LAS char* dst) { wstage(rk, (2 * i) * 64 * 8192, dst, w, l); }, W + 16);
    load8_image(lds, w, l, rk, 8192, [&](int t) { return (2 * t + 1) * 64 * 8192 + w * 32; });
    image_to_frags(lds, w, l);
    stage_m0(p, lds, w, l);
    WSTAMP(232, h, 0);
    unsigned spins = 0;
#pragma nounroll
    while ((unsigned)__builtin_amdgcn_readfirstlane(__hip_atomic_load(cword(p, C_PROD), RLX_AGENT)) < 48u) {
      __builtin_amdgcn_s_sleep(4);
      if (++spins > SPIN_CAP) return false;
    }
    WSTAMP(232, h, 1);
    sc[1] = xa + XT_OFF + cp * 128;
    sc[4] = xb + XO_OFF + piece;
    sc[5] = OFF_QKV + h * QKV_H;
    sc[6] = OFF_OT + (h * 8 + w) * 512;
    if (!qk_loop(W, wsr(p), sc)) return false;
    bar();
    if (w == 0) post(kparams(), R_QKEND + h, 64);
    WSTAMP(232, h, 2);
  } else {
    const int l = lane_id();
    const rsrc_t rv = mkrsrc(p->Wv + h * DH, W_BYTES - h * DH * 2), ro = mkrsrc(p->Wo, W_BYTES);
    load8(lds, w, l, [&](int i, LAS char* dst) { wstage(rv, i * 64 * 8192, dst, w, l); }, W);
    load8(lds, w, l, [&](int i, LAS char* dst) { wstage(rv, (8 + i) * 64 * 8192, dst, w, l); }, W + 8);
    load8_image(lds, w, l, ro, 2048, [&](int t) { return (512 * w + 64 * t) * 2048 + h * 64; });
#pragma unroll
    for (int t = 0; t < 8; ++t) W[16 + t] = get_frag_tr(lds + CL_W + w * (NLDS * 2048) + l * 8, t);
    WAITL(0);
    SCHED();
    load8_image(lds, w, l, ro, 2048, [&](int t) { return (512 * w + 64 * t) * 2048 + h * 64 + 32; });
    image_to_frags(lds, w, l);
    stage_m0(p, lds, w, l);
    WSTAMP(233, h, 0);
    sc[4] = xb + XO_OFF + cp * XO_COPY + w * 16384;
    sc[5] = xa + (h >> 4) * 16384 + ((2 * h) & 31) * 256;
    sc[1] = h;
    sc[6] = xa + XT_OFF + cp * 128;
    static_assert(OFF_FLAG + R_CHAIN * 1024 == CHAIN_FLAG_OFF, "dev/gen_chain.py");
    if (!vo_loop(W, wsr(p), sc)) return false;
    bar();
  }
  return true;
}

__device__ __forceinline__ void produce(LAS char* lds, int w) {
  int qn = 0;
#pragma nounroll
  for (;;) {
    const KP p = kparams();
    if (w == 0) {
      int code = -1;
      for (; qn < 8; ++qn) {
        const int q = (blockIdx.x + qn) & 7, sh = q < 4 ? 2 : 3;
        unsigned t = 0;
        if (lane_id() == 0) t = __hip_atomic_fetch_add(cword(p, C_TICKET + q), 1u, RLX_AGENT);
        t = __builtin_amdgcn_readfirstlane(t);
        if (t < (16u << sh)) { code = (t >> sh) * 64 + (q < 4 ? q * 4 : 16 + (q - 4) * 8) + (t & ((1 << sh) - 1)); break; }
      }
      *(LAS volatile int*)lds = code;
    }
    WAITL(0);
    bar();
    const int code = __builtin_amdgcn_readfirstlane(*(LAS volatile int*)lds);
    WAITL(0);
    bar();
    if (code < 0) return;
    const int rb = code >> 6, cb = code & 63;
    WSTAMP(240 + rb, cb, 0);
    {
      typedef const __attribute__((address_space(4))) char* KC;
      GemmArgs ga;
      ga.ra = wsr(p); ga.rb = mkrsrc(*(const bf16_t* const __attribute__((address_space(4)))*)((KC)p + 16 + (cb >> 4) * 8), W_BYTES);
      ga.a_off = sgpr(OFF_H + rb * 128 * 2048); ga.lda = 2048; ga.b_off = sgpr((cb & 15) * 512); ga.ldb = 8192; ga.nk = 16;
      gemm_tile<128, 256, 2, 4, 3>(lds, w, ga);
    }
    epi_qkv(kparams(), lds, w, rb, cb);
    WAITV(0);
    WAITL(0);
    bar();
    if (w == 0 && lane_id() == 0) __hip_atomic_fetch_add(cword(kparams(), C_PROD + rb), 1u, RLX_AGENT);
    WSTAMP(240 + rb, cb, 1);
  }
}
static_assert(__builtin_offsetof(Params, Wq) == 16 && __builtin_offsetof(Params, Wv) == 32, "argument offsets");

__device__ __forceinline__ const unsigned* flagp(KP p, int dep, int n) {
  return (const unsigned*)(p->ws + OFF_FLAG + ((dep + min(lane_id(), n - 1)) * 8 + (blockIdx.x & 7)) * 128);
}
__device__ __forceinline__ void peek_load(unsigned& d, const unsigned* fp) { asm volatile("global_load_dword %0, %1, off sc1" : "+v"(d) : "v"(fp) : "memory"); }
__device__ __forceinline__ int await(KP p, LAS char* word, int w, int dep, int n, int need) {
  if (w == 0) {
    const unsigned* fp = flagp(p, dep, n);
    unsigned v, spins = 0;
#pragma nounroll
    for (;;) {
      v = __hip_atomic_load(fp, RLX_AGENT);
      if (none(v < (unsigned)need)) break;
      __builtin_amdgcn_s_sleep(WSLEEP);
      if (++spins > SPIN_CAP || ((spins & 63u) == 0u && __builtin_amdgcn_readfirstlane(__hip_atomic_load(cword(p, C_TMO), RLX_AGENT)) != 0)) {
        __hip_atomic_store(cword(p, C_TMO), 1u, RLX_AGENT);
        v = 0;
        break;
      }
    }
    *(LAS volatile unsigned*)word = !none(v == 0u) ? 0 : none(v < need + 1u) ? need + 1 : need;
  }
  WAITL(0);
  bar();
  return __builtin_amdgcn_readfirstlane(*(LAS volatile int*)word);
}

struct Stage {
  int role, dep, ndep, nunit;
  const bf16_t* wgt; int wbytes, w_off, ldw;
  int a_off, a_unit, a_kt, a_tile;
  int shift;
  int o_off, o_tile;
};
template <int TN, int NKT, bool GELU  > __device__ __forceinline__ void stage_gemm(LAS char* lds, int w, const Stage& st) {
  const KP p = kparams();
  const rsrc_t rw = wsr(p);
  const int l = lane_id(), r16 = l & 15, g = l >> 4, kh = w >> 2, cg = w & 3;
  u32x4 Wf[2 * NKT][TN];
  {
    const rsrc_t rb = mkrsrc(st.wgt, st.wbytes);
    const int vo = (8 * ((l >> 3) & 3) + 4 * (l >> 5) + ((l >> 1) & 3)) * st.ldw + (l & 1) * 16;
    LAS char* wd = lds + w * 16384;
#pragma unroll
    for (int ni = 0; ni < TN; ++ni) {
#pragma unroll
      for (int k = 0; k < 2 * NKT; ++k) glds<0>(rb, wd + k * 1024, vo, st.w_off + (kh * 2 * NKT + k) * 32 * st.ldw + (cg * TN + ni) * 32);
      WAITV(0);
#pragma unroll
      for (int k = 0; k < 2 * NKT; ++k) { const u32x2 lo = trread(wd + k * 1024 + l * 8), hi = trread(wd + k * 1024 + 512 + l * 8); Wf[k][ni] = u32x4{lo.x, lo.y, hi.x, hi.y}; }
      WAITL(0);
      SCHED();
    }
  }
  int va[2];
  if (GELU) {
    const int row = 8 * cg + (l >> 3);
    va[0] = va[1] = row * 2048 + (((l & 7) ^ ((row >> 1) & 7)) << 4);
  } else {
#pragma unroll
    for (int i = 0; i < 2; ++i) {
      const int rr = 16 * (cg >> 1) + ((l >> 1) & 15) + (st.shift ? 14 + (i ? kh + 1 : -kh - 1) : 0);
      va[i] = (rr >> 4) * st.a_tile + (cg & 1) * 1024 + (l >> 5) * 512 + (rr & 15) * 32 + (l & 1) * 16;
    }
  }
  const int fa = kh * NKT * 4096 + (GELU ? r16 * 128 + ((g ^ (r16 >> 1)) << 4) : (g >> 1) * 512 + r16 * 32 + (g & 1) * 16);
  auto issue = [&](int u) {
    const int so = st.a_off + u * st.a_unit + kh * NKT * st.a_kt;
    LAS char* dst = lds + (u & 1) * 65536 + kh * NKT * 4096 + cg * 1024;
#pragma unroll
    for (int i = 0; i < NKT; ++i) glds<AUX_SC1>(rw, dst + i * 4096, va[i >> 2], so + i * st.a_kt);
  };
  bool have = false, pre = false;
#pragma nounroll
  for (int u = 0; u < st.nunit; ++u) {
    LAS char* scr = lds + 131072 + (u & 1) * 16384;
    TS_DECL;
    TS(0);
    if (!have) {
      const bool fin = st.role < R_W4 && u >= NSEG - 2;
      const int need = st.shift ? min(u + 1, NSEG) : u + 1;
      const int avail = await(p, scr, w, fin ? R_QKEND : st.dep, fin ? 32 : st.ndep, need);
      if (avail == 0) return;
      pre = (avail > need || (st.shift && need == NSEG)) && u + 1 < st.nunit;
    }
    TS(1);
    if (!have) issue(u);
    if (pre) issue(u + 1);
    f32x4 acc[TN][2];
#pragma unroll
    for (int ni = 0; ni < TN; ++ni) { acc[ni][0] = f32x4{0.f, 0.f, 0.f, 0.f}; acc[ni][1] = acc[ni][0]; }
    LAS const char* ab = lds + (u & 1) * 65536 + fa;
    u32x4 Af[2][4][2];
    auto rdc = [&](int c) {
#pragma unroll
      for (int k = 0; k < 4; ++k)
        if (4 * c + k < 2 * NKT) {
          Af[c & 1][k][0] = lds128((LAS const char*)((size_t)ab ^ ((k & 1) * (GELU ? 64 : 1024))) + (2 * c + (k >> 1)) * 4096);
          Af[c & 1][k][1] = lds128((LAS const char*)((size_t)ab ^ ((k & 1) * (GELU ? 64 : 1024))) + (2 * c + (k >> 1)) * 4096 + 2048);
        }
    };
    auto landed = [&](int c) {
      if (!have) {
        constexpr int LEFT[4] = {NKT - 2, NKT - 4, NKT - 6, 0};
        if (pre) { if (c == 0) waitv<LEFT[0] + NKT>(); else if (c == 1) waitv<LEFT[1] + NKT>(); else if (c == 2) waitv<LEFT[2] + NKT>(); else waitv<NKT>(); }
        else { if (c == 0) waitv<LEFT[0]>(); else if (c == 1) waitv<LEFT[1]>(); else if (c == 2) waitv<LEFT[2]>(); else waitv<0>(); }
        bar();
      }
    };
    landed(0);
    rdc(0);
    unsigned peek = 0;
#pragma unroll
    for (int c = 0; c < 4; ++c) {
      if (have && c < 3) rdc(c + 1);
      if (c == 3 && pre && w == 0) peek_load(peek, flagp(p, st.dep, st.ndep));
      SCHED();
#pragma unroll
      for (int k = 0; k < 4; ++k)
        if (4 * c + k < 2 * NKT) {
#pragma unroll
          for (int ni = 0; ni < TN; ++ni) { acc[ni][0] = mfma(Wf[4 * c + k][ni], Af[c & 1][k][0], acc[ni][0]); acc[ni][1] = mfma(Wf[4 * c + k][ni], Af[c & 1][k][1], acc[ni][1]); }
        }
      SCHED();
      if (!have && c < 3) { landed(c + 1); rdc(c + 1); }
      if (have && c == 1) {
        if (pre) waitv<NKT>(); else waitv<0>();
        bar();
        if (w == 7) post(p, st.role, u);
      }
    }
    TS(3);
#pragma unroll
    for (int ni = 0; ni < TN; ++ni) *(LAS f32x4*)(scr + (w * TN + ni) * 1024 + l * 16) = kh ? acc[ni][0] : acc[ni][1];
    LAS volatile int* nw = (LAS volatile int*)(lds + 131072 + ((u + 1) & 1) * 16384);
    asm volatile("s_waitcnt vmcnt(0)" : "+v"(peek)::"memory");
    const bool pre2 = w == 0 && pre && none(peek < (unsigned)(st.shift ? min(u + 3, NSEG) : u + 3)) && u + 2 < st.nunit;
    WAITL(0);
    TS(4);
    bar();
    TS(5);
    if (w == 0) *nw = pre2;
    const int oo = st.o_off + (2 * u + kh) * st.o_tile;
#pragma unroll
    for (int ni = 0; ni < TN; ++ni) {
      f32x4 r = (kh ? acc[ni][1] : acc[ni][0]) + *(LAS const f32x4*)(scr + ((w ^ 4) * TN + ni) * 1024 + l * 16);
      if (GELU) {
        const unsigned u0 = pk(r[0], r[1]), u1 = pk(r[2], r[3]);
        st64(u32x2{pk(gelu_tanh(bflo(u0)), gelu_tanh(bfhi(u0))), pk(gelu_tanh(bflo(u1)), gelu_tanh(bfhi(u1)))}, rw, r16 * 32 + g * 8, oo + (cg * TN + ni) * 512);
      } else {
        st128(__builtin_bit_cast(u32x4, r), rw, r16 * 64 + g * 16, oo + (cg * TN + ni) * 1024);
      }
    }
    TS(6);
    if (!pre) WAITV(0);
    WAITL(0);
    bar();
    if (!pre && w == 7) post(p, st.role, u + 1);
    have = pre;
    pre = __builtin_amdgcn_readfirstlane(*nw) != 0;
    WAITL(0);
    bar();
    TS(2);
    TS_OUT(st.role, u);
  }
}

__device__ __forceinline__ float psum(float v) {
  v += dppf<0xB1>(v); v += dppf<0x4E>(v); v += dppf<0x128>(v);
  return xsum1632(v);
}
__device__ __forceinline__ u32x2 pk4(f32x4 a) { u32x2 u = {pk(a[0], a[1]), pk(a[2], a[3])}; asm("" : "+v"(u)); return u; }
__device__ __forceinline__ f32x4 unpk4(u32x2 u) { return f32x4{bflo(u.x), bfhi(u.x), bflo(u.y), bfhi(u.y)}; }
__device__ __forceinline__ f32x4 rnd4(f32x4 a) { return unpk4(pk4(a)); }
template <int NP, class F> __device__ __forceinline__ void norm_pairs(f32x4 (&v)[NP], LAS char* red, int w, int l, F atbar) {
#pragma unroll
  for (int i = 0; i < NP; ++i) {
    const float x0 = v[i][0];
    const float pa = __builtin_bit_cast(float, __builtin_amdgcn_readlane(__builtin_bit_cast(int, x0), 0));
    const float pb = __builtin_bit_cast(float, __builtin_amdgcn_readlane(__builtin_bit_cast(int, x0), 4));
    const float pv = (l & 4) ? pb : pa;
    const f32x4 d = v[i] - pv;
    const float s = psum(hsum(d)) * (1.0f / 128), q = psum(hsum(d * d));
    if ((l & 59) == 0) *(LAS f32x2*)(red + ((i * 2 + (l >> 2)) * 8 + w) * 8) = f32x2{pv + s, q - 128.0f * s * s};
  }
  atbar(0);
  WAITL(0);
  bar();
  atbar(1);
#pragma unroll
  for (int i = 0; i < NP; ++i) {
    LAS const f32x4* rp = (LAS const f32x4*)(red + (i * 2 + ((l >> 2) & 1)) * 64);
    const f32x4 t0 = rp[0], t1 = rp[1], t2 = rp[2], t3 = rp[3];
    const f32x4 ts = (t0 + t1) + (t2 + t3);
    const float mean = (ts[0] + ts[2]) * 0.125f;
    const f32x4 e0 = f32x4{t0[0], t0[2], t1[0], t1[2]} - mean, e1 = f32x4{t2[0], t2[2], t3[0], t3[2]} - mean;
    const float q = (ts[1] + ts[3]) + 128.0f * hsum(e0 * e0 + e1 * e1);
    v[i] = (v[i] - mean) * __builtin_amdgcn_rsqf(q * (1.0f / DM) + LN_EPS);
  }
}
template <int KIND, int NP> __device__ __forceinline__ void stage_rows(LAS char* lds, int w, int role, int idx) {
  const KP p = kparams();
  const rsrc_t rw = wsr(p), rx = mkrsrc(p->x, ACT_BYTES), ro = mkrsrc(p->out, ACT_BYTES);
  const int l = lane_id(), col = 128 * w + (l >> 3) * 16 + (l & 3) * 4, rl = (l >> 2) & 1, r0 = idx * 2 * NP;
  const float* mod = (const float*)(p->ws + OFF_MOD) + col;
  const f32x4 gate = *(const f32x4*)(mod + (KIND == 0 ? 2 : 5) * DM), sh = *(const f32x4*)(mod + 3 * DM), sc = rnd4(*(const f32x4*)(mod + 4 * DM) + 1.0f);
  asm volatile("s_waitcnt vmcnt(0)" ::"v"(gate), "v"(sh), "v"(sc) : "memory");
  const int vp = (col >> 4) * 1024 + rl * 64 + (l & 3) * 16, vr = rl * 2048 + col * 2, l16 = l * 16;
  constexpr int NU = KIND == 0 ? NSEG : NSEG + 1, NPART = KIND == 1 ? 1 : 4, NOUT = KIND == 0 ? 3 : KIND == 1 ? 2 : 1, ND = NP * NPART + 1;
  constexpr int DEP = KIND == 0 ? R_WO : KIND == 1 ? R_W2 : R_W4, NDEP = KIND == 1 ? 16 : 32;
  auto issue = [&](int u) {
    LAS char* buf = lds + (u & 1) * 65536;
    const int row = u * 32 + r0;
#pragma unroll
    for (int i = 0; i < NP; ++i)
#pragma unroll
      for (int k = 0; k < NPART; ++k)
        glds<AUX_SC1>(rw, buf + ((i * NPART + k) * 8 + w) * 1024, vp, (KIND == 0 ? OFF_P1 : KIND == 1 ? OFF_P2 : OFF_P4) + k * PSTR + (row >> 4) * 65536 + ((row & 15) + 2 * i) * 64);
    if (KIND == 0) glds<0>(rx, buf + 32768 + (w & 3) * 1024, l16, row * 2048 + (w & 3) * 1024);
    if (KIND == 1) glds<AUX_SC1>(rw, buf + 32768 + w * 1024, l16, OFF_H2 + (row - 2) * 2048 + w * 1024);
    if (KIND == 2) glds<AUX_SC1>(rw, buf + 32768 + w * 1024, l16, (w < 4 ? OFF_X1 - 4096 : OFF_Y1 - 4096) + row * 2048 + w * 1024);
  };
  bool have = false, pre = false;
#pragma nounroll
  for (int u = 0; u < NU; ++u) {
    LAS char* buf = lds + (u & 1) * 65536;
    TS_DECL;
    TS(0);
    if (!have) {
      const int avail = await(p, buf + 61440, w, DEP, NDEP, u + 1);
      if (avail == 0) return;
      pre = avail > u + 1 && u + 1 < NU;
      issue(u);
    }
    TS(1);
    if (pre) issue(u + 1);
    if (!have) {
      if (pre) waitv<ND>(); else waitv<0>();
      bar();
    }
    unsigned peek = 0;
    auto early = [&](int after) {
      if (!after) { if (have) { if (pre) waitv<ND>(); else waitv<0>(); } }
      else { if (have && w == 7) post(p, role, u); if (pre && w == 0) peek_load(peek, flagp(p, DEP, NDEP)); }
    };
    if (KIND == 2 && pre && w == 0) peek_load(peek, flagp(p, DEP, NDEP));
    TS(3);
    f32x4 v[NP];
    auto put = [&](int o) {
#pragma unroll
      for (int i = 0; i < NP; ++i) { const u32x2 d = pk4(v[i]); *(LAS u32x2*)(buf + 40960 + (o * NP + i) * 4096 + vr) = d; v[i] = unpk4(d); }
    };
#pragma unroll
    for (int i = 0; i < NP; ++i) {
      LAS const f32x4* pp = (LAS const f32x4*)(buf + (i * NPART * 8 + w) * 1024 + l16);
      f32x4 s = pp[0];
      if (NPART == 4) s = (s + pp[512]) + (pp[1024] + pp[1536]);
      s = rnd4(s);
      if (KIND == 2) s += unpk4(lds64(buf + 32768 + 4096 + vr));
      if (KIND != 1) s *= gate;
      v[i] = s + unpk4(lds64(buf + 32768 + i * 4096 + vr));
    }
    put(NOUT - 1);
    if (KIND == 0) {
      norm_pairs<NP>(v, buf + 57344, w, l, early);
#pragma unroll
      for (int i = 0; i < NP; ++i) v[i] = v[i] * sc + sh;
      put(1);
    }
    if (KIND == 0) { norm_pairs<NP>(v, buf + 59392, w, l, [](int) {}); put(0); }
    if (KIND == 1) { norm_pairs<NP>(v, buf + 59392, w, l, early); put(0); }
    TS(4);
    asm volatile("s_waitcnt vmcnt(0)" : "+v"(peek)::"memory");
    if (w == 0) *(LAS volatile int*)(buf + 61444) = pre && none(peek < u + 3u) && u + 2 < NU;
    WAITL(0);
    bar();
    TS(5);
#pragma unroll
    for (int jj = 0; jj < (NOUT * NP + 1) / 2; ++jj) {
      const int j = jj * 8 + w;
      if (j < NOUT * NP * 4) {
        const int o = j / (NP * 4), rr = (j % (NP * 4)) >> 1, row = u * 32 + r0 + rr;
        const u32x4 d = lds128(buf + 40960 + j * 1024 + l16);
        const int so = row * 2048 + (j & 1) * 1024;
        if (KIND == 0) st128(d, rw, l16, (o == 0 ? OFF_N2 : o == 1 ? OFF_H2 : OFF_X1) + so);
        if (KIND == 1) st128(d, rw, l16, (o == 0 ? OFF_N3 : OFF_Y1) + so);
        if (KIND == 2 && row >= 2 && row < SEQ + 2) st128(d, ro, l16, so - 4096);
      }
    }
    TS(6);
    if (KIND != 2 && !pre) {
      WAITV(0);
      bar();
      if (w == 7) post(p, role, u + 1);
    }
    TS(2);
    TS_OUT(role, u);
    have = pre;
    pre = __builtin_amdgcn_readfirstlane(*(LAS volatile int*)(buf + 61444)) != 0;
  }
}

__device__ __forceinline__ void stream(LAS char* lds, int w) {
  const KP p = kparams();
  const int x = blockIdx.x & 7, i = blockIdx.x >> 3, q = x & 3;
  const int kind = x >= 4 ? (i < 8 ? 0 : i < 16 ? 1 : i < 30 ? 2 : 3) : 4 + ((i - 16) >> 2);
  const int idx = x >= 4 ? (i < 16 ? i & 7 : i < 30 ? q * 14 + i - 16 : q * 2 + i - 30) : q * 4 + (i & 3);
  if (kind == 3) { stage_rows<1, 2>(lds, w, R_LN3 + idx, idx); return; }
  if (kind == 6) { stage_rows<0, 1>(lds, w, R_LN2 + idx, idx); return; }
  if (kind == 7) { stage_rows<2, 1>(lds, w, R_FIN + idx, idx); return; }
  Stage st;
  st.shift = 0;
  if (kind == 0) {
    st.role = R_WO + q * 8 + idx; st.dep = R_CHAIN; st.ndep = 1; st.nunit = NSEG;
    st.wgt = p->Wo; st.wbytes = W_BYTES; st.ldw = 2048; st.w_off = q * 1024 * 2048 + idx * 256;
    st.a_off = OFF_OT + q * 16 * 2048; st.a_unit = 2 * 131072; st.a_kt = 2048; st.a_tile = 131072;
    st.o_off = OFF_P1 + q * PSTR + idx * 8192; st.o_tile = 65536;
    stage_gemm<2, 8, false>(lds, w, st);
    return;
  }
  if (kind == 1) {
    st.role = R_W4 + q * 8 + idx; st.dep = R_W3 + q * 14; st.ndep = 14; st.nunit = NSEG + 1;
    st.wgt = p->w4; st.wbytes = DM * FF * 2; st.ldw = 2048; st.w_off = q * 896 * 2048 + idx * 256;
    st.a_off = OFF_G3 + q * 14 * 2048; st.a_unit = 2 * FF * 32; st.a_kt = 2048; st.a_tile = FF * 32;
    st.o_off = OFF_P4 + q * PSTR + idx * 8192; st.o_tile = 65536;
    stage_gemm<2, 7, false>(lds, w, st);
    return;
  }
  if (kind == 5) {
    st.role = R_W2 + idx; st.dep = R_W1; st.ndep = 16; st.nunit = NSEG + 1; st.shift = 1;
    st.wgt = p->w2; st.wbytes = DM * DM * 2; st.ldw = 2048; st.w_off = idx * 128;
    st.a_off = OFF_G; st.a_unit = 65536; st.a_kt = 2048; st.a_tile = 32768;
    st.o_off = OFF_P2 + idx * 4096; st.o_tile = 65536;
    stage_gemm<1, 8, false>(lds, w, st);
    return;
  }
  const bool w3 = kind == 2;
  st.role = (w3 ? R_W3 : R_W1) + idx; st.dep = w3 ? R_LN3 : R_LN2; st.ndep = w3 ? 8 : 16; st.nunit = w3 ? NSEG + 1 : NSEG;
  st.wgt = w3 ? p->w3 : p->w1; st.wbytes = w3 ? DM * FF * 2 : DM * DM * 2; st.ldw = w3 ? FF * 2 : 2048; st.w_off = idx * 128;
  st.a_off = w3 ? OFF_N3 : OFF_N2; st.a_unit = 65536; st.a_kt = 128; st.a_tile = 0;
  st.o_off = (w3 ? OFF_G3 : OFF_G + 32768) + idx * 2048; st.o_tile = w3 ? FF * 32 : 32768;
  stage_gemm<1, 8, true>(lds, w, st);
}

__global__ __launch_bounds__(512, 2) void k_mega(Params) {
  LAS char* lds = (LAS char*)smem;
  const int b = blockIdx.x, w = __builtin_amdgcn_readfirstlane(threadIdx.x >> 6);
  if ((b & 7) < 4 && (b >> 3) < 16) {
    if (!chain_role(kparams(), (b >> 4) * 4 + (b & 3), (b >> 3) & 1, w, lds)) __hip_atomic_store(cword(kparams(), C_TMO), 1u, RLX_AGENT);
    return;
  }
  produce(lds, w);
  stream(lds, w);
}

extern "C" void kernel_launch(void* const* d_in, const int* in_sizes, int n_in, void* d_out, int out_size, void* d_ws, size_t ws_size,
                              hipStream_t stream) {
  Params p;
  p.x = (const bf16_t*)d_in[0]; p.mem0 = (const bf16_t*)d_in[1]; p.Wq = (const bf16_t*)d_in[2]; p.Wk = (const bf16_t*)d_in[3];
  p.Wv = (const bf16_t*)d_in[4]; p.Wo = (const bf16_t*)d_in[5]; p.w1 = (const bf16_t*)d_in[8]; p.w2 = (const bf16_t*)d_in[9];
  p.w3 = (const bf16_t*)d_in[10]; p.w4 = (const bf16_t*)d_in[11]; p.c = (const bf16_t*)d_in[12]; p.wmod = (const bf16_t*)d_in[13];
  p.out = (bf16_t*)d_out; p.ws = (unsigned char*)d_ws;
  (void)hipFuncSetAttribute((const void*)k_mega, hipFuncAttributeMaxDynamicSharedMemorySize, LDS_BYTES);
  k_pre<<<256, 512, 0, stream>>>(p);
  k_ln1<<<256, 512, 0, stream>>>(p);
  k_mega<<<256, 512, LDS_BYTES, stream>>>(p);
}
```

```cpp
#include <hip/hip_runtime.h>
#include <stdint.h>
#include <type_traits>

#define LAS __attribute__((address_space(3)))
typedef unsigned short bf16_t;
typedef __bf16 bfx8 __attribute__((ext_vector_type(8)));
typedef __bf16 bfx16 __attribute__((ext_vector_type(16)));
typedef __bf16 bfx2 __attribute__((ext_vector_type(2)));
typedef short s16x4 __attribute__((ext_vector_type(4)));
typedef float f32x4 __attribute__((ext_vector_type(4)));
typedef float f32x2 __attribute__((ext_vector_type(2)));
typedef unsigned u32x2 __attribute__((ext_vector_type(2)));
typedef unsigned u32x4 __attribute__((ext_vector_type(4)));
typedef unsigned u32x8 __attribute__((ext_vector_type(8)));
typedef __amdgpu_buffer_rsrc_t rsrc_t;

constexpr int SEQ = 2048, DM = 1024, NH = 32, DH = 128, NSEG = 64, FF = 3584;
constexpr float LN_EPS = 1e-5f;
constexpr unsigned SENT = 0xFFFFFFFFu;
constexpr int AUX_SC1 = 16;

constexpr int ACT_BYTES = SEQ * DM * 2, W_BYTES = DM * NH * DH * 2, WS_BYTES = 256 << 20;
constexpr int OFF_CTRL = 0;
constexpr int OFF_MOD = 65536;
constexpr int OFF_X = 131072, PG = 2097152;
constexpr int XM_SLOT = 131072, XM_COPY = 32768;
constexpr int XV_OFF = 524288, XV_SLOT = 131072;
constexpr int XT_OFF = 786432;
constexpr int XO_OFF = PG;
constexpr int OFF_QKV = OFF_X + 3 * PG;
constexpr int QKV_S = 24576, QKV_H = 64 * QKV_S + 4096;
constexpr int OFF_OT = OFF_QKV + 32 * QKV_H;
constexpr int OFF_H = OFF_OT + SEQ * 4096 * 2;
constexpr int OFF_X1 = OFF_H + ACT_BYTES;
constexpr int OFF_H2 = OFF_X1 + ACT_BYTES;
constexpr int OFF_N2 = OFF_H2 + ACT_BYTES;
constexpr int OFF_G = OFF_N2 + ACT_BYTES;
constexpr int OFF_Y1 = OFF_G + 132 * 32768;
constexpr int OFF_N3 = OFF_Y1 + 2080 * 2048;
constexpr int OFF_G3 = OFF_N3 + 2080 * 2048;
constexpr int PSTR = 130 * 65536;
constexpr int OFF_P1 = OFF_G3 + 130 * FF * 32;
constexpr int OFF_P2 = OFF_P1 + 4 * PSTR;
constexpr int OFF_P4 = OFF_P2 + PSTR;
constexpr int OFF_FLAG = OFF_P4 + 4 * PSTR;
constexpr int OFF_PROD = OFF_FLAG + 262144;
constexpr int OFF_MDBG = OFF_PROD + 65536;
constexpr int WS_END = OFF_MDBG + 65 * 16384;
static_assert(WS_END + (8 << 20) <= WS_BYTES, "workspace");

struct Params {
  const bf16_t *x, *mem0, *Wq, *Wk, *Wv, *Wo, *w1, *w2, *w3, *w4, *c, *wmod;
  bf16_t* out;
  unsigned char* ws;
};
typedef const __attribute__((address_space(4))) Params* KP;
__device__ __forceinline__ KP kparams() { KP k = (KP)__builtin_amdgcn_kernarg_segment_ptr(); asm volatile("" : "+s"(k)); return k; }

__device__ __forceinline__ float bf2f(unsigned short b) { return __builtin_bit_cast(float, (unsigned)b << 16); }
__device__ __forceinline__ float bflo(unsigned u) { return __builtin_bit_cast(float, u << 16); }
__device__ __forceinline__ float bfhi(unsigned u) { return __builtin_bit_cast(float, u & 0xFFFF0000u); }
__device__ __forceinline__ unsigned pk(float lo, float hi) { bfx2 v; v[0] = (__bf16)lo; v[1] = (__bf16)hi; return __builtin_bit_cast(unsigned, v); }
__device__ __forceinline__ float rbf(float x) { return bflo(pk(x, 0.f)); }
__device__ __forceinline__ unsigned san(unsigned u) { return u < 0xFFFFFFFEu ? u : 0xFFFFFFFEu; }
__device__ __forceinline__ f32x4 mfma(u32x4 a, u32x4 b, f32x4 c) {
  return __builtin_amdgcn_mfma_f32_16x16x32_bf16(__builtin_bit_cast(bfx8, a), __builtin_bit_cast(bfx8, b), c, 0, 0, 0);
}
__device__ __forceinline__ f32x4 mfma16(u32x2 a, u32x2 b, f32x4 c) {
  return __builtin_amdgcn_mfma_f32_16x16x16bf16_1k(__builtin_bit_cast(s16x4, a), __builtin_bit_cast(s16x4, b), c, 0, 0, 0);
}
__device__ __forceinline__ f32x4 smfma(u32x4 a, u32x8 b, f32x4 c, int idx) {
  return __builtin_amdgcn_smfmac_f32_16x16x64_bf16(__builtin_bit_cast(bfx8, a), __builtin_bit_cast(bfx16, b), c, idx, 0, 0);
}
__device__ __forceinline__ u32x2 trread(LAS const char* p) { return __builtin_bit_cast(u32x2, __builtin_amdgcn_ds_read_tr16_b64_v4i16((LAS s16x4*)p)); }
__device__ __forceinline__ u32x4 lds128(LAS const char* p) { return *(LAS const u32x4*)p; }
__device__ __forceinline__ u32x2 lds64(LAS const char* p) { return *(LAS const u32x2*)p; }
__device__ __forceinline__ rsrc_t mkrsrc(const void* p, int bytes) { return __builtin_amdgcn_make_buffer_rsrc((void*)p, 0, bytes, 0x00020000); }
__device__ __forceinline__ rsrc_t wsr(KP p) { return mkrsrc(p->ws, WS_BYTES); }
template <int AUX> __device__ __forceinline__ void glds(rsrc_t rs, LAS char* dst, int voff, int soff) {
  const unsigned d = (unsigned)(size_t)dst;
  if (AUX) asm volatile("s_mov_b32 m0, %0\n\ts_nop 0\n\tbuffer_load_dwordx4 %1, %2, %3 offen sc1 lds" ::"s"(d), "v"(voff), "s"(rs), "s"(soff) : "memory");
  else asm volatile("s_mov_b32 m0, %0\n\ts_nop 0\n\tbuffer_load_dwordx4 %1, %2, %3 offen lds" ::"s"(d), "v"(voff), "s"(rs), "s"(soff) : "memory");
}
__device__ __forceinline__ void swap16(float& a, float& b) {
  auto r = __builtin_amdgcn_permlane16_swap(__builtin_bit_cast(unsigned, a), __builtin_bit_cast(unsigned, b), false, false);
  a = __builtin_bit_cast(float, (unsigned)r[0]); b = __builtin_bit_cast(float, (unsigned)r[1]);
}
__device__ __forceinline__ void swap32(float& a, float& b) {
  auto r = __builtin_amdgcn_permlane32_swap(__builtin_bit_cast(unsigned, a), __builtin_bit_cast(unsigned, b), false, false);
  a = __builtin_bit_cast(float, (unsigned)r[0]); b = __builtin_bit_cast(float, (unsigned)r[1]);
}
__device__ __forceinline__ float xsum1632(float v) {
  float a = v, b = v; swap16(a, b); v = a + b;
  a = v; b = v; swap32(a, b); return a + b;
}
__device__ __forceinline__ float xmax1632(float v) {
  float a = v, b = v; swap16(a, b); v = __builtin_fmaxf(a, b);
  a = v; b = v; swap32(a, b); return __builtin_fmaxf(a, b);
}
template <int CTRL> __device__ __forceinline__ float dppf(float v) { return __builtin_bit_cast(float, __builtin_amdgcn_mov_dpp(__builtin_bit_cast(int, v), CTRL, 0xf, 0xf, true)); }
__device__ __forceinline__ float ror8(float v) { return dppf<0x128>(v); }
__device__ __forceinline__ float gelu_tanh(float x) {
  const float u = x * (1.0f + 0.044715f * x * x) * (-2.0f * 0.7978845608028654f * 1.4426950408889634f);
  return x * __builtin_amdgcn_rcpf(1.0f + __builtin_amdgcn_exp2f(u));
}
__device__ __forceinline__ int xoff(KP p) { return (int)((((size_t)(p->ws + OFF_X) + PG - 1) & ~(size_t)(PG - 1)) - (size_t)p->ws); }
__device__ __forceinline__ unsigned xcc_id() { unsigned x; asm volatile("s_getreg_b32 %0, hwreg(HW_REG_XCC_ID)" : "=s"(x)); return x & 7u; }
#define WAITV(n) asm volatile("s_waitcnt vmcnt(" #n ")" ::: "memory")
#define WAITL(n) asm volatile("s_waitcnt lgkmcnt(" #n ")" ::: "memory")
template <int N> __device__ __forceinline__ void waitv() { asm volatile("s_waitcnt vmcnt(%0)" ::"i"(N) : "memory"); }
__device__ __forceinline__ void bar() { asm volatile("" ::: "memory"); __builtin_amdgcn_s_barrier(); asm volatile("" ::: "memory"); }
#define SCHED() __builtin_amdgcn_sched_barrier(0)
__device__ __forceinline__ int opaque(int x) { asm volatile("" : "+v"(x)); return x; }
__device__ __forceinline__ int sgpr(int x) { int r; asm volatile("s_nop 0\n\tv_readfirstlane_b32 %0, %1\n\ts_nop 4" : "=s"(r) : "v"(x)); return r; }
__device__ __forceinline__ u32x4 ld128(rsrc_t rs, int voff, int soff) { return __builtin_amdgcn_raw_buffer_load_b128(rs, voff, soff, AUX_SC1); }
__device__ __forceinline__ u32x2 ld64(rsrc_t rs, int voff, int soff) { return __builtin_amdgcn_raw_buffer_load_b64(rs, voff, soff, AUX_SC1); }
__device__ __forceinline__ void st128(u32x4 v, rsrc_t rs, int voff, int soff) {
  __builtin_amdgcn_raw_buffer_store_b128(v, rs, voff, soff, AUX_SC1);
  asm volatile("s_nop 1" ::"v"(v) : "memory");
}
__device__ __forceinline__ void st64(u32x2 v, rsrc_t rs, int voff, int soff) { __builtin_amdgcn_raw_buffer_store_b64(v, rs, voff, soff, AUX_SC1); }
__device__ __forceinline__ u32x4 pk8(f32x4 a, f32x4 b) { return u32x4{pk(a[0], a[1]), pk(a[2], a[3]), pk(b[0], b[1]), pk(b[2], b[3])}; }
__device__ __forceinline__ void unpk8(u32x4 u, f32x4& a, f32x4& b) {
  a = f32x4{bflo(u[0]), bfhi(u[0]), bflo(u[1]), bfhi(u[1])}; b = f32x4{bflo(u[2]), bfhi(u[2]), bflo(u[3]), bfhi(u[3])};
}
__device__ __forceinline__ void rnd8(f32x4& a, f32x4& b) { unpk8(pk8(a, b), a, b); }
__device__ __forceinline__ void add8(f32x4& a, f32x4& b, u32x4 r) { f32x4 x, y; unpk8(r, x, y); a += x; b += y; }
__device__ __forceinline__ u32x2 pk4(f32x4 a) { u32x2 u = {pk(a[0], a[1]), pk(a[2], a[3])}; asm("" : "+v"(u)); return u; }
__device__ __forceinline__ f32x4 unpk4(u32x2 u) { return f32x4{bflo(u.x), bfhi(u.x), bflo(u.y), bfhi(u.y)}; }
__device__ __forceinline__ f32x4 rnd4(f32x4 a) { return unpk4(pk4(a)); }
__device__ __forceinline__ int lane_id() { int l; asm volatile("v_mbcnt_lo_u32_b32 %0, -1, 0\n\tv_mbcnt_hi_u32_b32 %0, -1, %0" : "=v"(l)); return l; }
#define RLX_AGENT __ATOMIC_RELAXED, __HIP_MEMORY_SCOPE_AGENT

constexpr int C_TMO = 0, C_H = 1  , C_LINES = 17, C_PROBE = 48  ;
constexpr int R_WO = 0, R_W4 = 32, R_W3 = 64, R_LN3 = 120, R_W1 = 128, R_W2 = 144, R_LN2 = 160, R_FIN = 176, R_QK = 192;
__device__ __forceinline__ unsigned* cword(KP p, int line) { return (unsigned*)(p->ws + OFF_CTRL) + line * 32; }
__device__ __forceinline__ bool none(bool x) { return __builtin_amdgcn_ballot_w64(x) == 0ull; }
__device__ __forceinline__ void post(KP p, int role, unsigned v) {
  const int l = lane_id();
  if (l < 8) __hip_atomic_store((unsigned*)(p->ws + OFF_FLAG + (role * 8 + l) * 128), v, RLX_AGENT);
}
constexpr unsigned SPIN_CAP = 400000u;
#define WSLEEP 2
#define DMAALL 1
#define PRIO 1
#define FPOS 12
#define PACEW 1

#define WSTAMPS 0
#define WSTAMP(role, u, k)
#define TS(k)
#define TS_ARG
#define TS_DECL
#define TS_OUT(role, u)

__device__ __forceinline__ void touch(unsigned& d, rsrc_t rs, int voff, int soff) {
  asm volatile("buffer_load_dword %0, %1, %2, %3 offen" : "+v"(d) : "v"(voff), "s"(rs), "s"(soff) : "memory");
}
__global__ __launch_bounds__(512) void k_pre(Params) {
  const KP p = kparams();
  __shared__ float red[128][33];
  const int tid = threadIdx.x, b = blockIdx.x;
  if (b < 192) {
    const int cg = tid & 3, kr = tid >> 2;
    float acc[8];
#pragma unroll
    for (int i = 0; i < 8; ++i) acc[i] = 0.f;
    u32x4 w[8];
#pragma unroll
    for (int it = 0; it < 8; ++it) w[it] = *(const u32x4*)(p->wmod + (size_t)(it * 128 + kr) * 6144 + b * 32 + cg * 8);
    unsigned short cs[8];
#pragma unroll
    for (int it = 0; it < 8; ++it) cs[it] = p->c[it * 128 + kr];
#pragma unroll
    for (int it = 0; it < 8; ++it) {
      const float cv = bf2f(cs[it]);
      const float sv = rbf(cv / (1.0f + __expf(-cv)));
#pragma unroll
      for (int i = 0; i < 4; ++i) { acc[2 * i] += sv * bflo(w[it][i]); acc[2 * i + 1] += sv * bfhi(w[it][i]); }
    }
#pragma unroll
    for (int i = 0; i < 8; ++i) red[kr][cg * 8 + i] = acc[i];
    __syncthreads();
    if (tid < 128) {
      float sum = 0.f;
#pragma unroll
      for (int r = 0; r < 32; ++r) sum += red[(tid >> 5) * 32 + r][tid & 31];
      red[(tid >> 5) * 32][tid & 31] = sum;
    }
    __syncthreads();
    if (tid < 32) ((float*)(p->ws + OFF_MOD))[b * 32 + tid] = rbf((red[0][tid] + red[32][tid]) + (red[64][tid] + red[96][tid]));
  } else if (b < 202) {
    if (tid < 64) {
      const int v = b - 192;
      unsigned char* q = p->ws + xoff(p) + (v / 5) * PG + (v % 5) * 131072 + 1048576;
      if (tid < 10) __hip_atomic_store((unsigned*)(q + (tid & 1) * 8192 + (tid >> 1) * 16384), SENT, RLX_AGENT);
      WAITV(0);
      unsigned best[2] = {1u << 30, 1u << 30};
#pragma unroll
      for (int r = 0; r < 10; ++r) {
        unsigned d; unsigned long long t0, t1;
        asm volatile("s_memtime %0\n\ts_waitcnt lgkmcnt(0)" : "=s"(t0)::"memory");
        asm volatile("global_load_dword %0, %1, off sc1\n\ts_waitcnt vmcnt(0)" : "=v"(d) : "v"(q + (r & 1) * 8192 + (r >> 1) * 16384) : "memory");
        asm volatile("s_memtime %0\n\ts_waitcnt lgkmcnt(0)" : "=s"(t1)::"memory");
        best[r & 1] = min(best[r & 1], (unsigned)(t1 - t0));
      }
      if (tid == 0) cword(p, C_PROBE)[v] = (unsigned)(best[1] < best[0]) ^ (xcc_id() >> 2);
    }
  } else if (b == 202) {
    const u32x4 z = {0u, 0u, 0u, 0u};
#pragma unroll
    for (int i = 0; i < 4; ++i) {
      *(u32x4*)(p->ws + OFF_G + (i * 512 + tid) * 16) = z;
      *(u32x4*)(p->ws + OFF_G + 129 * 32768 + (i * 512 + tid) * 16) = z;
      *(unsigned*)(p->ws + OFF_FLAG + (i * 512 + tid) * 128) = 0u;
    }
    *(unsigned*)(p->ws + OFF_PROD + tid * 128) = 0u;
    if (tid < C_LINES) *cword(p, tid) = 0u;
  } else {
    const u32x4 sv = {SENT, SENT, SENT, SENT};
    unsigned char* x = p->ws + xoff(p);
    for (int i = (b - 203) * 512 + tid; i < (802816 + PG / 2) / 16; i += 53 * 512) {
      const int o = i * 16;
      *(u32x4*)(x + (o < 802816 ? o : o + (PG - 802816))) = sv;
    }
  }
}

__device__ __forceinline__ float wsum(float v) {
  v += dppf<0xB1>(v);
  v += dppf<0x4E>(v);
  v += dppf<0x124>(v);
  v += dppf<0x128>(v);
  return xsum1632(v);
}
struct Row { f32x4 v[4]; };
__device__ __forceinline__ float hsum(f32x4 a) { return (a[0] + a[1]) + (a[2] + a[3]); }
__device__ __forceinline__ void norm_row(Row& r) {
  const float mean = wsum(hsum((r.v[0] + r.v[1]) + (r.v[2] + r.v[3]))) * (1.0f / DM);
  f32x4 q = {0.f, 0.f, 0.f, 0.f};
#pragma unroll
  for (int i = 0; i < 4; ++i) { const f32x4 d = r.v[i] - mean; q += d * d; }
  const float sd = rbf(__builtin_amdgcn_sqrtf(rbf(rbf(wsum(hsum(q)) * (1.0f / DM)) + LN_EPS))), mb = rbf(mean), rs = __builtin_amdgcn_rcpf(sd);
#pragma unroll
  for (int i = 0; i < 4; ++i) r.v[i] = rnd4(r.v[i] - mb);
#pragma unroll
  for (int i = 0; i < 4; ++i) { const f32x4 t = r.v[i] * rs; r.v[i] = __builtin_elementwise_fma(__builtin_elementwise_fma(-t, (f32x4)sd, r.v[i]), (f32x4)rs, t); }
}
template <int NR> __device__ __forceinline__ void ln1_rows(KP p, int row, int step) {
  const int l16 = lane_id() * 16;
  u32x4 a[NR], b[NR];
#pragma unroll
  for (int r = 0; r < NR; ++r) {
    const unsigned char* src = (const unsigned char*)p->x + (row + r * step) * 2048 + l16;
    a[r] = *(const u32x4*)src; b[r] = *(const u32x4*)(src + 1024);
  }
  const float* sp = (const float*)(p->ws + OFF_MOD) + l16 / 2;
  Row sc, sh;
#pragma unroll
  for (int i = 0; i < 4; ++i) { sh.v[i] = *(const f32x4*)(sp + (i >> 1) * 512 + (i & 1) * 4); sc.v[i] = rnd4(*(const f32x4*)(sp + DM + (i >> 1) * 512 + (i & 1) * 4) + 1.0f); }
#pragma unroll
  for (int r = 0; r < NR; ++r) {
    Row v;
    unpk8(a[r], v.v[0], v.v[1]); unpk8(b[r], v.v[2], v.v[3]);
    norm_row(v);
#pragma unroll
    for (int i = 0; i < 4; ++i) v.v[i] = rnd4(rnd4(v.v[i]) * sc.v[i]) + sh.v[i];
    a[r] = pk8(v.v[0], v.v[1]); b[r] = pk8(v.v[2], v.v[3]);
  }
#pragma unroll
  for (int r = 0; r < NR; ++r) { st128(a[r], wsr(p), l16, OFF_H + (row + r * step) * 2048); st128(b[r], wsr(p), l16 + 1024, OFF_H + (row + r * step) * 2048); }
}

extern __shared__ __attribute__((aligned(16))) char smem[];

template <int BN, class TILE> __device__ __forceinline__ void qkv_tiles(LAS char* lds, int w, int ntile, TILE tile  ) {
  constexpr int BM = 128, WN = 4, NST = 3, NK = 16, TM = 4, TN = BN / WN / 16;
  constexpr int A_BYTES = BM * 128, B_BYTES = BN * 128, ST_BYTES = A_BYTES + B_BYTES;
  constexpr int LA = BM / 64, LB = BN / 64, LD = LA + LB;
  constexpr int RB = BN * 2;
  constexpr int NMMA = TM * TN, NRD = TM + 2 * TN, BROWS = 8192 / RB;
  typedef const __attribute__((address_space(4))) char* KC;
  const KP p = kparams();
  const rsrc_t rw = wsr(p);
  const int l = lane_id(), r16 = l & 15, g = l >> 4, wm = w / WN, wn = w % WN;
  const int va = (8 * w + (l >> 3)) * 2048 + (((l & 7) ^ ((4 * w + (l >> 4)) & 7)) << 4);
  int vb;
  if (BN == 256) { const int k = 2 * w + (l >> 5), f = (k & 3) | (((k >> 3) & 1) << 2); vb = k * 8192 + (((l & 31) ^ (f << 1)) << 4); }
  else { const int k = 4 * w + (l >> 4), f = (k & 3) | (((k >> 3) & 1) << 2); vb = k * 8192 + (((l & 15) ^ (f << 1)) << 4); }
  auto wptr = [&](int code) { return *(const bf16_t* const __attribute__((address_space(4)))*)((KC)p + 16 + ((code >> 5) & 3) * 8); };
  const bf16_t* lw;
  int la, lb;
  auto aim = [&](int code) { lw = wptr(code); la = OFF_H + (code >> 7) * 128 * 2048; lb = (code & 31) * 256; };
  auto piece = [&](int i, LAS char* dst) {
    if (i < LA) glds<AUX_SC1>(rw, dst + i * 8192, va, la + i * 64 * 2048);
    else glds<0>(mkrsrc(lw, W_BYTES), dst + A_BYTES + (i - LA) * 8192, vb, lb + (i - LA) * BROWS * 8192);
  };
  const int fa = (wm * TM * 16 + r16) * 128 + ((g ^ (r16 >> 1)) << 4);
  int fb[TN];
#pragma unroll
  for (int ni = 0; ni < TN; ++ni) fb[ni] = A_BYTES + (8 * g + (r16 >> 2)) * RB + (((wn * TN + ni) ^ ((r16 >> 2) | ((g & 1) << 2))) << 5) + (r16 & 3) * 8;
  u32x4 A0[TM], A1[TM];
  u32x2 B0[TN][2], B1[TN][2];
  auto rd = [&](int i, LAS const char* base, int ks, u32x4 (&Af)[TM], u32x2 (&Bf)[TN][2]) {
    if (i < 2 * TN) Bf[i >> 1][i & 1] = trread(base + fb[i >> 1] + (32 * ks + 4 * (i & 1)) * RB);
    else Af[i - 2 * TN] = lds128(base + (fa ^ (ks * 64)) + (i - 2 * TN) * 2048);
  };
  auto count = [&](int code) {
    if (l < BN / 128) __hip_atomic_fetch_add((unsigned*)(p->ws + OFF_PROD + ((code >> 7) * 32 + (code & 31) + l) * 128), 1u, RLX_AGENT);
  };
  int code = sgpr(tile(0)), prev = -1;
  aim(code);
#pragma nounroll
  for (int s = 0; s < NST; ++s) {
#pragma unroll
    for (int i = 0; i < LD; ++i) piece(i, lds + s * ST_BYTES + w * 1024);
    la += 128; lb += 64 * 8192;
  }
  waitv<(NST - 1) * LD>();
  bar();
#pragma unroll
  for (int i = 0; i < NRD; ++i) rd(i, lds, 0, A0, B0);
  int buf = 0;
#pragma nounroll
  for (int t = 0; t < ntile; ++t) {
    TS_DECL;
    TS(0);
    const int next = t + 1 < ntile ? sgpr(tile(t + 1)) : -1;
    f32x4 acc[TN][TM];
#pragma unroll
    for (int i = 0; i < TN; ++i)
#pragma unroll
      for (int j = 0; j < TM; ++j) acc[i][j] = f32x4{0.f, 0.f, 0.f, 0.f};
    auto kloop = [&](auto isv) {
      auto mm = [&](int i, const u32x4 (&Af)[TM], const u32x2 (&Bf)[TN][2]) {
        const int mi = i / TN, ni = i % TN;
        const u32x4 bw = {Bf[ni][0].x, Bf[ni][0].y, Bf[ni][1].x, Bf[ni][1].y};
        acc[ni][mi] = isv.value ? mfma(Af[mi], bw, acc[ni][mi]) : mfma(bw, Af[mi], acc[ni][mi]);
      };
#pragma nounroll
      for (int kt = 0; kt < NK; ++kt) {
        LAS const char* cur = lds + buf * ST_BYTES;
#pragma unroll
        for (int i = 0; i < NMMA; ++i) {
          mm(i, A0, B0);
#pragma unroll
          for (int r = 0; r < NRD; ++r) if (r * (NMMA * 3 / 4) / NRD == i) rd(r, cur, 1, A1, B1);
          SCHED();
        }
        if (kt + NST == NK) aim(next);
        const bool more = kt + NST < NK || next >= 0;
        WAITL(0);
        if (next >= 0 || kt + NST <= NK) waitv<(NST - 2) * LD>(); else WAITV(0);
        bar();
        if (kt == 1 && prev >= 0 && w == 0) count(prev);
        LAS char* dst = lds + buf * ST_BYTES + w * 1024;
        buf = buf + 1 == NST ? 0 : buf + 1;
        LAS const char* nxt = lds + buf * ST_BYTES;
#pragma unroll
        for (int i = 0; i < NMMA; ++i) {
          mm(i, A1, B1);
#pragma unroll
          for (int r = 0; r < NRD; ++r) if (r * (NMMA * 3 / 4) / NRD == i) rd(r, nxt, 0, A0, B0);
#pragma unroll
          for (int r = 0; r < LD; ++r) if ((r * NMMA + NMMA / 2) / LD == i && more) piece(r, dst);
          SCHED();
        }
        la += 128; lb += 64 * 8192;
      }
    };
    const int which = (code >> 5) & 3;
    const int so = OFF_QKV + ((code & 31) + (BN == 256 ? wn >> 1 : 0)) * QKV_H + ((code >> 7) * 4 + wm * 2) * QKV_S + which * 8192;
    const int c16 = (wn * TN) & 7;
    if (which == 2) {
      kloop(std::true_type());
      TS(4);
#pragma unroll
      for (int sg = 0; sg < 2; ++sg)
#pragma unroll
        for (int ni = 0; ni < TN; ++ni) st128(pk8(acc[ni][2 * sg], acc[ni][2 * sg + 1]), rw, l * 16, so + sg * QKV_S + (c16 + ni) * 1024);
    } else {
      kloop(std::false_type());
      TS(4);
#pragma unroll
      for (int mi = 0; mi < TM; ++mi)
#pragma unroll
        for (int np = 0; np < TN / 2; ++np) st128(pk8(acc[2 * np][mi], acc[2 * np + 1][mi]), rw, l * 16, so + (mi >> 1) * QKV_S + ((mi & 1) * 4 + (c16 >> 1) + np) * 1024);
    }
    TS(1);
    TS_OUT(240 + (code >> 7), (code & 127) >> 1);
    prev = code;
    code = next;
  }
  WAITV(0);
  WAITL(0);
  bar();
  if (w == 0) count(prev);
}

constexpr int NREG = 24, NLDS = 8;
constexpr int CL_M = 0;
constexpr int CL_KX = 16384;
constexpr int CL_IMG = 24576;
constexpr int CL_PX = 28672;
constexpr int CL_W = 32768;
constexpr int LDS_BYTES = 163840;

__device__ __forceinline__ void wstage(rsrc_t rw, int soff, LAS char* dst, int w, int l) {
  const int r = 4 * w + (l >> 4);
  const int f = (r & 1) | (((r >> 2) & 3) << 1);
  const int v = r * 8192 + (((l & 15) ^ (f << 1)) << 4);
  glds<0>(rw, dst + w * 1024, v, soff);
  glds<0>(rw, dst + 8192 + w * 1024, v, soff + 32 * 8192);
}
__device__ __forceinline__ u32x8 wfrag(LAS const char* img, int nt, int l) {
  const int g = l >> 4, q = (l & 15) >> 2, pp = l & 3;
  const int rx = 16 * (g >> 1) + 4 * (g & 1) + (q & 1) + 8 * (q >> 1);
  const int f = (rx & 1) | (((rx >> 2) & 3) << 1);
  LAS const char* a = img + rx * 256 + ((nt ^ f) << 5) + pp * 8;
  const u32x2 x0 = trread(a), x1 = trread(a + 2 * 256), x2 = trread(a + 32 * 256), x3 = trread(a + 34 * 256);
  return u32x8{x0.x, x0.y, x1.x, x1.y, x2.x, x2.y, x3.x, x3.y};
}
__device__ __forceinline__ void put_frag(LAS char* d, u32x8 f) {
  *(LAS u32x4*)d = u32x4{f[0], f[1], f[2], f[3]};
  *(LAS u32x4*)(d + 1024) = u32x4{f[4], f[5], f[6], f[7]};
}
template <int NB, int NREGB, class SRC> __device__ __forceinline__ void load_ring(LAS char* lds, int w, int l, SRC src, u32x8* out) {
#pragma unroll
  for (int n = 0; n < 8; ++n) src(n, lds + CL_W + n * 16384);
#pragma unroll
  for (int n = 0; n < NB; n += 2) {
    constexpr int dummy = 0; (void)dummy;
    const int left = NB - n - 2 < 6 ? NB - n - 2 : 6;
    if (left == 6) WAITV(12); else if (left == 4) WAITV(8); else if (left == 2) WAITV(4); else WAITV(0);
    bar();
    const u32x8 f0 = wfrag(lds + CL_W + (n & 7) * 16384, w, l), f1 = wfrag(lds + CL_W + ((n + 1) & 7) * 16384, w, l);
    WAITL(0);
    bar();
    if (n + 8 < NB) { src(n + 8, lds + CL_W + (n & 7) * 16384); src(n + 9, lds + CL_W + ((n + 1) & 7) * 16384); }
    if (n < NREGB) { out[n < NREGB ? n : 0] = f0; out[n < NREGB ? n + 1 : 0] = f1; }
    else { put_frag(lds + CL_W + (n & 7) * 16384 + w * 2048 + l * 16, f0); put_frag(lds + CL_W + ((n + 1) & 7) * 16384 + w * 2048 + l * 16, f1); }
    SCHED();
  }
}
__device__ __forceinline__ void stage_m0(KP p, LAS char* lds, int w, int l) {
#pragma unroll
  for (int i = 0; i < 2; ++i)
    *(LAS u32x4*)(lds + CL_M + (2 * w + i) * 1024 + l * 16) = *(const u32x4*)(p->mem0 + (l & 7) * DM + 64 * (2 * w + i) + 16 * (l >> 4) + 8 * ((l >> 3) & 1));
  WAITL(0);
}

__device__ __forceinline__ bool qk_loop(const u32x8 (&W)[NREG], rsrc_t rw, u32x8 sc, int fo, u32x2 po) {
  int ok;
  asm volatile(
    "s_mov_b32 %0, 0\n"
    "s_mov_b32 s95, 0\n"
    "v_mbcnt_lo_u32_b32 v63, -1, 0\n"
    "v_mbcnt_hi_u32_b32 v63, -1, v63\n"
    "v_lshlrev_b32 v0, 4, v63\n"
    "v_and_b32 v62, 15, v63\n"
    "v_lshrrev_b32 v61, 4, v63\n"
    "v_and_b32 v60, 8, v63\n"
    "v_cmp_ne_u32 vcc, 0, v60\n"
    "v_mov_b32 v1, 0x4444\n"
    "v_mov_b32 v60, 0xeeee\n"
    "v_cndmask_b32 v1, v1, v60, vcc\n"
    "s_lshl_b32 s76, s68, 11\n"
    "s_add_u32 s76, s76, 32768\n"
    "v_add_u32 v2, s76, v0\n"
    "s_lshl_b32 s76, s68, 11\n"
    "v_add_u32 v3, s76, v0\n"
    "s_lshl_b32 s76, s68, 9\n"
    "s_add_u32 s76, s76, 24576\n"
    "v_lshlrev_b32 v10, 5, v62\n"
    "v_lshl_add_u32 v10, v61, 3, v10\n"
    "v_add_u32 v10, s76, v10\n"
    "v_and_b32 v11, 31, v63\n"
    "v_lshlrev_b32 v11, 3, v11\n"
    "v_and_b32 v60, 7, v63\n"
    "v_lshlrev_b32 v60, 4, v60\n"
    "v_lshl_add_u32 v12, v61, 7, v60\n"
    "v_add_u32 v12, 28672, v12\n"
    "v_and_b32 v59, 1, v61\n"
    "v_lshl_add_u32 v13, v59, 3, v60\n"
    "v_add_u32 v13, 29184, v13\n"
    "v_and_b32 v14, 3, v63\n"
    "v_lshlrev_b32 v14, 4, v14\n"
    "v_lshl_add_u32 v14, v59, 3, v14\n"
    "v_lshrrev_b32 v58, 1, v61\n"
    "v_lshl_add_u32 v14, v58, 6, v14\n"
    "v_lshrrev_b32 v57, 2, v62\n"
    "v_lshl_add_u32 v14, v57, 16, v14\n"
    "v_cmp_le_u32 s[78:79], 32, v63\n"
    "s_lshl_b32 s77, s68, 10\n"
    "s_add_u32 s98, s77, 16384\n"
    "s_sub_u32 s76, s68, 1\n"
    "s_lshl_b32 s76, s76, 4\n"
    "v_lshlrev_b32 v60, 2, v61\n"
    "v_sub_u32 v60, v62, v60\n"
    "v_add_u32 v60, s76, v60\n"
    "v_cmp_gt_i32 s[40:41], 0, v60\n"
    "v_cmp_gt_i32 s[48:49], 16, v60\n"
    "v_cmp_gt_i32 s[42:43], 1, v60\n"
    "v_cmp_gt_i32 s[50:51], 17, v60\n"
    "v_cmp_gt_i32 s[44:45], 2, v60\n"
    "v_cmp_gt_i32 s[52:53], 18, v60\n"
    "v_cmp_gt_i32 s[46:47], 3, v60\n"
    "v_cmp_gt_i32 s[54:55], 19, v60\n"
    "s_mov_b32 s80, 0\n"
    "s_mov_b32 s81, s73\n"
    "s_mov_b32 s82, s74\n"
    "s_mov_b32 s86, 0x3e0293ee\n"
    "s_mov_b32 s87, 0x3db504f3\n"
    "s_mov_b32 s88, 0x3fb8aa3b\n"
    "s_add_u32 s76, s81, s77\n"
    "s_add_u32 s76, s76, 8192\n"
    "s_mov_b32 m0, s98\n"
    "s_nop 0\n"
    "buffer_load_dwordx4 v0, s[64:67], s76 offen sc1 lds\n"
    "s_add_u32 s76, s76, 8192\n"
    "buffer_load_dwordx4 v[4:7], v0, s[64:67], s76 offen sc1\n"
    "ds_read_b128 v[56:59], v3\n"
    "ds_read_b128 v[60:63], v3 offset:1024\n"
    "s_waitcnt vmcnt(0)\n"
    "s_memrealtime s[96:97]\n"
    "v_mov_b32 v16, 29312\n"
    "s_waitcnt lgkmcnt(0)\n"
    "v_mov_b32 v18, s96\n"
    "v_mov_b32 v19, s96\n"
    "v_mov_b32 v20, 0\n"
    "ds_write_b64 v16, v[18:19]\n"
    "ds_write_b32 v16, v20 offset:16\n"
    "s_getpc_b64 s[56:57]\n"
    "pc0_%=:\n"
    "s_mul_i32 s76, s68, var1_%=-var0_%=\n"
    "s_add_u32 s76, s76, var0_%=-pc0_%=\n"
    "s_add_u32 s56, s56, s76\n"
    "s_addc_u32 s57, s57, 0\n"
    "s_and_b32 s76, s80, 1\n"
    "s_lshl_b32 s76, s76, 17\n"
    "s_add_u32 s83, s71, s76\n"
    "s_and_b32 s76, s80, 3\n"
    "s_lshl_b32 s76, s76, 18\n"
    "s_add_u32 s84, s72, s76\n"
    "s_mov_b32 s94, 0\n"
    "v_and_b32 v9, 0x33f, v0\n"
    "v_bfe_u32 v8, v0, 6, 1\n"
    "v_lshl_add_u32 v9, v8, 7, v9\n"
    "v_bfe_u32 v8, v0, 7, 1\n"
    "v_lshl_add_u32 v9, v8, 6, v9\n"
    "v_mov_b64 v[16:17], 0\n"
    "v_mov_b64 v[18:19], 0\n"
    "v_mov_b64 v[20:21], 0\n"
    "v_mov_b64 v[22:23], 0\n"
    "v_mov_b32 v8, s80\n"
    "v_add_u32 v15, 0x10000, v2\n"
    "s_cmp_eq_u32 s80, s80\n"
    "s_nop 0\n"
    "s_setpc_b64 s[56:57]\n"
    "var0_%=:\n"
    "ds_read_b128 v[40:43], v2 offset:0\n"
    "ds_read_b128 v[44:47], v2 offset:1024\n"
    "ds_read_b128 v[48:51], v2 offset:16384\n"
    "ds_read_b128 v[52:55], v2 offset:17408\n"
    "s_cbranch_scc1 go0_%=\n"
    "s_nop 0\n"
    "mwait0_%=:\n"
    "s_memrealtime s[96:97]\n"
    "s_waitcnt lgkmcnt(0)\n"
    "s_sub_u32 s96, s92, s96\n"
    "s_sub_u32 s96, s96, 1\n"
    "s_cmp_lt_u32 s96, 1000\n"
    "s_cbranch_scc1 mwait0_%=\n"
    "mpoll0_%=:\n"
    "buffer_load_dwordx4 v[56:59], v9, s[64:67], s91 offen sc1\n"
    "buffer_load_dwordx4 v[60:63], v9, s[64:67], s91 offen offset:1024 sc1\n"
    "s_waitcnt vmcnt(1)\n"
    "ds_write_b128 v3, v[56:59]\n"
    "v_max3_u32 v24, v56, v57, v58\n"
    "s_waitcnt vmcnt(0)\n"
    "ds_write_b128 v3, v[60:63] offset:1024\n"
    "v_max3_u32 v24, v24, v59, v60\n"
    "v_max3_u32 v24, v24, v61, v62\n"
    "v_max_u32 v24, v24, v63\n"
    "v_cmp_eq_u32 vcc, -1, v24\n"
    "s_cbranch_vccnz mretry0_%=\n"
    "go0_%=:\n"
    "s_waitcnt lgkmcnt(0)\n"
    "s_barrier\n"
    "ds_read_b128 v[24:27], v0 offset:2048\n"
    "ds_read_b128 v[28:31], v0 offset:3072\n"
    "ds_read_b128 v[32:35], v0 offset:4096\n"
    "ds_read_b128 v[36:39], v0 offset:5120\n"
    "v_smfmac_f32_16x16x64_bf16 v[16:19], v[56:59], v[64:71], v1\n"
    "v_smfmac_f32_16x16x64_bf16 v[20:23], v[56:59], v[192:199], v1\n"
    "ds_read_b128 v[56:59], v0 offset:6144\n"
    "v_smfmac_f32_16x16x64_bf16 v[20:23], v[60:63], v[40:47], v1\n"
    "ds_read_b128 v[40:43], v2 offset:32768\n"
    "v_smfmac_f32_16x16x64_bf16 v[16:19], v[60:63], v[128:135], v1\n"
    "ds_read_b128 v[44:47], v2 offset:33792\n"
    "s_waitcnt lgkmcnt(6)\n"
    "v_smfmac_f32_16x16x64_bf16 v[16:19], v[24:27], v[72:79], v1\n"
    "ds_read_b128 v[60:63], v0 offset:7168\n"
    "v_smfmac_f32_16x16x64_bf16 v[20:23], v[24:27], v[200:207], v1\n"
    "ds_read_b128 v[24:27], v0 offset:8192\n"
    "s_waitcnt lgkmcnt(7)\n"
    "v_smfmac_f32_16x16x64_bf16 v[20:23], v[28:31], v[48:55], v1\n"
    "ds_read_b128 v[48:51], v2 offset:49152\n"
    "v_smfmac_f32_16x16x64_bf16 v[16:19], v[28:31], v[136:143], v1\n"
    "ds_read_b128 v[52:55], v2 offset:50176\n"
    "s_waitcnt lgkmcnt(8)\n"
    "v_smfmac_f32_16x16x64_bf16 v[16:19], v[32:35], v[80:87], v1\n"
    "ds_read_b128 v[28:31], v0 offset:9216\n"
    "v_smfmac_f32_16x16x64_bf16 v[20:23], v[32:35], v[208:215], v1\n"
    "ds_read_b128 v[32:35], v0 offset:10240\n"
    "s_waitcnt lgkmcnt(9)\n"
    "s_waitcnt lgkmcnt(6)\n"
    "v_smfmac_f32_16x16x64_bf16 v[20:23], v[36:39], v[40:47], v1\n"
    "ds_read_b128 v[40:43], v15 offset:0\n"
    "v_smfmac_f32_16x16x64_bf16 v[16:19], v[36:39], v[144:151], v1\n"
    "ds_read_b128 v[44:47], v15 offset:1024\n"
    "v_smfmac_f32_16x16x64_bf16 v[16:19], v[56:59], v[88:95], v1\n"
    "ds_read_b128 v[36:39], v0 offset:11264\n"
    "v_smfmac_f32_16x16x64_bf16 v[20:23], v[56:59], v[216:223], v1\n"
    "ds_read_b128 v[56:59], v0 offset:12288\n"
    "s_waitcnt lgkmcnt(9)\n"
    "s_waitcnt lgkmcnt(6)\n"
    "v_smfmac_f32_16x16x64_bf16 v[20:23], v[60:63], v[48:55], v1\n"
    "ds_read_b128 v[48:51], v15 offset:16384\n"
    "v_smfmac_f32_16x16x64_bf16 v[16:19], v[60:63], v[152:159], v1\n"
    "ds_read_b128 v[52:55], v15 offset:17408\n"
    "v_smfmac_f32_16x16x64_bf16 v[16:19], v[24:27], v[96:103], v1\n"
    "ds_read_b128 v[60:63], v0 offset:13312\n"
    "v_smfmac_f32_16x16x64_bf16 v[20:23], v[24:27], v[224:231], v1\n"
    "ds_read_b128 v[24:27], v0 offset:14336\n"
    "s_waitcnt lgkmcnt(9)\n"
    "s_waitcnt lgkmcnt(6)\n"
    "v_smfmac_f32_16x16x64_bf16 v[20:23], v[28:31], v[40:47], v1\n"
    "ds_read_b128 v[40:43], v15 offset:32768\n"
    "v_smfmac_f32_16x16x64_bf16 v[16:19], v[28:31], v[160:167], v1\n"
    "ds_read_b128 v[44:47], v15 offset:33792\n"
    "v_smfmac_f32_16x16x64_bf16 v[16:19], v[32:35], v[104:111], v1\n"
    "ds_read_b128 v[28:31], v0 offset:15360\n"
    "v_smfmac_f32_16x16x64_bf16 v[20:23], v[32:35], v[232:239], v1\n"
    "s_waitcnt lgkmcnt(8)\n"
    "s_waitcnt lgkmcnt(5)\n"
    "v_smfmac_f32_16x16x64_bf16 v[20:23], v[36:39], v[48:55], v1\n"
    "ds_read_b128 v[48:51], v15 offset:49152\n"
    "v_smfmac_f32_16x16x64_bf16 v[16:19], v[36:39], v[168:175], v1\n"
    "ds_read_b128 v[52:55], v15 offset:50176\n"
    "v_smfmac_f32_16x16x64_bf16 v[16:19], v[56:59], v[112:119], v1\n"
    "v_smfmac_f32_16x16x64_bf16 v[20:23], v[56:59], v[240:247], v1\n"
    "s_waitcnt lgkmcnt(6)\n"
    "s_waitcnt lgkmcnt(3)\n"
    "v_smfmac_f32_16x16x64_bf16 v[20:23], v[60:63], v[40:47], v1\n"
    "v_smfmac_f32_16x16x64_bf16 v[16:19], v[60:63], v[176:183], v1\n"
    "v_smfmac_f32_16x16x64_bf16 v[16:19], v[24:27], v[120:127], v1\n"
    "v_smfmac_f32_16x16x64_bf16 v[20:23], v[24:27], v[248:255], v1\n"
    "s_waitcnt lgkmcnt(2)\n"
    "s_waitcnt lgkmcnt(0)\n"
    "v_smfmac_f32_16x16x64_bf16 v[20:23], v[28:31], v[48:55], v1\n"
    "v_smfmac_f32_16x16x64_bf16 v[16:19], v[28:31], v[184:191], v1\n"
    "s_branch gemvdone_%=\n"
    "mretry0_%=:\n"
    "s_nop 7\n"
    "s_sleep 3\n"
    "s_add_u32 s94, s94, 1\n"
    "s_cmp_lt_u32 s94, 400000\n"
    "s_cbranch_scc1 mpoll0_%=\n"
    "s_branch fail_%=\n"
    "var1_%=:\n"
    "ds_read_b128 v[40:43], v2 offset:16384\n"
    "ds_read_b128 v[44:47], v2 offset:17408\n"
    "ds_read_b128 v[48:51], v2 offset:0\n"
    "ds_read_b128 v[52:55], v2 offset:1024\n"
    "s_cbranch_scc1 go1_%=\n"
    "s_nop 0\n"
    "mwait1_%=:\n"
    "s_memrealtime s[96:97]\n"
    "s_waitcnt lgkmcnt(0)\n"
    "s_sub_u32 s96, s92, s96\n"
    "s_sub_u32 s96, s96, 1\n"
    "s_cmp_lt_u32 s96, 1000\n"
    "s_cbranch_scc1 mwait1_%=\n"
    "mpoll1_%=:\n"
    "buffer_load_dwordx4 v[56:59], v9, s[64:67], s91 offen sc1\n"
    "buffer_load_dwordx4 v[60:63], v9, s[64:67], s91 offen offset:1024 sc1\n"
    "s_waitcnt vmcnt(1)\n"
    "ds_write_b128 v3, v[56:59]\n"
    "v_max3_u32 v24, v56, v57, v58\n"
    "s_waitcnt vmcnt(0)\n"
    "ds_write_b128 v3, v[60:63] offset:1024\n"
    "v_max3_u32 v24, v24, v59, v60\n"
    "v_max3_u32 v24, v24, v61, v62\n"
    "v_max_u32 v24, v24, v63\n"
    "v_cmp_eq_u32 vcc, -1, v24\n"
    "s_cbranch_vccnz mretry1_%=\n"
    "go1_%=:\n"
    "s_waitcnt lgkmcnt(0)\n"
    "s_barrier\n"
    "ds_read_b128 v[24:27], v0 offset:0\n"
    "ds_read_b128 v[28:31], v0 offset:1024\n"
    "ds_read_b128 v[32:35], v0 offset:4096\n"
    "ds_read_b128 v[36:39], v0 offset:5120\n"
    "v_smfmac_f32_16x16x64_bf16 v[16:19], v[56:59], v[72:79], v1\n"
    "v_smfmac_f32_16x16x64_bf16 v[20:23], v[56:59], v[200:207], v1\n"
    "ds_read_b128 v[56:59], v0 offset:6144\n"
    "v_smfmac_f32_16x16x64_bf16 v[20:23], v[60:63], v[40:47], v1\n"
    "ds_read_b128 v[40:43], v2 offset:32768\n"
    "v_smfmac_f32_16x16x64_bf16 v[16:19], v[60:63], v[136:143], v1\n"
    "ds_read_b128 v[44:47], v2 offset:33792\n"
    "s_waitcnt lgkmcnt(6)\n"
    "v_smfmac_f32_16x16x64_bf16 v[16:19], v[24:27], v[64:71], v1\n"
    "ds_read_b128 v[60:63], v0 offset:7168\n"
    "v_smfmac_f32_16x16x64_bf16 v[20:23], v[24:27], v[192:199], v1\n"
    "ds_read_b128 v[24:27], v0 offset:8192\n"
    "s_waitcnt lgkmcnt(7)\n"
    "v_smfmac_f32_16x16x64_bf16 v[20:23], v[28:31], v[48:55], v1\n"
    "ds_read_b128 v[48:51], v2 offset:49152\n"
    "v_smfmac_f32_16x16x64_bf16 v[16:19], v[28:31], v[128:135], v1\n"
    "ds_read_b128 v[52:55], v2 offset:50176\n"
    "s_waitcnt lgkmcnt(8)\n"
    "v_smfmac_f32_16x16x64_bf16 v[16:19], v[32:35], v[80:87], v1\n"
    "ds_read_b128 v[28:31], v0 offset:9216\n"
    "v_smfmac_f32_16x16x64_bf16 v[20:23], v[32:35], v[208:215], v1\n"
    "ds_read_b128 v[32:35], v0 offset:10240\n"
    "s_waitcnt lgkmcnt(9)\n"
    "s_waitcnt lgkmcnt(6)\n"
    "v_smfmac_f32_16x16x64_bf16 v[20:23], v[36:39], v[40:47], v1\n"
    "ds_read_b128 v[40:43], v15 offset:0\n"
    "v_smfmac_f32_16x16x64_bf16 v[16:19], v[36:39], v[144:151], v1\n"
    "ds_read_b128 v[44:47], v15 offset:1024\n"
    "v_smfmac_f32_16x16x64_bf16 v[16:19], v[56:59], v[88:95], v1\n"
    "ds_read_b128 v[36:39], v0 offset:11264\n"
    "v_smfmac_f32_16x16x64_bf16 v[20:23], v[56:59], v[216:223], v1\n"
    "ds_read_b128 v[56:59], v0 offset:12288\n"
    "s_waitcnt lgkmcnt(9)\n"
    "s_waitcnt lgkmcnt(6)\n"
    "v_smfmac_f32_16x16x64_bf16 v[20:23], v[60:63], v[48:55], v1\n"
    "ds_read_b128 v[48:51], v15 offset:16384\n"
    "v_smfmac_f32_16x16x64_bf16 v[16:19], v[60:63], v[152:159], v1\n"
    "ds_read_b128 v[52:55], v15 offset:17408\n"
    "v_smfmac_f32_16x16x64_bf16 v[16:19], v[24:27], v[96:103], v1\n"
    "ds_read_b128 v[60:63], v0 offset:13312\n"
    "v_smfmac_f32_16x16x64_bf16 v[20:23], v[24:27], v[224:231], v1\n"
    "ds_read_b128 v[24:27], v0 offset:14336\n"
    "s_waitcnt lgkmcnt(9)\n"
    "s_waitcnt lgkmcnt(6)\n"
    "v_smfmac_f32_16x16x64_bf16 v[20:23], v[28:31], v[40:47], v1\n"
    "ds_read_b128 v[40:43], v15 offset:32768\n"
    "v_smfmac_f32_16x16x64_bf16 v[16:19], v[28:31], v[160:167], v1\n"
    "ds_read_b128 v[44:47], v15 offset:33792\n"
    "v_smfmac_f32_16x16x64_bf16 v[16:19], v[32:35], v[104:111], v1\n"
    "ds_read_b128 v[28:31], v0 offset:15360\n"
    "v_smfmac_f32_16x16x64_bf16 v[20:23], v[32:35], v[232:239], v1\n"
    "s_waitcnt lgkmcnt(8)\n"
    "s_waitcnt lgkmcnt(5)\n"
    "v_smfmac_f32_16x16x64_bf16 v[20:23], v[36:39], v[48:55], v1\n"
    "ds_read_b128 v[48:51], v15 offset:49152\n"
    "v_smfmac_f32_16x16x64_bf16 v[16:19], v[36:39], v[168:175], v1\n"
    "ds_read_b128 v[52:55], v15 offset:50176\n"
    "v_smfmac_f32_16x16x64_bf16 v[16:19], v[56:59], v[112:119], v1\n"
    "v_smfmac_f32_16x16x64_bf16 v[20:23], v[56:59], v[240:247], v1\n"
    "s_waitcnt lgkmcnt(6)\n"
    "s_waitcnt lgkmcnt(3)\n"
    "v_smfmac_f32_16x16x64_bf16 v[20:23], v[60:63], v[40:47], v1\n"
    "v_smfmac_f32_16x16x64_bf16 v[16:19], v[60:63], v[176:183], v1\n"
    "v_smfmac_f32_16x16x64_bf16 v[16:19], v[24:27], v[120:127], v1\n"
    "v_smfmac_f32_16x16x64_bf16 v[20:23], v[24:27], v[248:255], v1\n"
    "s_waitcnt lgkmcnt(2)\n"
    "s_waitcnt lgkmcnt(0)\n"
    "v_smfmac_f32_16x16x64_bf16 v[20:23], v[28:31], v[48:55], v1\n"
    "v_smfmac_f32_16x16x64_bf16 v[16:19], v[28:31], v[184:191], v1\n"
    "s_branch gemvdone_%=\n"
    "mretry1_%=:\n"
    "s_nop 7\n"
    "s_sleep 3\n"
    "s_add_u32 s94, s94, 1\n"
    "s_cmp_lt_u32 s94, 400000\n"
    "s_cbranch_scc1 mpoll1_%=\n"
    "s_branch fail_%=\n"
    "var2_%=:\n"
    "ds_read_b128 v[40:43], v2 offset:32768\n"
    "ds_read_b128 v[44:47], v2 offset:33792\n"
    "ds_read_b128 v[48:51], v2 offset:0\n"
    "ds_read_b128 v[52:55], v2 offset:1024\n"
    "s_cbranch_scc1 go2_%=\n"
    "s_nop 0\n"
    "mwait2_%=:\n"
    "s_memrealtime s[96:97]\n"
    "s_waitcnt lgkmcnt(0)\n"
    "s_sub_u32 s96, s92, s96\n"
    "s_sub_u32 s96, s96, 1\n"
    "s_cmp_lt_u32 s96, 1000\n"
    "s_cbranch_scc1 mwait2_%=\n"
    "mpoll2_%=:\n"
    "buffer_load_dwordx4 v[56:59], v9, s[64:67], s91 offen sc1\n"
    "buffer_load_dwordx4 v[60:63], v9, s[64:67], s91 offen offset:1024 sc1\n"
    "s_waitcnt vmcnt(1)\n"
    "ds_write_b128 v3, v[56:59]\n"
    "v_max3_u32 v24, v56, v57, v58\n"
    "s_waitcnt vmcnt(0)\n"
    "ds_write_b128 v3, v[60:63] offset:1024\n"
    "v_max3_u32 v24, v24, v59, v60\n"
    "v_max3_u32 v24, v24, v61, v62\n"
    "v_max_u32 v24, v24, v63\n"
    "v_cmp_eq_u32 vcc, -1, v24\n"
    "s_cbranch_vccnz mretry2_%=\n"
    "go2_%=:\n"
    "s_waitcnt lgkmcnt(0)\n"
    "s_barrier\n"
    "ds_read_b128 v[24:27], v0 offset:0\n"
    "ds_read_b128 v[28:31], v0 offset:1024\n"
    "ds_read_b128 v[32:35], v0 offset:2048\n"
    "ds_read_b128 v[36:39], v0 offset:3072\n"
    "v_smfmac_f32_16x16x64_bf16 v[16:19], v[56:59], v[80:87], v1\n"
    "v_smfmac_f32_16x16x64_bf16 v[20:23], v[56:59], v[208:215], v1\n"
    "ds_read_b128 v[56:59], v0 offset:6144\n"
    "v_smfmac_f32_16x16x64_bf16 v[20:23], v[60:63], v[40:47], v1\n"
    "ds_read_b128 v[40:43], v2 offset:16384\n"
    "v_smfmac_f32_16x16x64_bf16 v[16:19], v[60:63], v[144:151], v1\n"
    "ds_read_b128 v[44:47], v2 offset:17408\n"
    "s_waitcnt lgkmcnt(6)\n"
    "v_smfmac_f32_16x16x64_bf16 v[16:19], v[24:27], v[64:71], v1\n"
    "ds_read_b128 v[60:63], v0 offset:7168\n"
    "v_smfmac_f32_16x16x64_bf16 v[20:23], v[24:27], v[192:199], v1\n"
    "ds_read_b128 v[24:27], v0 offset:8192\n"
    "s_waitcnt lgkmcnt(7)\n"
    "v_smfmac_f32_16x16x64_bf16 v[20:23], v[28:31], v[48:55], v1\n"
    "ds_read_b128 v[48:51], v2 offset:49152\n"
    "v_smfmac_f32_16x16x64_bf16 v[16:19], v[28:31], v[128:135], v1\n"
    "ds_read_b128 v[52:55], v2 offset:50176\n"
    "s_waitcnt lgkmcnt(8)\n"
    "v_smfmac_f32_16x16x64_bf16 v[16:19], v[32:35], v[72:79], v1\n"
    "ds_read_b128 v[28:31], v0 offset:9216\n"
    "v_smfmac_f32_16x16x64_bf16 v[20:23], v[32:35], v[200:207], v1\n"
    "ds_read_b128 v[32:35], v0 offset:10240\n"
    "s_waitcnt lgkmcnt(9)\n"
    "s_waitcnt lgkmcnt(6)\n"
    "v_smfmac_f32_16x16x64_bf16 v[20:23], v[36:39], v[40:47], v1\n"
    "ds_read_b128 v[40:43], v15 offset:0\n"
    "v_smfmac_f32_16x16x64_bf16 v[16:19], v[36:39], v[136:143], v1\n"
    "ds_read_b128 v[44:47], v15 offset:1024\n"
    "v_smfmac_f32_16x16x64_bf16 v[16:19], v[56:59], v[88:95], v1\n"
    "ds_read_b128 v[36:39], v0 offset:11264\n"
    "v_smfmac_f32_16x16x64_bf16 v[20:23], v[56:59], v[216:223], v1\n"
    "ds_read_b128 v[56:59], v0 offset:12288\n"
    "s_waitcnt lgkmcnt(9)\n"
    "s_waitcnt lgkmcnt(6)\n"
    "v_smfmac_f32_16x16x64_bf16 v[20:23], v[60:63], v[48:55], v1\n"
    "ds_read_b128 v[48:51], v15 offset:16384\n"
    "v_smfmac_f32_16x16x64_bf16 v[16:19], v[60:63], v[152:159], v1\n"
    "ds_read_b128 v[52:55], v15 offset:17408\n"
    "v_smfmac_f32_16x16x64_bf16 v[16:19], v[24:27], v[96:103], v1\n"
    "ds_read_b128 v[60:63], v0 offset:13312\n"
    "v_smfmac_f32_16x16x64_bf16 v[20:23], v[24:27], v[224:231], v1\n"
    "ds_read_b128 v[24:27], v0 offset:14336\n"
    "s_waitcnt lgkmcnt(9)\n"
    "s_waitcnt lgkmcnt(6)\n"
    "v_smfmac_f32_16x16x64_bf16 v[20:23], v[28:31], v[40:47], v1\n"
    "ds_read_b128 v[40:43], v15 offset:32768\n"
    "v_smfmac_f32_16x16x64_bf16 v[16:19], v[28:31], v[160:167], v1\n"
    "ds_read_b128 v[44:47], v15 offset:33792\n"
    "v_smfmac_f32_16x16x64_bf16 v[16:19], v[32:35], v[104:111], v1\n"
    "ds_read_b128 v[28:31], v0 offset:15360\n"
    "v_smfmac_f32_16x16x64_bf16 v[20:23], v[32:35], v[232:239], v1\n"
    "s_waitcnt lgkmcnt(8)\n"
    "s_waitcnt lgkmcnt(5)\n"
    "v_smfmac_f32_16x16x64_bf16 v[20:23], v[36:39], v[48:55], v1\n"
    "ds_read_b128 v[48:51], v15 offset:49152\n"
    "v_smfmac_f32_16x16x64_bf16 v[16:19], v[36:39], v[168:175], v1\n"
    "ds_read_b128 v[52:55], v15 offset:50176\n"
    "v_smfmac_f32_16x16x64_bf16 v[16:19], v[56:59], v[112:119], v1\n"
    "v_smfmac_f32_16x16x64_bf16 v[20:23], v[56:59], v[240:247], v1\n"
    "s_waitcnt lgkmcnt(6)\n"
    "s_waitcnt lgkmcnt(3)\n"
    "v_smfmac_f32_16x16x64_bf16 v[20:23], v[60:63], v[40:47], v1\n"
    "v_smfmac_f32_16x16x64_bf16 v[16:19], v[60:63], v[176:183], v1\n"
    "v_smfmac_f32_16x16x64_bf16 v[16:19], v[24:27], v[120:127], v1\n"
    "v_smfmac_f32_16x16x64_bf16 v[20:23], v[24:27], v[248:255], v1\n"
    "s_waitcnt lgkmcnt(2)\n"
    "s_waitcnt lgkmcnt(0)\n"
    "v_smfmac_f32_16x16x64_bf16 v[20:23], v[28:31], v[48:55], v1\n"
    "v_smfmac_f32_16x16x64_bf16 v[16:19], v[28:31], v[184:191], v1\n"
    "s_branch gemvdone_%=\n"
    "mretry2_%=:\n"
    "s_nop 7\n"
    "s_sleep 3\n"
    "s_add_u32 s94, s94, 1\n"
    "s_cmp_lt_u32 s94, 400000\n"
    "s_cbranch_scc1 mpoll2_%=\n"
    "s_branch fail_%=\n"
    "var3_%=:\n"
    "ds_read_b128 v[40:43], v2 offset:49152\n"
    "ds_read_b128 v[44:47], v2 offset:50176\n"
    "ds_read_b128 v[48:51], v2 offset:0\n"
    "ds_read_b128 v[52:55], v2 offset:1024\n"
    "s_cbranch_scc1 go3_%=\n"
    "s_nop 0\n"
    "mwait3_%=:\n"
    "s_memrealtime s[96:97]\n"
    "s_waitcnt lgkmcnt(0)\n"
    "s_sub_u32 s96, s92, s96\n"
    "s_sub_u32 s96, s96, 1\n"
    "s_cmp_lt_u32 s96, 1000\n"
    "s_cbranch_scc1 mwait3_%=\n"
    "mpoll3_%=:\n"
    "buffer_load_dwordx4 v[56:59], v9, s[64:67], s91 offen sc1\n"
    "buffer_load_dwordx4 v[60:63], v9, s[64:67], s91 offen offset:1024 sc1\n"
    "s_waitcnt vmcnt(1)\n"
    "ds_write_b128 v3, v[56:59]\n"
    "v_max3_u32 v24, v56, v57, v58\n"
    "s_waitcnt vmcnt(0)\n"
    "ds_write_b128 v3, v[60:63] offset:1024\n"
    "v_max3_u32 v24, v24, v59, v60\n"
    "v_max3_u32 v24, v24, v61, v62\n"
    "v_max_u32 v24, v24, v63\n"
    "v_cmp_eq_u32 vcc, -1, v24\n"
    "s_cbranch_vccnz mretry3_%=\n"
    "go3_%=:\n"
    "s_waitcnt lgkmcnt(0)\n"
    "s_barrier\n"
    "ds_read_b128 v[24:27], v0 offset:0\n"
    "ds_read_b128 v[28:31], v0 offset:1024\n"
    "ds_read_b128 v[32:35], v0 offset:2048\n"
    "ds_read_b128 v[36:39], v0 offset:3072\n"
    "v_smfmac_f32_16x16x64_bf16 v[16:19], v[56:59], v[88:95], v1\n"
    "v_smfmac_f32_16x16x64_bf16 v[20:23], v[56:59], v[216:223], v1\n"
    "ds_read_b128 v[56:59], v0 offset:4096\n"
    "v_smfmac_f32_16x16x64_bf16 v[20:23], v[60:63], v[40:47], v1\n"
    "ds_read_b128 v[40:43], v2 offset:16384\n"
    "v_smfmac_f32_16x16x64_bf16 v[16:19], v[60:63], v[152:159], v1\n"
    "ds_read_b128 v[44:47], v2 offset:17408\n"
    "s_waitcnt lgkmcnt(6)\n"
    "v_smfmac_f32_16x16x64_bf16 v[16:19], v[24:27], v[64:71], v1\n"
    "ds_read_b128 v[60:63], v0 offset:5120\n"
    "v_smfmac_f32_16x16x64_bf16 v[20:23], v[24:27], v[192:199], v1\n"
    "ds_read_b128 v[24:27], v0 offset:8192\n"
    "s_waitcnt lgkmcnt(7)\n"
    "v_smfmac_f32_16x16x64_bf16 v[20:23], v[28:31], v[48:55], v1\n"
    "ds_read_b128 v[48:51], v2 offset:32768\n"
    "v_smfmac_f32_16x16x64_bf16 v[16:19], v[28:31], v[128:135], v1\n"
    "ds_read_b128 v[52:55], v2 offset:33792\n"
    "s_waitcnt lgkmcnt(8)\n"
    "v_smfmac_f32_16x16x64_bf16 v[16:19], v[32:35], v[72:79], v1\n"
    "ds_read_b128 v[28:31], v0 offset:9216\n"
    "v_smfmac_f32_16x16x64_bf16 v[20:23], v[32:35], v[200:207], v1\n"
    "ds_read_b128 v[32:35], v0 offset:10240\n"
    "s_waitcnt lgkmcnt(9)\n"
    "s_waitcnt lgkmcnt(6)\n"
    "v_smfmac_f32_16x16x64_bf16 v[20:23], v[36:39], v[40:47], v1\n"
    "ds_read_b128 v[40:43], v15 offset:0\n"
    "v_smfmac_f32_16x16x64_bf16 v[16:19], v[36:39], v[136:143], v1\n"
    "ds_read_b128 v[44:47], v15 offset:1024\n"
    "v_smfmac_f32_16x16x64_bf16 v[16:19], v[56:59], v[80:87], v1\n"
    "ds_read_b128 v[36:39], v0 offset:11264\n"
    "v_smfmac_f32_16x16x64_bf16 v[20:23], v[56:59], v[208:215], v1\n"
    "ds_read_b128 v[56:59], v0 offset:12288\n"
    "s_waitcnt lgkmcnt(9)\n"
    "s_waitcnt lgkmcnt(6)\n"
    "v_smfmac_f32_16x16x64_bf16 v[20:23], v[60:63], v[48:55], v1\n"
    "ds_read_b128 v[48:51], v15 offset:16384\n"
    "v_smfmac_f32_16x16x64_bf16 v[16:19], v[60:63], v[144:151], v1\n"
    "ds_read_b128 v[52:55], v15 offset:17408\n"
    "v_smfmac_f32_16x16x64_bf16 v[16:19], v[24:27], v[96:103], v1\n"
    "ds_read_b128 v[60:63], v0 offset:13312\n"
    "v_smfmac_f32_16x16x64_bf16 v[20:23], v[24:27], v[224:231], v1\n"
    "ds_read_b128 v[24:27], v0 offset:14336\n"
    "s_waitcnt lgkmcnt(9)\n"
    "s_waitcnt lgkmcnt(6)\n"
    "v_smfmac_f32_16x16x64_bf16 v[20:23], v[28:31], v[40:47], v1\n"
    "ds_read_b128 v[40:43], v15 offset:32768\n"
    "v_smfmac_f32_16x16x64_bf16 v[16:19], v[28:31], v[160:167], v1\n"
    "ds_read_b128 v[44:47], v15 offset:33792\n"
    "v_smfmac_f32_16x16x64_bf16 v[16:19], v[32:35], v[104:111], v1\n"
    "ds_read_b128 v[28:31], v0 offset:15360\n"
    "v_smfmac_f32_16x16x64_bf16 v[20:23], v[32:35], v[232:239], v1\n"
    "s_waitcnt lgkmcnt(8)\n"
    "s_waitcnt lgkmcnt(5)\n"
    "v_smfmac_f32_16x16x64_bf16 v[20:23], v[36:39], v[48:55], v1\n"
    "ds_read_b128 v[48:51], v15 offset:49152\n"
    "v_smfmac_f32_16x16x64_bf16 v[16:19], v[36:39], v[168:175], v1\n"
    "ds_read_b128 v[52:55], v15 offset:50176\n"
    "v_smfmac_f32_16x16x64_bf16 v[16:19], v[56:59], v[112:119], v1\n"
    "v_smfmac_f32_16x16x64_bf16 v[20:23], v[56:59], v[240:247], v1\n"
    "s_waitcnt lgkmcnt(6)\n"
    "s_waitcnt lgkmcnt(3)\n"
    "v_smfmac_f32_16x16x64_bf16 v[20:23], v[60:63], v[40:47], v1\n"
    "v_smfmac_f32_16x16x64_bf16 v[16:19], v[60:63], v[176:183], v1\n"
    "v_smfmac_f32_16x16x64_bf16 v[16:19], v[24:27], v[120:127], v1\n"
    "v_smfmac_f32_16x16x64_bf16 v[20:23], v[24:27], v[248:255], v1\n"
    "s_waitcnt lgkmcnt(2)\n"
    "s_waitcnt lgkmcnt(0)\n"
    "v_smfmac_f32_16x16x64_bf16 v[20:23], v[28:31], v[48:55], v1\n"
    "v_smfmac_f32_16x16x64_bf16 v[16:19], v[28:31], v[184:191], v1\n"
    "s_branch gemvdone_%=\n"
    "mretry3_%=:\n"
    "s_nop 7\n"
    "s_sleep 3\n"
    "s_add_u32 s94, s94, 1\n"
    "s_cmp_lt_u32 s94, 400000\n"
    "s_cbranch_scc1 mpoll3_%=\n"
    "s_branch fail_%=\n"
    "var4_%=:\n"
    "ds_read_b128 v[40:43], v15 offset:0\n"
    "ds_read_b128 v[44:47], v15 offset:1024\n"
    "ds_read_b128 v[48:51], v2 offset:0\n"
    "ds_read_b128 v[52:55], v2 offset:1024\n"
    "s_cbranch_scc1 go4_%=\n"
    "s_nop 0\n"
    "mwait4_%=:\n"
    "s_memrealtime s[96:97]\n"
    "s_waitcnt lgkmcnt(0)\n"
    "s_sub_u32 s96, s92, s96\n"
    "s_sub_u32 s96, s96, 1\n"
    "s_cmp_lt_u32 s96, 1000\n"
    "s_cbranch_scc1 mwait4_%=\n"
    "mpoll4_%=:\n"
    "buffer_load_dwordx4 v[56:59], v9, s[64:67], s91 offen sc1\n"
    "buffer_load_dwordx4 v[60:63], v9, s[64:67], s91 offen offset:1024 sc1\n"
    "s_waitcnt vmcnt(1)\n"
    "ds_write_b128 v3, v[56:59]\n"
    "v_max3_u32 v24, v56, v57, v58\n"
    "s_waitcnt vmcnt(0)\n"
    "ds_write_b128 v3, v[60:63] offset:1024\n"
    "v_max3_u32 v24, v24, v59, v60\n"
    "v_max3_u32 v24, v24, v61, v62\n"
    "v_max_u32 v24, v24, v63\n"
    "v_cmp_eq_u32 vcc, -1, v24\n"
    "s_cbranch_vccnz mretry4_%=\n"
    "go4_%=:\n"
    "s_waitcnt lgkmcnt(0)\n"
    "s_barrier\n"
    "ds_read_b128 v[24:27], v0 offset:0\n"
    "ds_read_b128 v[28:31], v0 offset:1024\n"
    "ds_read_b128 v[32:35], v0 offset:2048\n"
    "ds_read_b128 v[36:39], v0 offset:3072\n"
    "v_smfmac_f32_16x16x64_bf16 v[16:19], v[56:59], v[96:103], v1\n"
    "v_smfmac_f32_16x16x64_bf16 v[20:23], v[56:59], v[224:231], v1\n"
    "ds_read_b128 v[56:59], v0 offset:4096\n"
    "v_smfmac_f32_16x16x64_bf16 v[20:23], v[60:63], v[40:47], v1\n"
    "ds_read_b128 v[40:43], v2 offset:16384\n"
    "v_smfmac_f32_16x16x64_bf16 v[16:19], v[60:63], v[160:167], v1\n"
    "ds_read_b128 v[44:47], v2 offset:17408\n"
    "s_waitcnt lgkmcnt(6)\n"
    "v_smfmac_f32_16x16x64_bf16 v[16:19], v[24:27], v[64:71], v1\n"
    "ds_read_b128 v[60:63], v0 offset:5120\n"
    "v_smfmac_f32_16x16x64_bf16 v[20:23], v[24:27], v[192:199], v1\n"
    "ds_read_b128 v[24:27], v0 offset:6144\n"
    "s_waitcnt lgkmcnt(7)\n"
    "v_smfmac_f32_16x16x64_bf16 v[20:23], v[28:31], v[48:55], v1\n"
    "ds_read_b128 v[48:51], v2 offset:32768\n"
    "v_smfmac_f32_16x16x64_bf16 v[16:19], v[28:31], v[128:135], v1\n"
    "ds_read_b128 v[52:55], v2 offset:33792\n"
    "s_waitcnt lgkmcnt(8)\n"
    "v_smfmac_f32_16x16x64_bf16 v[16:19], v[32:35], v[72:79], v1\n"
    "ds_read_b128 v[28:31], v0 offset:7168\n"
    "v_smfmac_f32_16x16x64_bf16 v[20:23], v[32:35], v[200:207], v1\n"
    "ds_read_b128 v[32:35], v0 offset:10240\n"
    "s_waitcnt lgkmcnt(9)\n"
    "s_waitcnt lgkmcnt(6)\n"
    "v_smfmac_f32_16x16x64_bf16 v[20:23], v[36:39], v[40:47], v1\n"
    "ds_read_b128 v[40:43], v2 offset:49152\n"
    "v_smfmac_f32_16x16x64_bf16 v[16:19], v[36:39], v[136:143], v1\n"
    "ds_read_b128 v[44:47], v2 offset:50176\n"
    "v_smfmac_f32_16x16x64_bf16 v[16:19], v[56:59], v[80:87], v1\n"
    "ds_read_b128 v[36:39], v0 offset:11264\n"
    "v_smfmac_f32_16x16x64_bf16 v[20:23], v[56:59], v[208:215], v1\n"
    "ds_read_b128 v[56:59], v0 offset:12288\n"
    "s_waitcnt lgkmcnt(9)\n"
    "s_waitcnt lgkmcnt(6)\n"
    "v_smfmac_f32_16x16x64_bf16 v[20:23], v[60:63], v[48:55], v1\n"
    "ds_read_b128 v[48:51], v15 offset:16384\n"
    "v_smfmac_f32_16x16x64_bf16 v[16:19], v[60:63], v[144:151], v1\n"
    "ds_read_b128 v[52:55], v15 offset:17408\n"
    "v_smfmac_f32_16x16x64_bf16 v[16:19], v[24:27], v[88:95], v1\n"
    "ds_read_b128 v[60:63], v0 offset:13312\n"
    "v_smfmac_f32_16x16x64_bf16 v[20:23], v[24:27], v[216:223], v1\n"
    "ds_read_b128 v[24:27], v0 offset:14336\n"
    "s_waitcnt lgkmcnt(9)\n"
    "s_waitcnt lgkmcnt(6)\n"
    "v_smfmac_f32_16x16x64_bf16 v[20:23], v[28:31], v[40:47], v1\n"
    "ds_read_b128 v[40:43], v15 offset:32768\n"
    "v_smfmac_f32_16x16x64_bf16 v[16:19], v[28:31], v[152:159], v1\n"
    "ds_read_b128 v[44:47], v15 offset:33792\n"
    "v_smfmac_f32_16x16x64_bf16 v[16:19], v[32:35], v[104:111], v1\n"
    "ds_read_b128 v[28:31], v0 offset:15360\n"
    "v_smfmac_f32_16x16x64_bf16 v[20:23], v[32:35], v[232:239], v1\n"
    "s_waitcnt lgkmcnt(8)\n"
    "s_waitcnt lgkmcnt(5)\n"
    "v_smfmac_f32_16x16x64_bf16 v[20:23], v[36:39], v[48:55], v1\n"
    "ds_read_b128 v[48:51], v15 offset:49152\n"
    "v_smfmac_f32_16x16x64_bf16 v[16:19], v[36:39], v[168:175], v1\n"
    "ds_read_b128 v[52:55], v15 offset:50176\n"
    "v_smfmac_f32_16x16x64_bf16 v[16:19], v[56:59], v[112:119], v1\n"
    "v_smfmac_f32_16x16x64_bf16 v[20:23], v[56:59], v[240:247], v1\n"
    "s_waitcnt lgkmcnt(6)\n"
    "s_waitcnt lgkmcnt(3)\n"
    "v_smfmac_f32_16x16x64_bf16 v[20:23], v[60:63], v[40:47], v1\n"
    "v_smfmac_f32_16x16x64_bf16 v[16:19], v[60:63], v[176:183], v1\n"
    "v_smfmac_f32_16x16x64_bf16 v[16:19], v[24:27], v[120:127], v1\n"
    "v_smfmac_f32_16x16x64_bf16 v[20:23], v[24:27], v[248:255], v1\n"
    "s_waitcnt lgkmcnt(2)\n"
    "s_waitcnt lgkmcnt(0)\n"
    "v_smfmac_f32_16x16x64_bf16 v[20:23], v[28:31], v[48:55], v1\n"
    "v_smfmac_f32_16x16x64_bf16 v[16:19], v[28:31], v[184:191], v1\n"
    "s_branch gemvdone_%=\n"
    "mretry4_%=:\n"
    "s_nop 7\n"
    "s_sleep 3\n"
    "s_add_u32 s94, s94, 1\n"
    "s_cmp_lt_u32 s94, 400000\n"
    "s_cbranch_scc1 mpoll4_%=\n"
    "s_branch fail_%=\n"
    "var5_%=:\n"
    "ds_read_b128 v[40:43], v15 offset:16384\n"
    "ds_read_b128 v[44:47], v15 offset:17408\n"
    "ds_read_b128 v[48:51], v2 offset:0\n"
    "ds_read_b128 v[52:55], v2 offset:1024\n"
    "s_cbranch_scc1 go5_%=\n"
    "s_nop 0\n"
    "mwait5_%=:\n"
    "s_memrealtime s[96:97]\n"
    "s_waitcnt lgkmcnt(0)\n"
    "s_sub_u32 s96, s92, s96\n"
    "s_sub_u32 s96, s96, 1\n"
    "s_cmp_lt_u32 s96, 1000\n"
    "s_cbranch_scc1 mwait5_%=\n"
    "mpoll5_%=:\n"
    "buffer_load_dwordx4 v[56:59], v9, s[64:67], s91 offen sc1\n"
    "buffer_load_dwordx4 v[60:63], v9, s[64:67], s91 offen offset:1024 sc1\n"
    "s_waitcnt vmcnt(1)\n"
    "ds_write_b128 v3, v[56:59]\n"
    "v_max3_u32 v24, v56, v57, v58\n"
    "s_waitcnt vmcnt(0)\n"
    "ds_write_b128 v3, v[60:63] offset:1024\n"
    "v_max3_u32 v24, v24, v59, v60\n"
    "v_max3_u32 v24, v24, v61, v62\n"
    "v_max_u32 v24, v24, v63\n"
    "v_cmp_eq_u32 vcc, -1, v24\n"
    "s_cbranch_vccnz mretry5_%=\n"
    "go5_%=:\n"
    "s_waitcnt lgkmcnt(0)\n"
    "s_barrier\n"
    "ds_read_b128 v[24:27], v0 offset:0\n"
    "ds_read_b128 v[28:31], v0 offset:1024\n"
    "ds_read_b128 v[32:35], v0 offset:2048\n"
    "ds_read_b128 v[36:39], v0 offset:3072\n"
    "v_smfmac_f32_16x16x64_bf16 v[16:19], v[56:59], v[104:111], v1\n"
    "v_smfmac_f32_16x16x64_bf16 v[20:23], v[56:59], v[232:239], v1\n"
    "ds_read_b128 v[56:59], v0 offset:4096\n"
    "v_smfmac_f32_16x16x64_bf16 v[20:23], v[60:63], v[40:47], v1\n"
    "ds_read_b128 v[40:43], v2 offset:16384\n"
    "v_smfmac_f32_16x16x64_bf16 v[16:19], v[60:63], v[168:175], v1\n"
    "ds_read_b128 v[44:47], v2 offset:17408\n"
    "s_waitcnt lgkmcnt(6)\n"
    "v_smfmac_f32_16x16x64_bf16 v[16:19], v[24:27], v[64:71], v1\n"
    "ds_read_b128 v[60:63], v0 offset:5120\n"
    "v_smfmac_f32_16x16x64_bf16 v[20:23], v[24:27], v[192:199], v1\n"
    "ds_read_b128 v[24:27], v0 offset:6144\n"
    "s_waitcnt lgkmcnt(7)\n"
    "v_smfmac_f32_16x16x64_bf16 v[20:23], v[28:31], v[48:55], v1\n"
    "ds_read_b128 v[48:51], v2 offset:32768\n"
    "v_smfmac_f32_16x16x64_bf16 v[16:19], v[28:31], v[128:135], v1\n"
    "ds_read_b128 v[52:55], v2 offset:33792\n"
    "s_waitcnt lgkmcnt(8)\n"
    "v_smfmac_f32_16x16x64_bf16 v[16:19], v[32:35], v[72:79], v1\n"
    "ds_read_b128 v[28:31], v0 offset:7168\n"
    "v_smfmac_f32_16x16x64_bf16 v[20:23], v[32:35], v[200:207], v1\n"
    "ds_read_b128 v[32:35], v0 offset:8192\n"
    "s_waitcnt lgkmcnt(9)\n"
    "s_waitcnt lgkmcnt(6)\n"
    "v_smfmac_f32_16x16x64_bf16 v[20:23], v[36:39], v[40:47], v1\n"
    "ds_read_b128 v[40:43], v2 offset:49152\n"
    "v_smfmac_f32_16x16x64_bf16 v[16:19], v[36:39], v[136:143], v1\n"
    "ds_read_b128 v[44:47], v2 offset:50176\n"
    "v_smfmac_f32_16x16x64_bf16 v[16:19], v[56:59], v[80:87], v1\n"
    "ds_read_b128 v[36:39], v0 offset:9216\n"
    "v_smfmac_f32_16x16x64_bf16 v[20:23], v[56:59], v[208:215], v1\n"
    "ds_read_b128 v[56:59], v0 offset:12288\n"
    "s_waitcnt lgkmcnt(9)\n"
    "s_waitcnt lgkmcnt(6)\n"
    "v_smfmac_f32_16x16x64_bf16 v[20:23], v[60:63], v[48:55], v1\n"
    "ds_read_b128 v[48:51], v15 offset:0\n"
    "v_smfmac_f32_16x16x64_bf16 v[16:19], v[60:63], v[144:151], v1\n"
    "ds_read_b128 v[52:55], v15 offset:1024\n"
    "v_smfmac_f32_16x16x64_bf16 v[16:19], v[24:27], v[88:95], v1\n"
    "ds_read_b128 v[60:63], v0 offset:13312\n"
    "v_smfmac_f32_16x16x64_bf16 v[20:23], v[24:27], v[216:223], v1\n"
    "ds_read_b128 v[24:27], v0 offset:14336\n"
    "s_waitcnt lgkmcnt(9)\n"
    "s_waitcnt lgkmcnt(6)\n"
    "v_smfmac_f32_16x16x64_bf16 v[20:23], v[28:31], v[40:47], v1\n"
    "ds_read_b128 v[40:43], v15 offset:32768\n"
    "v_smfmac_f32_16x16x64_bf16 v[16:19], v[28:31], v[152:159], v1\n"
    "ds_read_b128 v[44:47], v15 offset:33792\n"
    "v_smfmac_f32_16x16x64_bf16 v[16:19], v[32:35], v[96:103], v1\n"
    "ds_read_b128 v[28:31], v0 offset:15360\n"
    "v_smfmac_f32_16x16x64_bf16 v[20:23], v[32:35], v[224:231], v1\n"
    "s_waitcnt lgkmcnt(8)\n"
    "s_waitcnt lgkmcnt(5)\n"
    "v_smfmac_f32_16x16x64_bf16 v[20:23], v[36:39], v[48:55], v1\n"
    "ds_read_b128 v[48:51], v15 offset:49152\n"
    "v_smfmac_f32_16x16x64_bf16 v[16:19], v[36:39], v[160:167], v1\n"
    "ds_read_b128 v[52:55], v15 offset:50176\n"
    "v_smfmac_f32_16x16x64_bf16 v[16:19], v[56:59], v[112:119], v1\n"
    "v_smfmac_f32_16x16x64_bf16 v[20:23], v[56:59], v[240:247], v1\n"
    "s_waitcnt lgkmcnt(6)\n"
    "s_waitcnt lgkmcnt(3)\n"
    "v_smfmac_f32_16x16x64_bf16 v[20:23], v[60:63], v[40:47], v1\n"
    "v_smfmac_f32_16x16x64_bf16 v[16:19], v[60:63], v[176:183], v1\n"
    "v_smfmac_f32_16x16x64_bf16 v[16:19], v[24:27], v[120:127], v1\n"
    "v_smfmac_f32_16x16x64_bf16 v[20:23], v[24:27], v[248:255], v1\n"
    "s_waitcnt lgkmcnt(2)\n"
    "s_waitcnt lgkmcnt(0)\n"
    "v_smfmac_f32_16x16x64_bf16 v[20:23], v[28:31], v[48:55], v1\n"
    "v_smfmac_f32_16x16x64_bf16 v[16:19], v[28:31], v[184:191], v1\n"
    "s_branch gemvdone_%=\n"
    "mretry5_%=:\n"
    "s_nop 7\n"
    "s_sleep 3\n"
    "s_add_u32 s94, s94, 1\n"
    "s_cmp_lt_u32 s94, 400000\n"
    "s_cbranch_scc1 mpoll5_%=\n"
    "s_branch fail_%=\n"
    "var6_%=:\n"
    "ds_read_b128 v[40:43], v15 offset:32768\n"
    "ds_read_b128 v[44:47], v15 offset:33792\n"
    "ds_read_b128 v[48:51], v2 offset:0\n"
    "ds_read_b128 v[52:55], v2 offset:1024\n"
    "s_cbranch_scc1 go6_%=\n"
    "s_nop 0\n"
    "mwait6_%=:\n"
    "s_memrealtime s[96:97]\n"
    "s_waitcnt lgkmcnt(0)\n"
    "s_sub_u32 s96, s92, s96\n"
    "s_sub_u32 s96, s96, 1\n"
    "s_cmp_lt_u32 s96, 1000\n"
    "s_cbranch_scc1 mwait6_%=\n"
    "mpoll6_%=:\n"
    "buffer_load_dwordx4 v[56:59], v9, s[64:67], s91 offen sc1\n"
    "buffer_load_dwordx4 v[60:63], v9, s[64:67], s91 offen offset:1024 sc1\n"
    "s_waitcnt vmcnt(1)\n"
    "ds_write_b128 v3, v[56:59]\n"
    "v_max3_u32 v24, v56, v57, v58\n"
    "s_waitcnt vmcnt(0)\n"
    "ds_write_b128 v3, v[60:63] offset:1024\n"
    "v_max3_u32 v24, v24, v59, v60\n"
    "v_max3_u32 v24, v24, v61, v62\n"
    "v_max_u32 v24, v24, v63\n"
    "v_cmp_eq_u32 vcc, -1, v24\n"
    "s_cbranch_vccnz mretry6_%=\n"
    "go6_%=:\n"
    "s_waitcnt lgkmcnt(0)\n"
    "s_barrier\n"
    "ds_read_b128 v[24:27], v0 offset:0\n"
    "ds_read_b128 v[28:31], v0 offset:1024\n"
    "ds_read_b128 v[32:35], v0 offset:2048\n"
    "ds_read_b128 v[36:39], v0 offset:3072\n"
    "v_smfmac_f32_16x16x64_bf16 v[16:19], v[56:59], v[112:119], v1\n"
    "v_smfmac_f32_16x16x64_bf16 v[20:23], v[56:59], v[240:247], v1\n"
    "ds_read_b128 v[56:59], v0 offset:4096\n"
    "v_smfmac_f32_16x16x64_bf16 v[20:23], v[60:63], v[40:47], v1\n"
    "ds_read_b128 v[40:43], v2 offset:16384\n"
    "v_smfmac_f32_16x16x64_bf16 v[16:19], v[60:63], v[176:183], v1\n"
    "ds_read_b128 v[44:47], v2 offset:17408\n"
    "s_waitcnt lgkmcnt(6)\n"
    "v_smfmac_f32_16x16x64_bf16 v[16:19], v[24:27], v[64:71], v1\n"
    "ds_read_b128 v[60:63], v0 offset:5120\n"
    "v_smfmac_f32_16x16x64_bf16 v[20:23], v[24:27], v[192:199], v1\n"
    "ds_read_b128 v[24:27], v0 offset:6144\n"
    "s_waitcnt lgkmcnt(7)\n"
    "v_smfmac_f32_16x16x64_bf16 v[20:23], v[28:31], v[48:55], v1\n"
    "ds_read_b128 v[48:51], v2 offset:32768\n"
    "v_smfmac_f32_16x16x64_bf16 v[16:19], v[28:31], v[128:135], v1\n"
    "ds_read_b128 v[52:55], v2 offset:33792\n"
    "s_waitcnt lgkmcnt(8)\n"
    "v_smfmac_f32_16x16x64_bf16 v[16:19], v[32:35], v[72:79], v1\n"
    "ds_read_b128 v[28:31], v0 offset:7168\n"
    "v_smfmac_f32_16x16x64_bf16 v[20:23], v[32:35], v[200:207], v1\n"
    "ds_read_b128 v[32:35], v0 offset:8192\n"
    "s_waitcnt lgkmcnt(9)\n"
    "s_waitcnt lgkmcnt(6)\n"
    "v_smfmac_f32_16x16x64_bf16 v[20:23], v[36:39], v[40:47], v1\n"
    "ds_read_b128 v[40:43], v2 offset:49152\n"
    "v_smfmac_f32_16x16x64_bf16 v[16:19], v[36:39], v[136:143], v1\n"
    "ds_read_b128 v[44:47], v2 offset:50176\n"
    "v_smfmac_f32_16x16x64_bf16 v[16:19], v[56:59], v[80:87], v1\n"
    "ds_read_b128 v[36:39], v0 offset:9216\n"
    "v_smfmac_f32_16x16x64_bf16 v[20:23], v[56:59], v[208:215], v1\n"
    "ds_read_b128 v[56:59], v0 offset:10240\n"
    "s_waitcnt lgkmcnt(9)\n"
    "s_waitcnt lgkmcnt(6)\n"
    "v_smfmac_f32_16x16x64_bf16 v[20:23], v[60:63], v[48:55], v1\n"
    "ds_read_b128 v[48:51], v15 offset:0\n"
    "v_smfmac_f32_16x16x64_bf16 v[16:19], v[60:63], v[144:151], v1\n"
    "ds_read_b128 v[52:55], v15 offset:1024\n"
    "v_smfmac_f32_16x16x64_bf16 v[16:19], v[24:27], v[88:95], v1\n"
    "ds_read_b128 v[60:63], v0 offset:11264\n"
    "v_smfmac_f32_16x16x64_bf16 v[20:23], v[24:27], v[216:223], v1\n"
    "ds_read_b128 v[24:27], v0 offset:14336\n"
    "s_waitcnt lgkmcnt(9)\n"
    "s_waitcnt lgkmcnt(6)\n"
    "v_smfmac_f32_16x16x64_bf16 v[20:23], v[28:31], v[40:47], v1\n"
    "ds_read_b128 v[40:43], v15 offset:16384\n"
    "v_smfmac_f32_16x16x64_bf16 v[16:19], v[28:31], v[152:159], v1\n"
    "ds_read_b128 v[44:47], v15 offset:17408\n"
    "v_smfmac_f32_16x16x64_bf16 v[16:19], v[32:35], v[96:103], v1\n"
    "ds_read_b128 v[28:31], v0 offset:15360\n"
    "v_smfmac_f32_16x16x64_bf16 v[20:23], v[32:35], v[224:231], v1\n"
    "s_waitcnt lgkmcnt(8)\n"
    "s_waitcnt lgkmcnt(5)\n"
    "v_smfmac_f32_16x16x64_bf16 v[20:23], v[36:39], v[48:55], v1\n"
    "ds_read_b128 v[48:51], v15 offset:49152\n"
    "v_smfmac_f32_16x16x64_bf16 v[16:19], v[36:39], v[160:167], v1\n"
    "ds_read_b128 v[52:55], v15 offset:50176\n"
    "v_smfmac_f32_16x16x64_bf16 v[16:19], v[56:59], v[104:111], v1\n"
    "v_smfmac_f32_16x16x64_bf16 v[20:23], v[56:59], v[232:239], v1\n"
    "s_waitcnt lgkmcnt(6)\n"
    "s_waitcnt lgkmcnt(3)\n"
    "v_smfmac_f32_16x16x64_bf16 v[20:23], v[60:63], v[40:47], v1\n"
    "v_smfmac_f32_16x16x64_bf16 v[16:19], v[60:63], v[168:175], v1\n"
    "v_smfmac_f32_16x16x64_bf16 v[16:19], v[24:27], v[120:127], v1\n"
    "v_smfmac_f32_16x16x64_bf16 v[20:23], v[24:27], v[248:255], v1\n"
    "s_waitcnt lgkmcnt(2)\n"
    "s_waitcnt lgkmcnt(0)\n"
    "v_smfmac_f32_16x16x64_bf16 v[20:23], v[28:31], v[48:55], v1\n"
    "v_smfmac_f32_16x16x64_bf16 v[16:19], v[28:31], v[184:191], v1\n"
    "s_branch gemvdone_%=\n"
    "mretry6_%=:\n"
    "s_nop 7\n"
    "s_sleep 3\n"
    "s_add_u32 s94, s94, 1\n"
    "s_cmp_lt_u32 s94, 400000\n"
    "s_cbranch_scc1 mpoll6_%=\n"
    "s_branch fail_%=\n"
    "var7_%=:\n"
    "ds_read_b128 v[40:43], v15 offset:49152\n"
    "ds_read_b128 v[44:47], v15 offset:50176\n"
    "ds_read_b128 v[48:51], v2 offset:0\n"
    "ds_read_b128 v[52:55], v2 offset:1024\n"
    "s_cbranch_scc1 go7_%=\n"
    "s_nop 0\n"
    "mwait7_%=:\n"
    "s_memrealtime s[96:97]\n"
    "s_waitcnt lgkmcnt(0)\n"
    "s_sub_u32 s96, s92, s96\n"
    "s_sub_u32 s96, s96, 1\n"
    "s_cmp_lt_u32 s96, 1000\n"
    "s_cbranch_scc1 mwait7_%=\n"
    "mpoll7_%=:\n"
    "buffer_load_dwordx4 v[56:59], v9, s[64:67], s91 offen sc1\n"
    "buffer_load_dwordx4 v[60:63], v9, s[64:67], s91 offen offset:1024 sc1\n"
    "buffer_load_dwordx2 v[26:27], off, s[64:67], s90 sc1\n"
    "v_mov_b32 v25, 29312\n"
    "s_waitcnt vmcnt(2)\n"
    "ds_write_b128 v3, v[56:59]\n"
    "v_max3_u32 v24, v56, v57, v58\n"
    "s_waitcnt vmcnt(1)\n"
    "ds_write_b128 v3, v[60:63] offset:1024\n"
    "v_max3_u32 v24, v24, v59, v60\n"
    "v_max3_u32 v24, v24, v61, v62\n"
    "v_max_u32 v24, v24, v63\n"
    "s_waitcnt vmcnt(0)\n"
    "ds_write_b64 v25, v[26:27]\n"
    "v_max3_u32 v24, v24, v26, v27\n"
    "v_cmp_eq_u32 vcc, -1, v24\n"
    "s_cbranch_vccnz mretry7_%=\n"
    "go7_%=:\n"
    "s_waitcnt lgkmcnt(0)\n"
    "s_barrier\n"
    "ds_read_b128 v[24:27], v0 offset:0\n"
    "ds_read_b128 v[28:31], v0 offset:1024\n"
    "ds_read_b128 v[32:35], v0 offset:2048\n"
    "ds_read_b128 v[36:39], v0 offset:3072\n"
    "s_memrealtime s[58:59]\n"
    "buffer_store_dword v8, off, s[64:67], s99 sc1\n"
    "v_smfmac_f32_16x16x64_bf16 v[16:19], v[56:59], v[120:127], v1\n"
    "v_smfmac_f32_16x16x64_bf16 v[20:23], v[56:59], v[248:255], v1\n"
    "ds_read_b128 v[56:59], v0 offset:4096\n"
    "v_smfmac_f32_16x16x64_bf16 v[20:23], v[60:63], v[40:47], v1\n"
    "ds_read_b128 v[40:43], v2 offset:16384\n"
    "v_smfmac_f32_16x16x64_bf16 v[16:19], v[60:63], v[184:191], v1\n"
    "ds_read_b128 v[44:47], v2 offset:17408\n"
    "s_waitcnt lgkmcnt(6)\n"
    "v_smfmac_f32_16x16x64_bf16 v[16:19], v[24:27], v[64:71], v1\n"
    "ds_read_b128 v[60:63], v0 offset:5120\n"
    "v_smfmac_f32_16x16x64_bf16 v[20:23], v[24:27], v[192:199], v1\n"
    "ds_read_b128 v[24:27], v0 offset:6144\n"
    "s_waitcnt lgkmcnt(7)\n"
    "v_smfmac_f32_16x16x64_bf16 v[20:23], v[28:31], v[48:55], v1\n"
    "ds_read_b128 v[48:51], v2 offset:32768\n"
    "v_smfmac_f32_16x16x64_bf16 v[16:19], v[28:31], v[128:135], v1\n"
    "ds_read_b128 v[52:55], v2 offset:33792\n"
    "s_waitcnt lgkmcnt(8)\n"
    "v_smfmac_f32_16x16x64_bf16 v[16:19], v[32:35], v[72:79], v1\n"
    "ds_read_b128 v[28:31], v0 offset:7168\n"
    "v_smfmac_f32_16x16x64_bf16 v[20:23], v[32:35], v[200:207], v1\n"
    "ds_read_b128 v[32:35], v0 offset:8192\n"
    "s_waitcnt lgkmcnt(9)\n"
    "s_waitcnt lgkmcnt(6)\n"
    "v_smfmac_f32_16x16x64_bf16 v[20:23], v[36:39], v[40:47], v1\n"
    "ds_read_b128 v[40:43], v2 offset:49152\n"
    "v_smfmac_f32_16x16x64_bf16 v[16:19], v[36:39], v[136:143], v1\n"
    "ds_read_b128 v[44:47], v2 offset:50176\n"
    "v_smfmac_f32_16x16x64_bf16 v[16:19], v[56:59], v[80:87], v1\n"
    "ds_read_b128 v[36:39], v0 offset:9216\n"
    "v_smfmac_f32_16x16x64_bf16 v[20:23], v[56:59], v[208:215], v1\n"
    "ds_read_b128 v[56:59], v0 offset:10240\n"
    "s_waitcnt lgkmcnt(9)\n"
    "s_waitcnt lgkmcnt(6)\n"
    "v_smfmac_f32_16x16x64_bf16 v[20:23], v[60:63], v[48:55], v1\n"
    "ds_read_b128 v[48:51], v15 offset:0\n"
    "v_smfmac_f32_16x16x64_bf16 v[16:19], v[60:63], v[144:151], v1\n"
    "ds_read_b128 v[52:55], v15 offset:1024\n"
    "v_smfmac_f32_16x16x64_bf16 v[16:19], v[24:27], v[88:95], v1\n"
    "ds_read_b128 v[60:63], v0 offset:11264\n"
    "v_smfmac_f32_16x16x64_bf16 v[20:23], v[24:27], v[216:223], v1\n"
    "ds_read_b128 v[24:27], v0 offset:12288\n"
    "s_waitcnt lgkmcnt(9)\n"
    "s_waitcnt lgkmcnt(6)\n"
    "v_smfmac_f32_16x16x64_bf16 v[20:23], v[28:31], v[40:47], v1\n"
    "ds_read_b128 v[40:43], v15 offset:16384\n"
    "v_smfmac_f32_16x16x64_bf16 v[16:19], v[28:31], v[152:159], v1\n"
    "ds_read_b128 v[44:47], v15 offset:17408\n"
    "v_smfmac_f32_16x16x64_bf16 v[16:19], v[32:35], v[96:103], v1\n"
    "ds_read_b128 v[28:31], v0 offset:13312\n"
    "v_smfmac_f32_16x16x64_bf16 v[20:23], v[32:35], v[224:231], v1\n"
    "s_waitcnt lgkmcnt(8)\n"
    "s_waitcnt lgkmcnt(5)\n"
    "v_smfmac_f32_16x16x64_bf16 v[20:23], v[36:39], v[48:55], v1\n"
    "ds_read_b128 v[48:51], v15 offset:32768\n"
    "v_smfmac_f32_16x16x64_bf16 v[16:19], v[36:39], v[160:167], v1\n"
    "ds_read_b128 v[52:55], v15 offset:33792\n"
    "v_smfmac_f32_16x16x64_bf16 v[16:19], v[56:59], v[104:111], v1\n"
    "v_smfmac_f32_16x16x64_bf16 v[20:23], v[56:59], v[232:239], v1\n"
    "s_waitcnt lgkmcnt(6)\n"
    "s_waitcnt lgkmcnt(3)\n"
    "v_smfmac_f32_16x16x64_bf16 v[20:23], v[60:63], v[40:47], v1\n"
    "v_smfmac_f32_16x16x64_bf16 v[16:19], v[60:63], v[168:175], v1\n"
    "v_smfmac_f32_16x16x64_bf16 v[16:19], v[24:27], v[112:119], v1\n"
    "v_smfmac_f32_16x16x64_bf16 v[20:23], v[24:27], v[240:247], v1\n"
    "s_waitcnt lgkmcnt(2)\n"
    "s_waitcnt lgkmcnt(0)\n"
    "v_smfmac_f32_16x16x64_bf16 v[20:23], v[28:31], v[48:55], v1\n"
    "v_smfmac_f32_16x16x64_bf16 v[16:19], v[28:31], v[176:183], v1\n"
    "s_branch gemvdone_%=\n"
    "mretry7_%=:\n"
    "s_nop 7\n"
    "s_sleep 3\n"
    "s_add_u32 s94, s94, 1\n"
    "s_cmp_lt_u32 s94, 400000\n"
    "s_cbranch_scc1 mpoll7_%=\n"
    "s_branch fail_%=\n"
    "gemvdone_%=:\n"
    "s_nop 6\n"
    "v_permlane32_swap_b32 v16, v20\n"
    "v_permlane32_swap_b32 v17, v21\n"
    "v_permlane32_swap_b32 v18, v22\n"
    "v_permlane32_swap_b32 v19, v23\n"
    "v_pk_add_f32 v[16:17], v[16:17], v[20:21]\n"
    "v_pk_add_f32 v[18:19], v[18:19], v[22:23]\n"
    "v_cvt_pk_bf16_f32 v16, v16, v17\n"
    "v_cvt_pk_bf16_f32 v17, v18, v19\n"
    "ds_write_b64 v10, v[16:17]\n"
    "s_cmp_lg_u32 s68, 0\n"
    "s_cbranch_scc1 notw0_%=\n"
    "ds_read_b128 v[44:47], v0 offset:16384\n"
    "ds_read_b128 v[48:51], v0 offset:20480\n"
    "ds_read_b128 v[52:55], v0 offset:17408\n"
    "ds_read_b128 v[56:59], v0 offset:21504\n"
    "ds_read_b128 v[60:63], v0 offset:18432\n"
    "v_lshrrev_b32 v15, 1, v0\n"
    "s_waitcnt lgkmcnt(0)\n"
    "s_barrier\n"
    "ds_read_b64_tr_b16 v[28:29], v15 offset:24576\n"
    "ds_read_b64_tr_b16 v[30:31], v15 offset:25088\n"
    "ds_read_b64_tr_b16 v[32:33], v15 offset:25600\n"
    "ds_read_b64_tr_b16 v[34:35], v15 offset:26112\n"
    "ds_read_b64_tr_b16 v[36:37], v15 offset:26624\n"
    "ds_read_b64_tr_b16 v[38:39], v15 offset:27136\n"
    "ds_read_b64_tr_b16 v[40:41], v15 offset:27648\n"
    "ds_read_b64_tr_b16 v[42:43], v15 offset:28160\n"
    "s_waitcnt lgkmcnt(6)\n"
    "v_mfma_f32_16x16x32_bf16 v[16:19], v[44:47], v[28:31], 0\n"
    "v_mfma_f32_16x16x32_bf16 v[20:23], v[48:51], v[28:31], 0\n"
    "ds_read_b128 v[44:47], v0 offset:22528\n"
    "ds_read_b128 v[48:51], v0 offset:19456\n"
    "buffer_load_dwordx2 v[8:9], v11, s[64:67], s83 offen sc1\n"
    "v_mfma_f32_16x16x32_bf16 v[24:27], v[28:31], v[28:31], 0\n"
    "s_waitcnt lgkmcnt(6)\n"
    "v_mfma_f32_16x16x32_bf16 v[16:19], v[52:55], v[32:35], v[16:19]\n"
    "ds_read_b128 v[52:55], v0 offset:23552\n"
    "v_mfma_f32_16x16x32_bf16 v[20:23], v[56:59], v[32:35], v[20:23]\n"
    "v_mfma_f32_16x16x32_bf16 v[24:27], v[32:35], v[32:35], v[24:27]\n"
    "s_waitcnt lgkmcnt(5)\n"
    "v_mfma_f32_16x16x32_bf16 v[24:27], v[36:39], v[36:39], v[24:27]\n"
    "v_mfma_f32_16x16x32_bf16 v[16:19], v[60:63], v[36:39], v[16:19]\n"
    "s_waitcnt lgkmcnt(3)\n"
    "v_mfma_f32_16x16x32_bf16 v[24:27], v[40:43], v[40:43], v[24:27]\n"
    "s_waitcnt lgkmcnt(2)\n"
    "v_mfma_f32_16x16x32_bf16 v[20:23], v[44:47], v[36:39], v[20:23]\n"
    "s_waitcnt lgkmcnt(1)\n"
    "v_mfma_f32_16x16x32_bf16 v[16:19], v[48:51], v[40:43], v[16:19]\n"
    "s_waitcnt lgkmcnt(0)\n"
    "v_mfma_f32_16x16x32_bf16 v[20:23], v[52:55], v[40:43], v[20:23]\n"
    "v_bfe_u32 v58, v0, 7, 1\n"
    "v_lshl_add_u32 v56, v58, 3, v12\n"
    "v_lshl_add_u32 v57, v58, 1, v13\n"
    "v_lshrrev_b32 v58, 9, v0\n"
    "v_lshl_add_u32 v57, v58, 2, v57\n"
    "v_mov_b32_dpp v24, v25 row_ror:8 row_mask:0xf bank_mask:0xc\n"
    "v_mov_b32_dpp v26, v27 row_ror:8 row_mask:0xf bank_mask:0xc\n"
    "s_nop 1\n"
    "v_permlane32_swap_b32 v24, v26\n"
    "v_mov_b32_dpp v16, v20 row_ror:8 row_mask:0xf bank_mask:0xc\n"
    "v_mov_b32_dpp v17, v21 row_ror:8 row_mask:0xf bank_mask:0xc\n"
    "v_mov_b32_dpp v18, v22 row_ror:8 row_mask:0xf bank_mask:0xc\n"
    "v_mov_b32_dpp v19, v23 row_ror:8 row_mask:0xf bank_mask:0xc\n"
    "v_max3_f32 v20, v26, v16, v17\n"
    "v_max3_f32 v20, v20, v18, v19\n"
    "s_nop 1\n"
    "v_max_f32_dpp v20, v20, v20 row_ror:8 row_mask:0xf bank_mask:0xf\n"
    "v_mov_b32 v21, v20\n"
    "s_nop 1\n"
    "v_permlane16_swap_b32 v20, v21\n"
    "v_max_f32 v20, v20, v21\n"
    "v_mov_b32 v21, v20\n"
    "s_nop 1\n"
    "v_permlane32_swap_b32 v20, v21\n"
    "v_max_f32 v20, v20, v21\n"
    "v_mul_f32 v20, s86, v20\n"
    "v_fma_f32 v16, v16, s86, -v20\n"
    "v_fma_f32 v17, v17, s86, -v20\n"
    "v_fma_f32 v18, v18, s86, -v20\n"
    "v_fma_f32 v19, v19, s86, -v20\n"
    "v_fma_f32 v26, v26, s86, -v20\n"
    "v_exp_f32 v16, v16\n"
    "v_exp_f32 v17, v17\n"
    "v_exp_f32 v18, v18\n"
    "v_exp_f32 v19, v19\n"
    "v_exp_f32 v26, v26\n"
    "v_add_f32 v20, v16, v17\n"
    "v_add_f32 v21, v18, v19\n"
    "v_add_f32 v20, v20, v21\n"
    "v_add_f32 v20, v20, v26\n"
    "s_nop 1\n"
    "v_add_f32_dpp v20, v20, v20 row_ror:8 row_mask:0xf bank_mask:0xf\n"
    "v_mov_b32 v21, v20\n"
    "s_nop 1\n"
    "v_permlane16_swap_b32 v20, v21\n"
    "v_add_f32 v20, v20, v21\n"
    "v_mov_b32 v21, v20\n"
    "s_nop 1\n"
    "v_permlane32_swap_b32 v20, v21\n"
    "v_add_f32 v20, v20, v21\n"
    "v_rcp_f32 v20, v20\n"
    "s_nop 0\n"
    "v_mul_f32 v16, v16, v20\n"
    "v_mul_f32 v17, v17, v20\n"
    "v_mul_f32 v18, v18, v20\n"
    "v_mul_f32 v19, v19, v20\n"
    "v_mul_f32 v26, v26, v20\n"
    "v_cvt_pk_bf16_f32 v16, v16, v17\n"
    "v_cvt_pk_bf16_f32 v17, v18, v19\n"
    "v_cvt_pk_bf16_f32 v26, v26, v26\n"
    "ds_write_b64 v56, v[16:17]\n"
    "ds_write_b16 v57, v26\n"
    "vcheck_%=:\n"
    "s_waitcnt vmcnt(0)\n"
    "vcheck2_%=:\n"
    "v_max_u32 v20, v8, v9\n"
    "v_cmp_eq_u32 vcc, -1, v20\n"
    "s_cbranch_vccnz vretry_%=\n"
    "v_cndmask_b32 v8, v8, 0, s[78:79]\n"
    "v_cndmask_b32 v9, v9, 0, s[78:79]\n"
    "s_waitcnt lgkmcnt(0)\n"
    "s_barrier\n"
    "ds_read_b128 v[16:19], v12\n"
    "ds_read_b64 v[20:21], v13\n"
    "s_waitcnt lgkmcnt(1)\n"
    "v_mfma_f32_16x16x32_bf16 v[24:27], v[4:7], v[16:19], 0\n"
    "s_waitcnt lgkmcnt(0)\n"
    "v_mfma_f32_16x16x16_bf16 v[28:31], v[8:9], v[20:21], 0\n"
    "s_nop 7\n"
    "v_pk_add_f32 v[24:25], v[24:25], v[28:29]\n"
    "v_pk_add_f32 v[26:27], v[26:27], v[30:31]\n"
    "v_cvt_pk_bf16_f32 v24, v24, v25\n"
    "v_cvt_pk_bf16_f32 v25, v26, v27\n"
    "v_min_u32 v24, -2, v24\n"
    "v_min_u32 v25, -2, v25\n"
    "buffer_store_dwordx2 v[24:25], v14, s[64:67], s84 offen sc1\n"
    "s_sub_u32 s76, s68, 1\n"
    "s_memrealtime s[96:97]\n"
    "v_mov_b32 v16, 29312\n"
    "ds_read_b64 v[16:17], v16\n"
    "s_waitcnt lgkmcnt(0)\n"
    "v_readfirstlane_b32 s92, v17\n"
    "s_cmp_lt_u32 s76, 2\n"
    "s_nop 3\n"
    "s_cbranch_scc0 notok_%=\n"
    "v_lshrrev_b32 v15, 1, v0\n"
    "ds_read_b128 v[28:31], v0 offset:16384\n"
    "ds_read_b128 v[32:35], v0 offset:20480\n"
    "ds_read_b64_tr_b16 v[36:37], v15 offset:24576\n"
    "ds_read_b64_tr_b16 v[38:39], v15 offset:25088\n"
    "ds_read_b128 v[40:43], v0 offset:17408\n"
    "ds_read_b128 v[44:47], v0 offset:21504\n"
    "s_waitcnt vmcnt(1)\n"
    "s_waitcnt lgkmcnt(5)\n"
    "v_mfma_f32_16x16x32_bf16 v[16:19], v[28:31], v[48:51], 0\n"
    "ds_read_b64_tr_b16 v[28:29], v15 offset:25600\n"
    "ds_read_b64_tr_b16 v[30:31], v15 offset:26112\n"
    "s_waitcnt lgkmcnt(6)\n"
    "v_mfma_f32_16x16x32_bf16 v[20:23], v[32:35], v[48:51], 0\n"
    "ds_read_b128 v[32:35], v0 offset:18432\n"
    "s_waitcnt lgkmcnt(5)\n"
    "v_mfma_f32_16x16x32_bf16 v[24:27], v[36:39], v[48:51], 0\n"
    "ds_read_b128 v[36:39], v0 offset:22528\n"
    "s_waitcnt lgkmcnt(5)\n"
    "v_mfma_f32_16x16x32_bf16 v[16:19], v[40:43], v[52:55], v[16:19]\n"
    "ds_read_b64_tr_b16 v[40:41], v15 offset:26624\n"
    "ds_read_b64_tr_b16 v[42:43], v15 offset:27136\n"
    "s_waitcnt lgkmcnt(6)\n"
    "v_mfma_f32_16x16x32_bf16 v[20:23], v[44:47], v[52:55], v[20:23]\n"
    "ds_read_b128 v[44:47], v0 offset:19456\n"
    "s_waitcnt lgkmcnt(5)\n"
    "v_mfma_f32_16x16x32_bf16 v[24:27], v[28:31], v[52:55], v[24:27]\n"
    "ds_read_b128 v[28:31], v0 offset:23552\n"
    "s_waitcnt lgkmcnt(5)\n"
    "v_mfma_f32_16x16x32_bf16 v[16:19], v[32:35], v[56:59], v[16:19]\n"
    "ds_read_b64_tr_b16 v[32:33], v15 offset:27648\n"
    "ds_read_b64_tr_b16 v[34:35], v15 offset:28160\n"
    "s_waitcnt lgkmcnt(6)\n"
    "v_mfma_f32_16x16x32_bf16 v[20:23], v[36:39], v[56:59], v[20:23]\n"
    "s_waitcnt lgkmcnt(4)\n"
    "v_mfma_f32_16x16x32_bf16 v[24:27], v[40:43], v[56:59], v[24:27]\n"
    "s_waitcnt lgkmcnt(3)\n"
    "v_mfma_f32_16x16x32_bf16 v[16:19], v[44:47], v[60:63], v[16:19]\n"
    "s_waitcnt lgkmcnt(2)\n"
    "v_mfma_f32_16x16x32_bf16 v[20:23], v[28:31], v[60:63], v[20:23]\n"
    "s_waitcnt lgkmcnt(0)\n"
    "v_mfma_f32_16x16x32_bf16 v[24:27], v[32:35], v[60:63], v[24:27]\n"
    "s_nop 3\n"
    "v_cvt_pk_bf16_f32 v28, v16, v17\n"
    "v_lshlrev_b32 v16, 16, v28\n"
    "v_and_b32 v17, 0xffff0000, v28\n"
    "v_cvt_pk_bf16_f32 v28, v18, v19\n"
    "v_lshlrev_b32 v18, 16, v28\n"
    "v_and_b32 v19, 0xffff0000, v28\n"
    "v_cvt_pk_bf16_f32 v28, v20, v21\n"
    "v_lshlrev_b32 v20, 16, v28\n"
    "v_and_b32 v21, 0xffff0000, v28\n"
    "v_cvt_pk_bf16_f32 v28, v22, v23\n"
    "v_lshlrev_b32 v22, 16, v28\n"
    "v_and_b32 v23, 0xffff0000, v28\n"
    "v_cvt_pk_bf16_f32 v28, v24, v25\n"
    "v_lshlrev_b32 v24, 16, v28\n"
    "v_and_b32 v25, 0xffff0000, v28\n"
    "v_cvt_pk_bf16_f32 v28, v26, v27\n"
    "v_lshlrev_b32 v26, 16, v28\n"
    "v_and_b32 v27, 0xffff0000, v28\n"
    "v_mul_f32 v16, s87, v16\n"
    "v_mul_f32 v17, s87, v17\n"
    "v_mul_f32 v18, s87, v18\n"
    "v_mul_f32 v19, s87, v19\n"
    "v_mul_f32 v20, s87, v20\n"
    "v_mul_f32 v21, s87, v21\n"
    "v_mul_f32 v22, s87, v22\n"
    "v_mul_f32 v23, s87, v23\n"
    "v_mul_f32 v24, s87, v24\n"
    "v_mul_f32 v25, s87, v25\n"
    "v_mul_f32 v26, s87, v26\n"
    "v_mul_f32 v27, s87, v27\n"
    "v_cvt_pk_bf16_f32 v28, v16, v17\n"
    "v_lshlrev_b32 v16, 16, v28\n"
    "v_and_b32 v17, 0xffff0000, v28\n"
    "v_cvt_pk_bf16_f32 v28, v18, v19\n"
    "v_lshlrev_b32 v18, 16, v28\n"
    "v_and_b32 v19, 0xffff0000, v28\n"
    "v_cvt_pk_bf16_f32 v28, v20, v21\n"
    "v_lshlrev_b32 v20, 16, v28\n"
    "v_and_b32 v21, 0xffff0000, v28\n"
    "v_cvt_pk_bf16_f32 v28, v22, v23\n"
    "v_lshlrev_b32 v22, 16, v28\n"
    "v_and_b32 v23, 0xffff0000, v28\n"
    "v_cvt_pk_bf16_f32 v28, v24, v25\n"
    "v_lshlrev_b32 v24, 16, v28\n"
    "v_and_b32 v25, 0xffff0000, v28\n"
    "v_cvt_pk_bf16_f32 v28, v26, v27\n"
    "v_lshlrev_b32 v26, 16, v28\n"
    "v_and_b32 v27, 0xffff0000, v28\n"
    "v_mov_b32 v28, 0xff800000\n"
    "v_cndmask_b32 v16, v16, v28, s[40:41]\n"
    "v_cndmask_b32 v20, v20, v28, s[48:49]\n"
    "v_cndmask_b32 v24, v28, v24, s[78:79]\n"
    "v_cndmask_b32 v17, v17, v28, s[42:43]\n"
    "v_cndmask_b32 v21, v21, v28, s[50:51]\n"
    "v_cndmask_b32 v25, v28, v25, s[78:79]\n"
    "v_cndmask_b32 v18, v18, v28, s[44:45]\n"
    "v_cndmask_b32 v22, v22, v28, s[52:53]\n"
    "v_cndmask_b32 v26, v28, v26, s[78:79]\n"
    "v_cndmask_b32 v19, v19, v28, s[46:47]\n"
    "v_cndmask_b32 v23, v23, v28, s[54:55]\n"
    "v_cndmask_b32 v27, v28, v27, s[78:79]\n"
    "v_max3_f32 v29, v16, v17, v18\n"
    "v_max3_f32 v29, v29, v19, v20\n"
    "v_max3_f32 v29, v29, v21, v22\n"
    "v_max3_f32 v29, v29, v23, v24\n"
    "v_max3_f32 v29, v29, v25, v26\n"
    "v_max_f32 v29, v29, v27\n"
    "v_mov_b32 v30, v29\n"
    "s_nop 1\n"
    "v_permlane16_swap_b32 v29, v30\n"
    "v_max_f32 v29, v29, v30\n"
    "v_mov_b32 v30, v29\n"
    "s_nop 1\n"
    "v_permlane32_swap_b32 v29, v30\n"
    "v_max_f32 v29, v29, v30\n"
    "v_mul_f32 v29, s88, v29\n"
    "v_fma_f32 v16, v16, s88, -v29\n"
    "v_fma_f32 v17, v17, s88, -v29\n"
    "v_fma_f32 v18, v18, s88, -v29\n"
    "v_fma_f32 v19, v19, s88, -v29\n"
    "v_fma_f32 v20, v20, s88, -v29\n"
    "v_fma_f32 v21, v21, s88, -v29\n"
    "v_fma_f32 v22, v22, s88, -v29\n"
    "v_fma_f32 v23, v23, s88, -v29\n"
    "v_fma_f32 v24, v24, s88, -v29\n"
    "v_fma_f32 v25, v25, s88, -v29\n"
    "v_fma_f32 v26, v26, s88, -v29\n"
    "v_fma_f32 v27, v27, s88, -v29\n"
    "v_exp_f32 v16, v16\n"
    "v_exp_f32 v17, v17\n"
    "v_exp_f32 v18, v18\n"
    "v_exp_f32 v19, v19\n"
    "v_exp_f32 v20, v20\n"
    "v_exp_f32 v21, v21\n"
    "v_exp_f32 v22, v22\n"
    "v_exp_f32 v23, v23\n"
    "v_exp_f32 v24, v24\n"
    "v_exp_f32 v25, v25\n"
    "v_exp_f32 v26, v26\n"
    "v_exp_f32 v27, v27\n"
    "v_add_f32 v29, v16, v17\n"
    "v_add_f32 v29, v29, v18\n"
    "v_add_f32 v29, v29, v19\n"
    "v_add_f32 v29, v29, v20\n"
    "v_add_f32 v29, v29, v21\n"
    "v_add_f32 v29, v29, v22\n"
    "v_add_f32 v29, v29, v23\n"
    "v_add_f32 v29, v29, v24\n"
    "v_add_f32 v29, v29, v25\n"
    "v_add_f32 v29, v29, v26\n"
    "v_add_f32 v29, v29, v27\n"
    "v_mov_b32 v30, v29\n"
    "s_nop 1\n"
    "v_permlane16_swap_b32 v29, v30\n"
    "v_add_f32 v29, v29, v30\n"
    "v_mov_b32 v30, v29\n"
    "s_nop 1\n"
    "v_permlane32_swap_b32 v29, v30\n"
    "v_add_f32 v29, v29, v30\n"
    "v_rcp_f32 v29, v29\n"
    "s_nop 0\n"
    "v_mul_f32 v16, v16, v29\n"
    "v_mul_f32 v17, v17, v29\n"
    "v_mul_f32 v18, v18, v29\n"
    "v_mul_f32 v19, v19, v29\n"
    "v_mul_f32 v20, v20, v29\n"
    "v_mul_f32 v21, v21, v29\n"
    "v_mul_f32 v22, v22, v29\n"
    "v_mul_f32 v23, v23, v29\n"
    "v_mul_f32 v24, v24, v29\n"
    "v_mul_f32 v25, v25, v29\n"
    "v_mul_f32 v26, v26, v29\n"
    "v_mul_f32 v27, v27, v29\n"
    "v_cvt_pk_bf16_f32 v32, v16, v17\n"
    "v_cvt_pk_bf16_f32 v33, v18, v19\n"
    "v_cvt_pk_bf16_f32 v34, v20, v21\n"
    "v_cvt_pk_bf16_f32 v35, v22, v23\n"
    "v_cvt_pk_bf16_f32 v36, v24, v25\n"
    "v_cvt_pk_bf16_f32 v37, v26, v27\n"
    "s_sub_u32 s76, s68, 1\n"
    "s_mul_i32 s76, s76, 1536\n"
    "v_add_u32 v28, s76, v0\n"
    "v_xor_b32 v29, 0x100, v15\n"
    "v_add_u32 v29, s76, v29\n"
    "ds_write_b128 v28, v[32:35] offset:29696\n"
    "ds_write_b64 v29, v[36:37] offset:30720\n"
    "notok_%=:\n"
    "s_add_u32 s89, s80, 1\n"
    "s_cmp_eq_u32 s68, 3\n"
    "s_cbranch_scc1 wave3_%=\n"
    "s_cmp_eq_u32 s68, 7\n"
    "s_cbranch_scc1 wave7_%=\n"
    "wave3done_%=:\n"
    "v_mov_b64 v[16:17], -1\n"
    "v_mov_b64 v[18:19], -1\n"
    "v_lshlrev_b32 v20, 9, v0\n"
    "v_and_b32 v20, 0x30000, v20\n"
    "v_and_b32 v21, 0x70, v0\n"
    "v_or_b32 v20, v20, v21\n"
    "v_and_b32 v21, 0xf0, v0\n"
    "s_add_u32 s76, s80, 2\n"
    "s_and_b32 s76, s76, 3\n"
    "s_lshl_b32 s76, s76, 18\n"
    "s_add_u32 s76, s76, s72\n"
    "buffer_store_dwordx4 v[16:19], v21, s[64:67], s83 offen sc1\n"
    "buffer_store_dwordx4 v[16:19], v20, s[64:67], s76 offen sc1\n"
    "v_lshrrev_b32 v15, 1, v0\n"
    "v_lshlrev_b32 v22, 1, v21\n"
    "v_bfe_u32 v23, v0, 8, 1\n"
    "v_lshl_add_u32 v22, v23, 17, v22\n"
    "v_lshrrev_b32 v23, 9, v0\n"
    "v_lshl_add_u32 v22, v23, 4, v22\n"
    "s_add_u32 s81, s81, 24576\n"
    "s_add_u32 s76, s81, s77\n"
    "s_add_u32 s76, s76, 8192\n"
    "s_mov_b32 m0, s98\n"
    "s_waitcnt lgkmcnt(0)\n"
    "s_barrier\n"
    "s_cmp_eq_u32 s89, 64\n"
    "s_cbranch_scc1 nokx_%=\n"
    "buffer_load_dwordx4 v0, s[64:67], s76 offen sc1 lds\n"
    "nokx_%=:\n"
    "ds_read_b128 v[24:27], v0 offset:29696\n"
    "ds_read_b64 v[28:29], v15 offset:30720\n"
    "ds_read_b128 v[32:35], v0 offset:31232\n"
    "ds_read_b64 v[30:31], v15 offset:32256\n"
    "s_waitcnt lgkmcnt(0)\n"
    "v_mfma_f32_16x16x32_bf16 v[36:39], v[4:7], v[24:27], 0\n"
    "v_mfma_f32_16x16x16_bf16 v[40:43], v[8:9], v[28:29], 0\n"
    "v_mfma_f32_16x16x32_bf16 v[44:47], v[4:7], v[32:35], 0\n"
    "v_mfma_f32_16x16x16_bf16 v[48:51], v[8:9], v[30:31], 0\n"
    "s_nop 5\n"
    "v_pk_add_f32 v[36:37], v[36:37], v[40:41]\n"
    "v_pk_add_f32 v[38:39], v[38:39], v[42:43]\n"
    "v_pk_add_f32 v[44:45], v[44:45], v[48:49]\n"
    "v_pk_add_f32 v[46:47], v[46:47], v[50:51]\n"
    "v_cvt_pk_bf16_f32 v36, v36, v37\n"
    "v_cvt_pk_bf16_f32 v37, v38, v39\n"
    "v_cvt_pk_bf16_f32 v38, v44, v45\n"
    "v_cvt_pk_bf16_f32 v39, v46, v47\n"
    "s_nop 0\n"
    "v_permlane16_swap_b32 v36, v38\n"
    "v_permlane16_swap_b32 v37, v39\n"
    "buffer_store_dwordx4 v[36:39], v22, s[64:67], s82 offen sc1\n"
    "s_cmp_eq_u32 s89, 64\n"
    "s_nop 0\n"
    "s_cbranch_scc1 done_%=\n"
    "s_add_u32 s76, s76, 8192\n"
    "buffer_load_dwordx4 v[4:7], v0, s[64:67], s76 offen sc1\n"
    "s_add_u32 s82, s82, 262144\n"
    "s_and_b32 s76, s89, 3\n"
    "s_lshl_b32 s90, s76, 9\n"
    "s_add_u32 s90, s90, s69\n"
    "s_lshl_b32 s76, s76, 17\n"
    "s_add_u32 s91, s70, s76\n"
    "s_mov_b32 s80, s89\n"
    "s_and_b32 s76, s80, 1\n"
    "s_lshl_b32 s76, s76, 17\n"
    "s_add_u32 s83, s71, s76\n"
    "s_and_b32 s76, s80, 3\n"
    "s_lshl_b32 s76, s76, 18\n"
    "s_add_u32 s84, s72, s76\n"
    "s_mov_b32 s94, 0\n"
    "v_and_b32 v9, 0x33f, v0\n"
    "v_bfe_u32 v8, v0, 6, 1\n"
    "v_lshl_add_u32 v9, v8, 7, v9\n"
    "v_bfe_u32 v8, v0, 7, 1\n"
    "v_lshl_add_u32 v9, v8, 6, v9\n"
    "v_mov_b64 v[16:17], 0\n"
    "v_mov_b64 v[18:19], 0\n"
    "v_mov_b64 v[20:21], 0\n"
    "v_mov_b64 v[22:23], 0\n"
    "v_mov_b32 v8, s80\n"
    "v_add_u32 v15, 0x10000, v2\n"
    "s_cmp_lg_u32 s80, s80\n"
    "s_nop 0\n"
    "s_setpc_b64 s[56:57]\n"
    "notw0_%=:\n"
    "s_waitcnt lgkmcnt(0)\n"
    "s_barrier\n"
    "s_sleep 0\n"
    "buffer_load_dwordx2 v[8:9], v11, s[64:67], s83 offen sc1\n"
    "s_sub_u32 s76, s68, 1\n"
    "s_cmp_lt_u32 s76, 2\n"
    "s_cbranch_scc0 vcheck_%=\n"
    "s_sleep 2\n"
    "s_lshl_b32 s76, s76, 12\n"
    "s_add_u32 s76, s76, s81\n"
    "buffer_load_dwordx4 v[48:51], v0, s[64:67], s76 offen offset:0 sc1\n"
    "buffer_load_dwordx4 v[52:55], v0, s[64:67], s76 offen offset:1024 sc1\n"
    "buffer_load_dwordx4 v[56:59], v0, s[64:67], s76 offen offset:2048 sc1\n"
    "buffer_load_dwordx4 v[60:63], v0, s[64:67], s76 offen offset:3072 sc1\n"
    "s_waitcnt vmcnt(4)\n"
    "s_branch vcheck2_%=\n"
    "vretry_%=:\n"
    "s_nop 7\n"
    "s_sleep 3\n"
    "buffer_load_dwordx2 v[8:9], v11, s[64:67], s83 offen sc1\n"
    "s_add_u32 s95, s95, 1\n"
    "s_cmp_lt_u32 s95, 400000\n"
    "s_cbranch_scc1 vcheck_%=\n"
    "s_branch fail_%=\n"
    "wave7_%=:\n"
    "s_sub_u32 s76, s96, s58\n"
    "v_mov_b32 v16, s76\n"
    "buffer_store_dword v16, off, s[64:67], s101 sc1\n"
    "s_nop 0\n"
    "s_branch wave3done_%=\n"
    "wave3_%=:\n"
    "s_and_b32 s76, s89, 0x43\n"
    "s_cmp_eq_u32 s76, 0\n"
    "s_cbranch_scc1 prodwait_%=\n"
    "proddone_%=:\n"
    "v_lshlrev_b32 v16, 3, v0\n"
    "s_add_u32 s76, s81, 32768\n"
    "buffer_load_dword v61, v16, s[64:67], s76 offen\n"
    "s_add_u32 s76, s76, 8192\n"
    "buffer_load_dword v62, v16, s[64:67], s76 offen\n"
    "s_add_u32 s76, s81, 24576\n"
    "buffer_load_dword v63, v16, s[64:67], s76 offen\n"
    "s_branch wave3done_%=\n"
    "prodwait_%=:\n"
    "s_lshl_b32 s76, s89, 10\n"
    "s_add_u32 s76, s76, s100\n"
    "v_mov_b32 v16, 0\n"
    "prodloop_%=:\n"
    "buffer_load_dword v17, v16, s[64:67], s76 offen sc1\n"
    "s_waitcnt vmcnt(0)\n"
    "v_cmp_le_u32 vcc, 3, v17\n"
    "s_nop 4\n"
    "s_cbranch_vccnz proddone_%=\n"
    "s_sleep 2\n"
    "s_add_u32 s95, s95, 1\n"
    "s_cmp_lt_u32 s95, 400000\n"
    "s_cbranch_scc1 prodloop_%=\n"
    "s_branch fail_%=\n"
    "done_%=:\n"
    "s_mov_b32 %0, 1\n"
    "fail_%=:\n"
    "s_waitcnt vmcnt(0) lgkmcnt(0)\n"
    "s_nop 7\n"
    : "=&s"(ok)
    : "{v[64:71]}"(W[0]), "{v[72:79]}"(W[1]), "{v[80:87]}"(W[2]), "{v[88:95]}"(W[3]), "{v[96:103]}"(W[4]), "{v[104:111]}"(W[5]), "{v[112:119]}"(W[6]), "{v[120:127]}"(W[7]), "{v[128:135]}"(W[8]), "{v[136:143]}"(W[9]), "{v[144:151]}"(W[10]), "{v[152:159]}"(W[11]), "{v[160:167]}"(W[12]), "{v[168:175]}"(W[13]), "{v[176:183]}"(W[14]), "{v[184:191]}"(W[15]), "{v[192:199]}"(W[16]), "{v[200:207]}"(W[17]), "{v[208:215]}"(W[18]), "{v[216:223]}"(W[19]), "{v[224:231]}"(W[20]), "{v[232:239]}"(W[21]), "{v[240:247]}"(W[22]), "{v[248:255]}"(W[23]),
      "{s[64:67]}"(rw), "{s[68:75]}"(sc), "{s99}"(fo), "{s[100:101]}"(po)
    : "memory", "vcc", "scc", "v0", "v1", "v2", "v3", "v4", "v5", "v6", "v7", "v8", "v9", "v10", "v11", "v12", "v13", "v14", "v15", "v16", "v17", "v18", "v19", "v20", "v21", "v22", "v23", "v24", "v25", "v26", "v27", "v28", "v29", "v30", "v31", "v32", "v33", "v34", "v35", "v36", "v37", "v38", "v39", "v40", "v41", "v42", "v43", "v44", "v45", "v46", "v47", "v48", "v49", "v50", "v51", "v52", "v53", "v54", "v55", "v56", "v57", "v58", "v59", "v60", "v61", "v62", "v63", "s40", "s41", "s42", "s43", "s44", "s45", "s46", "s47", "s48", "s49", "s50", "s51", "s52", "s53", "s54", "s55", "s56", "s57", "s58", "s59", "s76", "s77", "s78", "s79", "s80", "s81", "s82", "s83", "s84", "s85", "s86", "s87", "s88", "s89", "s90", "s91", "s92", "s93", "s94", "s95", "s96", "s97", "s98");
  return ok != 0;
}

__device__ __forceinline__ bool vo_loop(const u32x8 (&W)[NREG], rsrc_t rw, u32x8 sc, int fo, u32x2 po) {
  int ok;
  asm volatile(
    "s_mov_b32 %0, 0\n"
    "v_mbcnt_lo_u32_b32 v63, -1, 0\n"
    "v_mbcnt_hi_u32_b32 v63, -1, v63\n"
    "v_lshlrev_b32 v0, 4, v63\n"
    "v_and_b32 v62, 15, v63\n"
    "v_lshrrev_b32 v61, 4, v63\n"
    "v_and_b32 v60, 8, v63\n"
    "v_cmp_ne_u32 vcc, 0, v60\n"
    "v_mov_b32 v1, 0x4444\n"
    "v_mov_b32 v60, 0xeeee\n"
    "v_cndmask_b32 v1, v1, v60, vcc\n"
    "v_and_b32 v2, 0x33f, v0\n"
    "v_bfe_u32 v60, v0, 6, 1\n"
    "v_lshl_add_u32 v2, v60, 7, v2\n"
    "v_bfe_u32 v60, v0, 7, 1\n"
    "v_lshl_add_u32 v2, v60, 6, v2\n"
    "s_lshl_b32 s76, s68, 11\n"
    "v_add_u32 v3, s76, v0\n"
    "v_and_b32 v60, 1, v61\n"
    "v_lshlrev_b32 v4, 9, v60\n"
    "v_lshrrev_b32 v60, 1, v61\n"
    "v_lshl_add_u32 v4, v60, 6, v4\n"
    "v_lshl_add_u32 v4, v62, 2, v4\n"
    "s_lshl_b32 s76, s68, 10\n"
    "s_add_u32 s76, s76, 16384\n"
    "v_add_u32 v4, s76, v4\n"
    "v_bfe_u32 v60, v63, 2, 1\n"
    "v_lshlrev_b32 v5, 8, v60\n"
    "v_bfe_u32 v60, v63, 3, 2\n"
    "v_lshl_add_u32 v5, v60, 4, v5\n"
    "v_bfe_u32 v60, v63, 1, 1\n"
    "v_lshl_add_u32 v5, v60, 6, v5\n"
    "v_and_b32 v60, 1, v63\n"
    "v_lshl_add_u32 v5, v60, 3, v5\n"
    "v_lshrrev_b32 v60, 5, v63\n"
    "v_lshl_add_u32 v5, v60, 2, v5\n"
    "v_and_b32 v56, 31, v63\n"
    "v_lshlrev_b32 v56, 3, v56\n"
    "v_lshl_add_u32 v56, v60, 2, v56\n"
    "s_lshr_b32 s76, s68, 2\n"
    "s_lshl_b32 s76, s76, 13\n"
    "v_add_u32 v57, s76, v0\n"
    "s_and_b32 s76, s68, 3\n"
    "s_lshl_b32 s76, s76, 10\n"
    "s_add_u32 s76, s76, 24576\n"
    "v_add_u32 v58, s76, v0\n"
    "s_mov_b32 s80, 0\n"
    "s_lshl_b32 s77, s68, 15\n"
    "s_add_u32 s77, s77, s73\n"
    "s_memrealtime s[96:97]\n"
    "v_mov_b32 v16, 29312\n"
    "s_waitcnt lgkmcnt(0)\n"
    "v_mov_b32 v18, s96\n"
    "v_mov_b32 v19, s96\n"
    "v_mov_b32 v20, 0\n"
    "ds_write_b64 v16, v[18:19]\n"
    "ds_write_b32 v16, v20 offset:16\n"
    "v_mov_b64 v[8:9], 0\n"
    "v_mov_b64 v[10:11], 0\n"
    "s_mov_b32 s86, 0\n"
    "s_mov_b32 s81, 1792\n"
    "s_mov_b32 s87, 1600\n"
    "s_nop 0\n"
    "s_branch step_%=\n"
    "next7_%=:\n"
    "s_nop 0\n"
    "mwait7_%=:\n"
    "s_memrealtime s[96:97]\n"
    "s_waitcnt lgkmcnt(0)\n"
    "s_sub_u32 s96, s93, s96\n"
    "s_sub_u32 s96, s96, 1\n"
    "s_cmp_lt_u32 s96, 1000\n"
    "s_cbranch_scc1 mwait7_%=\n"
    "mpoll7_%=:\n"
    "buffer_load_dwordx4 v[16:19], v2, s[64:67], s91 offen sc1\n"
    "buffer_load_dwordx4 v[20:23], v2, s[64:67], s91 offen offset:1024 sc1\n"
    "buffer_load_dwordx2 v[26:27], off, s[64:67], s90 sc1\n"
    "v_mov_b32 v25, 29312\n"
    "s_waitcnt vmcnt(2)\n"
    "ds_write_b128 v3, v[16:19]\n"
    "v_max3_u32 v24, v16, v17, v18\n"
    "s_waitcnt vmcnt(1)\n"
    "ds_write_b128 v3, v[20:23] offset:1024\n"
    "v_max3_u32 v24, v24, v19, v20\n"
    "v_max3_u32 v24, v24, v21, v22\n"
    "v_max_u32 v24, v24, v23\n"
    "s_waitcnt vmcnt(0)\n"
    "ds_write_b64 v25, v[26:27]\n"
    "v_max3_u32 v24, v24, v26, v27\n"
    "v_cmp_eq_u32 vcc, -1, v24\n"
    "s_cbranch_vccnz mretry7_%=\n"
    "s_branch step_%=\n"
    "next_%=:\n"
    "s_nop 2\n"
    "mwait_%=:\n"
    "s_memrealtime s[96:97]\n"
    "s_waitcnt lgkmcnt(0)\n"
    "s_sub_u32 s96, s93, s96\n"
    "s_sub_u32 s96, s96, 1\n"
    "s_cmp_lt_u32 s96, 1000\n"
    "s_cbranch_scc1 mwait_%=\n"
    "mpoll_%=:\n"
    "buffer_load_dwordx4 v[16:19], v2, s[64:67], s91 offen sc1\n"
    "buffer_load_dwordx4 v[20:23], v2, s[64:67], s91 offen offset:1024 sc1\n"
    "s_waitcnt vmcnt(1)\n"
    "ds_write_b128 v3, v[16:19]\n"
    "v_max3_u32 v24, v16, v17, v18\n"
    "s_waitcnt vmcnt(0)\n"
    "ds_write_b128 v3, v[20:23] offset:1024\n"
    "v_max3_u32 v24, v24, v19, v20\n"
    "v_max3_u32 v24, v24, v21, v22\n"
    "v_max_u32 v24, v24, v23\n"
    "v_cmp_eq_u32 vcc, -1, v24\n"
    "s_cbranch_vccnz mretry_%=\n"
    "step_%=:\n"
    "s_waitcnt lgkmcnt(0)\n"
    "s_barrier\n"
    "ds_read_b128 v[16:19], v57 offset:0\n"
    "ds_read_b128 v[20:23], v57 offset:1024\n"
    "ds_read_b128 v[24:27], v57 offset:2048\n"
    "ds_read_b128 v[28:31], v57 offset:3072\n"
    "ds_read_b128 v[32:35], v57 offset:4096\n"
    "ds_read_b128 v[36:39], v57 offset:5120\n"
    "ds_read_b128 v[40:43], v57 offset:6144\n"
    "ds_read_b128 v[44:47], v57 offset:7168\n"
    "s_and_b32 s76, s80, 1\n"
    "s_lshl_b32 s76, s76, 17\n"
    "s_add_u32 s83, s71, s76\n"
    "s_and_b32 s76, s80, 3\n"
    "s_lshl_b32 s76, s76, 18\n"
    "s_add_u32 s84, s72, s76\n"
    "s_add_u32 s89, s80, 1\n"
    "s_waitcnt lgkmcnt(7)\n"
    "v_smfmac_f32_16x16x64_bf16 v[8:11], v[16:19], v[64:71], v1\n"
    "s_waitcnt lgkmcnt(6)\n"
    "v_smfmac_f32_16x16x64_bf16 v[8:11], v[20:23], v[72:79], v1\n"
    "s_waitcnt lgkmcnt(5)\n"
    "v_smfmac_f32_16x16x64_bf16 v[8:11], v[24:27], v[80:87], v1\n"
    "s_waitcnt lgkmcnt(4)\n"
    "v_smfmac_f32_16x16x64_bf16 v[8:11], v[28:31], v[88:95], v1\n"
    "s_waitcnt lgkmcnt(3)\n"
    "v_smfmac_f32_16x16x64_bf16 v[8:11], v[32:35], v[96:103], v1\n"
    "s_waitcnt lgkmcnt(2)\n"
    "v_smfmac_f32_16x16x64_bf16 v[8:11], v[36:39], v[104:111], v1\n"
    "s_waitcnt lgkmcnt(1)\n"
    "v_smfmac_f32_16x16x64_bf16 v[8:11], v[40:43], v[112:119], v1\n"
    "s_waitcnt lgkmcnt(0)\n"
    "v_smfmac_f32_16x16x64_bf16 v[8:11], v[44:47], v[120:127], v1\n"
    "s_cmp_lt_u32 s68, 4\n"
    "s_cbranch_scc0 vupper_%=\n"
    "s_barrier\n"
    "ds_read_b128 v[12:15], v58\n"
    "s_waitcnt lgkmcnt(0)\n"
    "s_nop 2\n"
    "v_pk_add_f32 v[8:9], v[8:9], v[12:13]\n"
    "v_pk_add_f32 v[10:11], v[10:11], v[14:15]\n"
    "s_nop 1\n"
    "v_permlane32_swap_b32 v8, v10\n"
    "v_permlane32_swap_b32 v9, v11\n"
    "v_pk_add_f32 v[8:9], v[8:9], v[10:11]\n"
    "v_cvt_pk_bf16_f32 v8, v8, v9\n"
    "v_min_u32 v8, -2, v8\n"
    "buffer_store_dword v8, v56, s[64:67], s83 offen sc1\n"
    "s_nop 0\n"
    "vdone_%=:\n"
    "s_cmp_eq_u32 s89, 64\n"
    "s_cbranch_scc1 done_%=\n"
    "s_cmp_eq_u32 s68, 4\n"
    "s_cbranch_scc0 norearm_%=\n"
    "v_mov_b64 v[16:17], -1\n"
    "v_mov_b64 v[18:19], -1\n"
    "v_lshlrev_b32 v20, 7, v0\n"
    "v_and_b32 v20, 0x18000, v20\n"
    "v_and_b32 v21, 0x70, v0\n"
    "v_or_b32 v20, v20, v21\n"
    "v_and_b32 v21, 0x80, v0\n"
    "v_lshl_add_u32 v20, v21, 1, v20\n"
    "s_add_u32 s76, s80, 3\n"
    "s_and_b32 s76, s76, 3\n"
    "s_lshl_b32 s76, s76, 17\n"
    "s_add_u32 s76, s76, s73\n"
    "buffer_store_dwordx4 v[16:19], v20, s[64:67], s76 offen sc1\n"
    "s_nop 1\n"
    "norearm_%=:\n"
    "v_mov_b32 v16, 29312\n"
    "ds_read_b64 v[16:17], v16\n"
    "s_waitcnt lgkmcnt(0)\n"
    "v_readfirstlane_b32 s92, v16\n"
    "v_readfirstlane_b32 s93, v17\n"
    "s_lshl_b32 s76, s69, 3\n"
    "s_or_b32 s76, s76, s68\n"
    "s_cmp_eq_u32 s76, 5\n"
    "s_cbranch_scc1 reports_%=\n"
    "reportsdone_%=:\n"
    "s_and_b32 s76, s89, 3\n"
    "s_lshl_b32 s90, s76, 9\n"
    "s_add_u32 s90, s90, s74\n"
    "s_lshl_b32 s76, s76, 17\n"
    "s_add_u32 s91, s70, s76\n"
    "s_add_u32 s82, s77, s76\n"
    "s_mov_b32 s94, 0\n"
    "v_mov_b64 v[8:9], 0\n"
    "v_mov_b64 v[10:11], 0\n"
    "v_mov_b64 v[12:13], 0\n"
    "v_mov_b64 v[14:15], 0\n"
    "v_mov_b64 v[32:33], 0\n"
    "v_mov_b64 v[34:35], 0\n"
    "v_mov_b64 v[36:37], 0\n"
    "v_mov_b64 v[38:39], 0\n"
    "s_nop 1\n"
    "owait_%=:\n"
    "s_memrealtime s[96:97]\n"
    "s_waitcnt lgkmcnt(0)\n"
    "s_sub_u32 s96, s92, s96\n"
    "s_sub_u32 s96, s96, 1\n"
    "s_cmp_lt_u32 s96, 1000\n"
    "s_cbranch_scc1 owait_%=\n"
    "buffer_load_dwordx4 v[16:19], v0, s[64:67], s84 offen offset:0 sc1\n"
    "s_sleep 1\n"
    "buffer_load_dwordx4 v[20:23], v0, s[64:67], s84 offen offset:1024 sc1\n"
    "s_sleep 1\n"
    "buffer_load_dwordx4 v[24:27], v0, s[64:67], s84 offen offset:2048 sc1\n"
    "s_sleep 1\n"
    "buffer_load_dwordx4 v[28:31], v0, s[64:67], s84 offen offset:3072 sc1\n"
    "s_waitcnt vmcnt(3)\n"
    "ochk0_%=:\n"
    "v_max3_u32 v6, v16, v17, v18\n"
    "v_max_u32 v6, v6, v19\n"
    "v_cmp_eq_u32 vcc, -1, v6\n"
    "s_cbranch_vccnz oretry0_%=\n"
    "v_smfmac_f32_16x16x64_bf16 v[32:35], v[16:19], v[144:151], v1\n"
    "v_smfmac_f32_16x16x64_bf16 v[36:39], v[16:19], v[152:159], v1\n"
    "v_smfmac_f32_16x16x64_bf16 v[8:11], v[16:19], v[128:135], v1\n"
    "v_smfmac_f32_16x16x64_bf16 v[12:15], v[16:19], v[136:143], v1\n"
    "s_waitcnt vmcnt(2)\n"
    "ochk1_%=:\n"
    "v_max3_u32 v6, v20, v21, v22\n"
    "v_max_u32 v6, v6, v23\n"
    "v_cmp_eq_u32 vcc, -1, v6\n"
    "s_cbranch_vccnz oretry1_%=\n"
    "v_smfmac_f32_16x16x64_bf16 v[32:35], v[20:23], v[176:183], v1\n"
    "v_smfmac_f32_16x16x64_bf16 v[36:39], v[20:23], v[184:191], v1\n"
    "v_smfmac_f32_16x16x64_bf16 v[8:11], v[20:23], v[160:167], v1\n"
    "v_smfmac_f32_16x16x64_bf16 v[12:15], v[20:23], v[168:175], v1\n"
    "s_waitcnt vmcnt(1)\n"
    "ochk2_%=:\n"
    "v_max3_u32 v6, v24, v25, v26\n"
    "v_max_u32 v6, v6, v27\n"
    "v_cmp_eq_u32 vcc, -1, v6\n"
    "s_cbranch_vccnz oretry2_%=\n"
    "v_smfmac_f32_16x16x64_bf16 v[32:35], v[24:27], v[208:215], v1\n"
    "v_smfmac_f32_16x16x64_bf16 v[36:39], v[24:27], v[216:223], v1\n"
    "v_smfmac_f32_16x16x64_bf16 v[8:11], v[24:27], v[192:199], v1\n"
    "v_smfmac_f32_16x16x64_bf16 v[12:15], v[24:27], v[200:207], v1\n"
    "s_waitcnt vmcnt(0)\n"
    "ochk3_%=:\n"
    "v_max3_u32 v6, v28, v29, v30\n"
    "v_max_u32 v6, v6, v31\n"
    "v_cmp_eq_u32 vcc, -1, v6\n"
    "s_cbranch_vccnz oretry3_%=\n"
    "v_smfmac_f32_16x16x64_bf16 v[32:35], v[28:31], v[240:247], v1\n"
    "v_smfmac_f32_16x16x64_bf16 v[36:39], v[28:31], v[248:255], v1\n"
    "v_smfmac_f32_16x16x64_bf16 v[8:11], v[28:31], v[224:231], v1\n"
    "v_smfmac_f32_16x16x64_bf16 v[12:15], v[28:31], v[232:239], v1\n"
    "s_nop 6\n"
    "v_mov_b32_dpp v32, v8 quad_perm:[0,1,2,3] row_mask:0x5 bank_mask:0xf\n"
    "v_mov_b32_dpp v33, v9 quad_perm:[0,1,2,3] row_mask:0x5 bank_mask:0xf\n"
    "v_mov_b32_dpp v34, v10 quad_perm:[0,1,2,3] row_mask:0x5 bank_mask:0xf\n"
    "v_mov_b32_dpp v35, v11 quad_perm:[0,1,2,3] row_mask:0x5 bank_mask:0xf\n"
    "v_mov_b32_dpp v36, v12 quad_perm:[0,1,2,3] row_mask:0x5 bank_mask:0xf\n"
    "v_mov_b32_dpp v37, v13 quad_perm:[0,1,2,3] row_mask:0x5 bank_mask:0xf\n"
    "v_mov_b32_dpp v38, v14 quad_perm:[0,1,2,3] row_mask:0x5 bank_mask:0xf\n"
    "v_mov_b32_dpp v39, v15 quad_perm:[0,1,2,3] row_mask:0x5 bank_mask:0xf\n"
    "v_permlane32_swap_b32 v32, v36\n"
    "v_permlane32_swap_b32 v33, v37\n"
    "v_permlane32_swap_b32 v34, v38\n"
    "v_permlane32_swap_b32 v35, v39\n"
    "v_pk_add_f32 v[8:9], v[32:33], v[36:37]\n"
    "v_pk_add_f32 v[10:11], v[34:35], v[38:39]\n"
    "ds_write2_b32 v4, v8, v9 offset1:32\n"
    "ds_write2_b32 v4, v10, v11 offset0:64 offset1:96\n"
    "s_cmp_lt_u32 s68, 4\n"
    "s_waitcnt lgkmcnt(0)\n"
    "s_barrier\n"
    "s_cbranch_scc0 nored_%=\n"
    "ds_read_b128 v[16:19], v0 offset:16384\n"
    "ds_read_b128 v[20:23], v0 offset:17408\n"
    "ds_read_b128 v[24:27], v0 offset:18432\n"
    "ds_read_b128 v[28:31], v0 offset:19456\n"
    "ds_read_b128 v[32:35], v0 offset:20480\n"
    "ds_read_b128 v[36:39], v0 offset:21504\n"
    "ds_read_b128 v[40:43], v0 offset:22528\n"
    "ds_read_b128 v[44:47], v0 offset:23552\n"
    "s_waitcnt lgkmcnt(6)\n"
    "v_pk_add_f32 v[16:17], v[16:17], v[20:21]\n"
    "v_pk_add_f32 v[18:19], v[18:19], v[22:23]\n"
    "s_waitcnt lgkmcnt(4)\n"
    "v_pk_add_f32 v[24:25], v[24:25], v[28:29]\n"
    "v_pk_add_f32 v[26:27], v[26:27], v[30:31]\n"
    "v_pk_add_f32 v[16:17], v[16:17], v[24:25]\n"
    "v_pk_add_f32 v[18:19], v[18:19], v[26:27]\n"
    "s_waitcnt lgkmcnt(2)\n"
    "v_pk_add_f32 v[32:33], v[32:33], v[36:37]\n"
    "v_pk_add_f32 v[34:35], v[34:35], v[38:39]\n"
    "v_pk_add_f32 v[16:17], v[16:17], v[32:33]\n"
    "v_pk_add_f32 v[18:19], v[18:19], v[34:35]\n"
    "s_waitcnt lgkmcnt(0)\n"
    "v_pk_add_f32 v[40:41], v[40:41], v[44:45]\n"
    "v_pk_add_f32 v[42:43], v[42:43], v[46:47]\n"
    "v_pk_add_f32 v[16:17], v[16:17], v[40:41]\n"
    "v_pk_add_f32 v[18:19], v[18:19], v[42:43]\n"
    "s_nop 1\n"
    "v_permlane32_swap_b32 v16, v18\n"
    "v_permlane32_swap_b32 v17, v19\n"
    "v_pk_add_f32 v[16:17], v[16:17], v[18:19]\n"
    "v_cvt_pk_bf16_f32 v16, v16, v17\n"
    "v_min_u32 v16, -2, v16\n"
    "buffer_store_dword v16, v5, s[64:67], s82 offen sc1\n"
    "s_memrealtime s[96:97]\n"
    "s_waitcnt lgkmcnt(0)\n"
    "s_add_u32 s96, s96, 22\n"
    "redjoin_%=:\n"
    "s_sub_i32 s76, s96, s93\n"
    "s_cmp_gt_i32 s76, 0\n"
    "s_cselect_b32 s93, s96, s93\n"
    "v_mov_b64 v[8:9], 0\n"
    "v_mov_b64 v[10:11], 0\n"
    "s_mov_b32 s80, s89\n"
    "s_mov_b32 s94, 0\n"
    "s_cmp_eq_u32 s68, 7\n"
    "s_nop 0\n"
    "s_cbranch_scc1 next7_%=\n"
    "s_branch next_%=\n"
    "vupper_%=:\n"
    "s_nop 7\n"
    "ds_write_b128 v58, v[8:11]\n"
    "s_waitcnt lgkmcnt(0)\n"
    "s_barrier\n"
    "s_branch vdone_%=\n"
    "nored_%=:\n"
    "s_memrealtime s[96:97]\n"
    "s_lshl_b32 s76, s69, 3\n"
    "s_or_b32 s76, s76, s68\n"
    "s_cmp_eq_u32 s76, 5\n"
    "s_waitcnt lgkmcnt(0)\n"
    "s_cbranch_scc1 times_%=\n"
    "timesdone_%=:\n"
    "s_add_u32 s96, s96, 31\n"
    "s_branch redjoin_%=\n"
    "reports_%=:\n"
    "v_lshlrev_b32 v16, 3, v0\n"
    "v_and_b32 v16, 0xf80, v16\n"
    "s_add_u32 s76, s73, 788480\n"
    "buffer_load_dword v17, v16, s[64:67], s76 offen sc1\n"
    "s_waitcnt vmcnt(0)\n"
    "v_max_u32_dpp v17, v17, v17 quad_perm:[1,0,3,2] row_mask:0xf bank_mask:0xf\n"
    "s_nop 1\n"
    "v_max_u32_dpp v17, v17, v17 quad_perm:[2,3,0,1] row_mask:0xf bank_mask:0xf\n"
    "s_nop 1\n"
    "v_max_u32_dpp v17, v17, v17 row_ror:4 row_mask:0xf bank_mask:0xf\n"
    "s_nop 1\n"
    "v_max_u32_dpp v17, v17, v17 row_ror:8 row_mask:0xf bank_mask:0xf\n"
    "s_nop 0\n"
    "v_readlane_b32 s76, v17, 0\n"
    "v_readlane_b32 s88, v17, 16\n"
    "s_max_u32 s76, s76, s88\n"
    "s_cmp_gt_u32 s76, 200\n"
    "s_nop 2\n"
    "s_cbranch_scc1 reportsdone_%=\n"
    "s_lshl_b32 s76, s76, 4\n"
    "s_sub_i32 s76, s76, s81\n"
    "s_min_i32 s76, s76, 192\n"
    "s_ashr_i32 s76, s76, 1\n"
    "s_add_i32 s81, s81, s76\n"
    "s_branch reportsdone_%=\n"
    "times_%=:\n"
    "v_mov_b32 v16, 29312\n"
    "ds_read_b32 v17, v16 offset:16\n"
    "v_mov_b32 v18, 0\n"
    "s_sub_u32 s88, s96, s92\n"
    "s_waitcnt lgkmcnt(0)\n"
    "ds_write_b32 v16, v18 offset:16\n"
    "v_readfirstlane_b32 s76, v17\n"
    "s_min_u32 s88, s88, 150\n"
    "s_add_u32 s88, s88, 39\n"
    "s_lshl_b32 s88, s88, 4\n"
    "s_sub_i32 s88, s88, s87\n"
    "s_ashr_i32 s88, s88, 2\n"
    "s_cmp_eq_u32 s76, 0\n"
    "s_cselect_b32 s88, s88, 0\n"
    "s_add_i32 s87, s87, s88\n"
    "s_cmp_eq_u32 s76, 0\n"
    "s_cselect_b32 s88, -6, 48\n"
    "s_add_i32 s86, s86, s88\n"
    "s_max_i32 s86, s86, 0\n"
    "s_min_i32 s86, s86, 320\n"
    "s_add_u32 s88, s96, 31\n"
    "s_add_u32 s78, s88, 18\n"
    "s_sub_i32 s76, s93, s88\n"
    "s_cmp_ge_i32 s76, 0\n"
    "s_cselect_b32 s88, s93, s78\n"
    "s_add_u32 s76, s81, s86\n"
    "s_lshr_b32 s76, s76, 4\n"
    "s_add_u32 s88, s88, s76\n"
    "s_add_u32 s88, s88, 52\n"
    "v_mov_b32 v18, s88\n"
    "s_lshr_b32 s76, s87, 4\n"
    "s_add_u32 s88, s88, s76\n"
    "v_mov_b32 v19, s88\n"
    "v_min_u32 v18, -2, v18\n"
    "v_min_u32 v19, -2, v19\n"
    "v_lshlrev_b32 v16, 3, v0\n"
    "s_and_b32 s76, s89, 3\n"
    "s_lshl_b32 s76, s76, 9\n"
    "s_add_u32 s88, s73, 786432\n"
    "s_add_u32 s76, s76, s88\n"
    "s_mov_b64 exec, 15\n"
    "buffer_store_dwordx2 v[18:19], v16, s[64:67], s76 offen sc1\n"
    "v_mov_b64 v[20:21], -1\n"
    "s_add_u32 s76, s80, 3\n"
    "s_and_b32 s76, s76, 3\n"
    "s_lshl_b32 s76, s76, 9\n"
    "s_add_u32 s76, s76, s88\n"
    "buffer_store_dwordx2 v[20:21], v16, s[64:67], s76 offen sc1\n"
    "s_mov_b64 exec, -1\n"
    "s_nop 0\n"
    "s_branch timesdone_%=\n"
    "oretry0_%=:\n"
    "v_mov_b32 v6, 29312\n"
    "v_mov_b32 v7, 1\n"
    "ds_write_b32 v6, v7 offset:16\n"
    "s_sleep 3\n"
    "s_waitcnt lgkmcnt(0)\n"
    "buffer_load_dwordx4 v[16:19], v0, s[64:67], s84 offen offset:0 sc1\n"
    "buffer_load_dwordx4 v[20:23], v0, s[64:67], s84 offen offset:1024 sc1\n"
    "buffer_load_dwordx4 v[24:27], v0, s[64:67], s84 offen offset:2048 sc1\n"
    "buffer_load_dwordx4 v[28:31], v0, s[64:67], s84 offen offset:3072 sc1\n"
    "s_add_u32 s94, s94, 1\n"
    "s_cmp_lt_u32 s94, 400000\n"
    "s_waitcnt vmcnt(0)\n"
    "s_cbranch_scc1 ochk0_%=\n"
    "s_branch fail_%=\n"
    "oretry1_%=:\n"
    "v_mov_b32 v6, 29312\n"
    "v_mov_b32 v7, 1\n"
    "ds_write_b32 v6, v7 offset:16\n"
    "s_sleep 3\n"
    "s_waitcnt lgkmcnt(0)\n"
    "buffer_load_dwordx4 v[20:23], v0, s[64:67], s84 offen offset:1024 sc1\n"
    "buffer_load_dwordx4 v[24:27], v0, s[64:67], s84 offen offset:2048 sc1\n"
    "buffer_load_dwordx4 v[28:31], v0, s[64:67], s84 offen offset:3072 sc1\n"
    "s_add_u32 s94, s94, 1\n"
    "s_cmp_lt_u32 s94, 400000\n"
    "s_waitcnt vmcnt(0)\n"
    "s_cbranch_scc1 ochk1_%=\n"
    "s_branch fail_%=\n"
    "oretry2_%=:\n"
    "v_mov_b32 v6, 29312\n"
    "v_mov_b32 v7, 1\n"
    "ds_write_b32 v6, v7 offset:16\n"
    "s_sleep 3\n"
    "s_waitcnt lgkmcnt(0)\n"
    "buffer_load_dwordx4 v[24:27], v0, s[64:67], s84 offen offset:2048 sc1\n"
    "buffer_load_dwordx4 v[28:31], v0, s[64:67], s84 offen offset:3072 sc1\n"
    "s_add_u32 s94, s94, 1\n"
    "s_cmp_lt_u32 s94, 400000\n"
    "s_waitcnt vmcnt(0)\n"
    "s_cbranch_scc1 ochk2_%=\n"
    "s_branch fail_%=\n"
    "oretry3_%=:\n"
    "v_mov_b32 v6, 29312\n"
    "v_mov_b32 v7, 1\n"
    "ds_write_b32 v6, v7 offset:16\n"
    "s_sleep 3\n"
    "s_waitcnt lgkmcnt(0)\n"
    "buffer_load_dwordx4 v[28:31], v0, s[64:67], s84 offen offset:3072 sc1\n"
    "s_add_u32 s94, s94, 1\n"
    "s_cmp_lt_u32 s94, 400000\n"
    "s_waitcnt vmcnt(0)\n"
    "s_cbranch_scc1 ochk3_%=\n"
    "s_branch fail_%=\n"
    "mretry_%=:\n"
    "s_nop 7\n"
    "s_sleep 3\n"
    "s_add_u32 s94, s94, 1\n"
    "s_cmp_lt_u32 s94, 400000\n"
    "s_cbranch_scc1 mpoll_%=\n"
    "s_branch fail_%=\n"
    "mretry7_%=:\n"
    "s_nop 7\n"
    "s_sleep 3\n"
    "s_add_u32 s94, s94, 1\n"
    "s_cmp_lt_u32 s94, 400000\n"
    "s_cbranch_scc1 mpoll7_%=\n"
    "s_branch fail_%=\n"
    "done_%=:\n"
    "s_mov_b32 %0, 1\n"
    "fail_%=:\n"
    "s_waitcnt vmcnt(0) lgkmcnt(0)\n"
    "s_nop 7\n"
    : "=&s"(ok)
    : "{v[64:71]}"(W[0]), "{v[72:79]}"(W[1]), "{v[80:87]}"(W[2]), "{v[88:95]}"(W[3]), "{v[96:103]}"(W[4]), "{v[104:111]}"(W[5]), "{v[112:119]}"(W[6]), "{v[120:127]}"(W[7]), "{v[128:135]}"(W[8]), "{v[136:143]}"(W[9]), "{v[144:151]}"(W[10]), "{v[152:159]}"(W[11]), "{v[160:167]}"(W[12]), "{v[168:175]}"(W[13]), "{v[176:183]}"(W[14]), "{v[184:191]}"(W[15]), "{v[192:199]}"(W[16]), "{v[200:207]}"(W[17]), "{v[208:215]}"(W[18]), "{v[216:223]}"(W[19]), "{v[224:231]}"(W[20]), "{v[232:239]}"(W[21]), "{v[240:247]}"(W[22]), "{v[248:255]}"(W[23]),
      "{s[64:67]}"(rw), "{s[68:75]}"(sc), "{s99}"(fo), "{s[100:101]}"(po)
    : "memory", "vcc", "scc", "v0", "v1", "v2", "v3", "v4", "v5", "v6", "v7", "v8", "v9", "v10", "v11", "v12", "v13", "v14", "v15", "v16", "v17", "v18", "v19", "v20", "v21", "v22", "v23", "v24", "v25", "v26", "v27", "v28", "v29", "v30", "v31", "v32", "v33", "v34", "v35", "v36", "v37", "v38", "v39", "v40", "v41", "v42", "v43", "v44", "v45", "v46", "v47", "v48", "v49", "v50", "v51", "v52", "v53", "v54", "v55", "v56", "v57", "v58", "v59", "v60", "v61", "v62", "v63", "s40", "s41", "s42", "s43", "s44", "s45", "s46", "s47", "s48", "s49", "s50", "s51", "s52", "s53", "s54", "s55", "s56", "s57", "s58", "s59", "s76", "s77", "s78", "s79", "s80", "s81", "s82", "s83", "s84", "s85", "s86", "s87", "s88", "s89", "s90", "s91", "s92", "s93", "s94", "s95", "s96", "s97", "s98");
  return ok != 0;
}

__device__ __forceinline__ bool chain_role(KP p, int h, int rh, int w, LAS char* lds) {
  const int l0 = lane_id();
  const unsigned long long votes = __builtin_amdgcn_ballot_w64(l0 < 10 && (cword(p, C_PROBE)[l0 < 10 ? l0 : 0] & 1));
  const int xa = xoff(p) + (__builtin_popcountll(votes & 31) >= 3 ? 8192 : 0), xb = xoff(p) + (__builtin_popcountll(votes >> 5) >= 3 ? 8192 : 0);
  const int cp = xcc_id() & 3, vpiece = xa + XV_OFF + (h >> 2) * 16384 + (h & 3) * 2048;
  u32x8 sc;
  sc[0] = w;
  sc[2] = xa + cp * XM_COPY + (w >> 2) * 16384 + (w & 3) * 2048;
  sc[7] = WS_END + ((rh + 1) * 32 + h) * 8 * 64 * 48 + w * 64 * 48;
  u32x8 W[NREG];
  if (rh < 0) {
    const int l = lane_id();
    const rsrc_t rq = mkrsrc(p->Wq + h * DH, W_BYTES - h * DH * 2), rk = mkrsrc(p->Wk + h * DH, W_BYTES - h * DH * 2);
    load_ring<32, 24>(lds, w, l, [&](int n, LAS char* dst) { wstage(n < 16 ? rq : rk, (2 * (n & 7) + ((n >> 3) & 1)) * 64 * 8192, dst, w, l); }, W);
    stage_m0(p, lds, w, l);
    WSTAMP(232, h, 0);
    unsigned spins = 0;
#pragma nounroll
    while (w == 0 && (unsigned)__builtin_amdgcn_readfirstlane(__hip_atomic_load((unsigned*)(p->ws + OFF_PROD + h * 128), RLX_AGENT)) < 3u) {
      __builtin_amdgcn_s_sleep(3);
      if (++spins > SPIN_CAP) return false;
    }
    bar();
    WSTAMP(232, h, 1);
    sc[1] = xa + XT_OFF + cp * 128;
    sc[3] = vpiece + w * 256;
    sc[4] = xb + XO_OFF + (h >> 3) * 16384 + (h & 7) * 1024 + w * 128;
    sc[5] = OFF_QKV + h * QKV_H;
    sc[6] = OFF_OT + (h * 8 + w) * 512;
    if (!qk_loop(W, wsr(p), sc, OFF_FLAG + ((R_QK + h) * 8 + 4 + (h >> 3)) * 128  ,
                 u32x2{(unsigned)(OFF_PROD + h * 128), (unsigned)(xa + XT_OFF + 2048 + h * 128)})) return false;
    bar();
    if (w == 0) post(kparams(), R_QK + h, 64);
    WSTAMP(232, h, 2);
  } else {
    const int l = lane_id();
    const rsrc_t rv = mkrsrc(p->Wv, W_BYTES), ro = mkrsrc(p->Wo, W_BYTES);
    const int d = 16 * (l >> 5) + 4 * ((l >> 4) & 1) + ((l >> 2) & 1) + 8 * ((l >> 3) & 1);
    const int voo = (2 * (d & ~7) + (d & 7)) * 2048 + (l & 3) * 16;
    const int vov = (d + ((l >> 1) & 1) * 64) * 8192 + (l & 1) * 16;
    LAS char* wd = lds + w * 20480;
    auto stage = [&](int n) {
#pragma unroll
      for (int i = 0; i < 4; ++i) {
        if (n < 4) glds<0>(rv, wd + (n % 5) * 4096 + i * 1024, vov, ((8 * (w >> 2) + 2 * n) * 64 + 2 * (i & 1) + 32 * (i >> 1)) * 8192 + h * 256 + (4 * rh + (w & 3)) * 32);
        else glds<0>(ro, wd + (n % 5) * 4096 + i * 1024, voo, ((4 * w + ((n - 4) >> 1)) * 128 + 8 * (n & 1) + 2 * (i & 1) + 64 * (i >> 1)) * 2048 + h * 64);
      }
    };
#pragma unroll
    for (int n = 0; n < 5; ++n) stage(n);
#pragma unroll
    for (int n = 0; n < 12; ++n) {
      if (n < 8) WAITV(16); else if (n == 8) WAITV(12); else if (n == 9) WAITV(8); else if (n == 10) WAITV(4); else WAITV(0);
#pragma unroll
      for (int nt = 0; nt < 2; ++nt) {
        LAS const char* a = wd + (n % 5) * 4096 + (l >> 2) * 64 + nt * 32 + (l & 3) * 8;
        const u32x2 x0 = trread(a), x1 = trread(a + 1024), x2 = trread(a + 2048), x3 = trread(a + 3072);
        W[2 * n + nt] = u32x8{x0.x, x0.y, x1.x, x1.y, x2.x, x2.y, x3.x, x3.y};
      }
      WAITL(0);
      SCHED();
      if (n + 5 < 12) stage(n + 5);
    }
    bar();
    stage_m0(p, lds, w, l);
    WSTAMP(233, rh * 32 + h, 0);
    sc[1] = rh * 32 + h;
    sc[3] = vpiece + (4 * rh + (w & 3)) * 256;
    sc[4] = xb + XO_OFF + ((cp & 1) * 2 + rh) * 65536 + (w >> 1) * 16384 + (w & 1) * 4096;
    sc[5] = xa + (h >> 4) * 16384 + ((2 * h) & 31) * 256 + rh * 128;
    sc[6] = xa + XT_OFF + cp * 128;
    if (!vo_loop(W, wsr(p), sc, 0, u32x2{0u, 0u})) return false;
  }
  return true;
}

__device__ __forceinline__ bool produce(LAS char* lds, int w) {
  const int x = blockIdx.x & 7, i = blockIdx.x >> 3;
  {
    const KP p = kparams();
    const int wi = i < 24 ? i * 4 + (x & 3) : 96 + (i - 24) * 8 + x;
    if (wi >= 64) ln1_rows<2>(p, wi * 8 + w, 768); else ln1_rows<1>(p, wi * 8 + w, 0);
    unsigned wd = 0;
    if (w > 0) touch(wd, mkrsrc(p->Wo, W_BYTES), lane_id() * 128, sgpr((wi * 7 + w - 1) * 8192));
    if (w > 0) WAITV(1); else WAITV(0);
    bar();
    const int l = lane_id();
    if (w == 0 && l < (wi >= 64 ? 2 : 1)) __hip_atomic_fetch_add(cword(p, C_H + ((wi + 96 * l) >> 4)), 1u, RLX_AGENT);
    asm volatile("s_waitcnt vmcnt(0)" ::"v"(wd) : "memory");
  }
  auto wait_h = [&](int lo, int n) {
    const KP p = kparams();
    if (w == 0) {
      const unsigned* cp = cword(p, C_H + lo + min(lane_id(), n - 1));
      unsigned spins = 0;
#pragma nounroll
      while (!none(__hip_atomic_load(cp, RLX_AGENT) < 16u) && ++spins < SPIN_CAP) __builtin_amdgcn_s_sleep(3);
      *(LAS volatile int*)(lds + 163824) = spins < SPIN_CAP;
    }
    WAITL(0);
    bar();
    return __builtin_amdgcn_readfirstlane(*(LAS volatile int*)(lds + 163824)) != 0;
  };
  const bool late = x < 4 ? i >= 28 : i == 31;
  const int n = x < 4 ? i - 24 : i, nc = late ? 48 : x < 4 ? 1 : 11, nw = late ? 20 : x < 4 ? 4 : 31, h0 = late ? 0 : x < 4 ? x * 2 : 8 + (x - 4) * 22;
  const bool small = !late && n < 2 * nc;
  if (small) {
    if (!wait_h(0, 1)) return false;
    qkv_tiles<128>(lds, w, 1, [&](int) { return h0 + n; });
  }
  const int t0 = late ? (x < 4 ? x * 4 + i - 28 : 12 + x) : small ? n + nw - 2 * nc : n - 2 * nc;
  if (!wait_h(0, 16)) return false;
  qkv_tiles<256>(lds, w, late ? 10 : x < 4 ? (t0 < 2 ? 4 : 3) : i < 8 ? 5 : i < 15 ? 4 : 3, [&](int t) {
    int k = t0 + t * nw;
    if (late) return k < 8 ? 128 + 8 + (k >> 1) * 22 + 2 * (9 + (k & 1)) : (12 + (k - 8) / 48) * 128 + 2 * ((k - 8) % 48);
    if (x < 4) return k < 11 ? (1 + k) * 128 + h0 : 11 * 128 + 8 + x * 22 + 2 * (k - 3);
    k = (t < 3 ? k : t == 3 ? 93 + i : 108 + i);
    k += k >= 9 ? 2 : 0;
    return (1 + k / 11) * 128 + h0 + 2 * (k % 11);
  });
  return true;
}
static_assert(__builtin_offsetof(Params, Wq) == 16 && __builtin_offsetof(Params, Wv) == 32, "argument offsets");

__device__ __forceinline__ const unsigned* flagp(KP p, int dep, int n) {
  return (const unsigned*)(p->ws + OFF_FLAG + ((dep + min(lane_id(), n - 1)) * 8 + (blockIdx.x & 7)) * 128);
}
__device__ __forceinline__ void peek_load(unsigned& d, const unsigned* fp) { asm volatile("global_load_dword %0, %1, off sc1" : "+v"(d) : "v"(fp) : "memory"); }
__device__ __forceinline__ int await(KP p, LAS char* word, int w, int dep, int n, int need) {
  if (w == 0) {
    const unsigned* fp = flagp(p, dep, n);
    unsigned v, spins = 0;
#pragma nounroll
    for (;;) {
      v = __hip_atomic_load(fp, RLX_AGENT);
      if (none(v < (unsigned)need)) break;
      __builtin_amdgcn_s_sleep(WSLEEP);
      if (++spins > SPIN_CAP || ((spins & 63u) == 0u && __builtin_amdgcn_readfirstlane(__hip_atomic_load(cword(p, C_TMO), RLX_AGENT)) != 0)) {
        __hip_atomic_store(cword(p, C_TMO), 1u, RLX_AGENT);
        v = 0;
        break;
      }
    }
    *(LAS volatile unsigned*)word = !none(v == 0u) ? 0 : none(v < need + 1u) ? need + 1 : need;
  }
  WAITL(0);
  bar();
  return __builtin_amdgcn_readfirstlane(*(LAS volatile int*)word);
}

struct Stage {
  int role, dep, ndep, nunit;
  const bf16_t* wgt; int wbytes, w_off, ldw;
  int a_off, a_unit, a_kt, a_tile;
  int shift;
  int o_off, o_tile;
  int si, sn;
};
template <int TM, int NKT, bool GELU> __device__ __forceinline__ void stage_gemm(LAS char* lds, int w, const Stage& st) {
  constexpr int TN = 2, KTB = TM * 2048, ND = TM == 2 ? NKT : NKT / 2;
  static_assert(TM == 2 || NKT == 8, "shape");
  const KP p = kparams();
  const rsrc_t rw = wsr(p);
  const int l = lane_id(), r16 = l & 15, g = l >> 4, kh = w >> 2, cg = w & 3;
  u32x4 Wf[2 * NKT][TN];
  {
    const rsrc_t rb = mkrsrc(st.wgt, st.wbytes);
    const int vo = (8 * (l >> 4) + ((l >> 2) & 3)) * st.ldw + (l & 3) * 16;
    LAS char* wd = lds + w * 16384;
#pragma unroll
    for (int hf = 0; hf < 2; ++hf) {
#pragma unroll
      for (int k = 0; k < 2 * NKT; ++k) glds<0>(rb, wd + k * 1024, vo, st.w_off + ((kh * 2 * NKT + hf * NKT + (k >> 1)) * 32 + (k & 1) * 4) * st.ldw + cg * 64);
      WAITV(0);
#pragma unroll
      for (int k = 0; k < 2 * NKT; ++k) {
        LAS const char* a = wd + (k >> 1) * 2048 + (l >> 2) * 64 + (k & 1) * 32 + (l & 3) * 8;
        const u32x2 lo = trread(a), hi = trread(a + 1024);
        Wf[hf * NKT + (k >> 1)][k & 1] = u32x4{lo.x, lo.y, hi.x, hi.y};
      }
      WAITL(0);
      SCHED();
    }
  }
  int va[2][2];
  if (GELU) {
    const int row = 8 * (TM == 2 ? cg : cg & 1) + (l >> 3);
    va[0][0] = va[0][1] = va[1][0] = va[1][1] = row * 2048 + (((l & 7) ^ ((row >> 1) & 7)) << 4);
  } else {
#pragma unroll
    for (int i = 0; i < 4; ++i) {
      const int hh = i < 2 ? kh : 0;
      const int rr = (TM == 2 ? 16 * (cg >> 1) : 0) + ((l >> 1) & 15) + (st.shift ? 14 + (i & 1 ? hh + 1 : -hh - 1) : 0);
      va[i >> 1][i & 1] = (rr >> 4) * st.a_tile + (cg & 1) * 1024 + (l >> 5) * 512 + (rr & 15) * 32 + (l & 1) * 16;
    }
  }
  const int fa = kh * NKT * KTB + (GELU ? r16 * 128 + ((g ^ (r16 >> 1)) << 4) : (g >> 1) * 512 + r16 * 32 + (g & 1) * 16);
  auto piece = [&](int u, int i, int low = 0) {
    const int hh = low ? 0 : kh;
    const int so = st.a_off + u * st.a_unit + (hh * NKT + (TM == 2 ? 0 : cg >> 1)) * st.a_kt;
    glds<AUX_SC1>(rw, lds + (u & 1) * 65536 + hh * NKT * KTB + cg * 1024 + i * 4096, va[low][TM == 2 ? i >> 2 : i >> 1], so + (TM == 2 ? i : 2 * i) * st.a_kt);
  };
  auto issue = [&](int u, bool paced) {
#pragma unroll
    for (int i = 0; i < ND; ++i) {
      piece(u, i);
      if (paced && i % TM == TM - 1 && i + 1 < ND) __builtin_amdgcn_s_sleep(TM * PACEW);
    }
  };
  bool have = false, pre = false;
  bool seen = false, asked = false;
  LAS char* scr = lds + 131072;
  unsigned hold = 0;
  if (PRIO && kh) __builtin_amdgcn_s_setprio(1);
  LAS volatile int* nw = (LAS volatile int*)(lds + 147456);
  auto out = [&](int u, int ni, int mi, f32x4 r) {
    const int oo = st.o_off + (2 * u + mi) * st.o_tile;
    if (GELU) {
      const f32x4 t = rnd4(r);
      st64(u32x2{pk(gelu_tanh(t[0]), gelu_tanh(t[1])), pk(gelu_tanh(t[2]), gelu_tanh(t[3]))}, rw, r16 * 32 + g * 8, oo + (cg * 2 + ni) * 512);
    } else {
      st128(__builtin_bit_cast(u32x4, r), rw, r16 * 64 + g * 16, oo + (cg * 2 + ni) * 1024);
    }
  };
#pragma nounroll
  for (int u = 0; u < st.nunit; ++u) {
    TS_DECL;
    TS(0);
    if (!have && !seen) {
      const int need = st.shift ? min(u + 1, NSEG) : u + 1;
      const int avail = await(p, (LAS char*)(nw + 2), w, st.dep, st.ndep, need);
      if (avail == 0) return;
      pre = avail > need && u + 2 < st.nunit;
    }
    TS(1);
    f32x4 acc[TN][TM];
    unsigned peek = 0;
    auto compute = [&](auto have_, auto pre_) {
      constexpr bool HAVE = have_.value, PRE = pre_.value;
      if (!HAVE) { if (!asked) issue(u, true); if (PRE) issue(u + 1, false); }
#pragma unroll
      for (int ni = 0; ni < TN; ++ni)
#pragma unroll
        for (int mi = 0; mi < TM; ++mi) acc[ni][mi] = f32x4{0.f, 0.f, 0.f, 0.f};
      LAS const char* ab = lds + (u & 1) * 65536 + fa;
      u32x4 Af[8][TM];
      auto rdk = [&](int k) {
#pragma unroll
        for (int mi = 0; mi < TM; ++mi)
          if (k < 2 * NKT) Af[k & 7][mi] = lds128((LAS const char*)((size_t)ab ^ ((k & 1) * (GELU ? 64 : 1024))) + (k >> 1) * KTB + mi * 2048);
      };
      auto landed = [&](int c) {
        constexpr int X = PRE ? ND : 0;
        if (c == 0) waitv<ND - TM + X>(); else if (c == 1) waitv<ND - 2 * TM + X>(); else if (c == 2) waitv<ND - 3 * TM + X>(); else waitv<X>();
        bar();
      };
      if (!HAVE) landed(0);
#pragma unroll
      for (int k = 0; k < 4; ++k) rdk(k);
      SCHED();
#pragma unroll
      for (int k = 0; k < 2 * NKT; ++k) {
        if (HAVE) rdk(k + 4);
        else if ((k & 3) == 2 && k + 2 < 2 * NKT) {
          landed((k >> 2) + 1);
#pragma unroll
          for (int j = 0; j < 4; ++j) rdk(k + 2 + j);
        }
#pragma unroll
        for (int ni = 0; ni < TN; ++ni)
#pragma unroll
          for (int mi = 0; mi < TM; ++mi) acc[ni][mi] = mfma(Wf[k][ni], Af[k & 7][mi], acc[ni][mi]);
        if (HAVE && k == (kh ? 2 * NKT - 1 : FPOS)) bar();
        if (HAVE && PRE) { if (DMAALL) { if (k < 2 * ND) piece(u + 1, k >> 1, k & 1); } else if (k < ND) piece(u + 1, k); }
        if (k == (HAVE ? 4 : 11) && w == 0) peek_load(peek, flagp(p, st.dep, st.ndep));
        SCHED();
      }
    };
    if (have) { if (pre && (kh || !DMAALL)) compute(std::true_type(), std::true_type()); else compute(std::true_type(), std::false_type()); }
    else if (pre) compute(std::false_type(), std::true_type());
    else compute(std::false_type(), std::false_type());
    TS(3);
    if (PRIO && !kh) __builtin_amdgcn_s_setprio(2);
    auto level = [&]() {
      const unsigned n1 = st.shift ? min(u + 2, NSEG) : u + 2, n2 = st.shift ? min(u + 3, NSEG) : u + 3;
      return none(peek < n2) && u + 3 < st.nunit ? 2 : none(peek < n1) && u + 1 < st.nunit ? 1 : 0;
    };
    if (pre) {
      if (kh) {
#pragma unroll
        for (int i = 0; i < TN * TM; ++i) *(LAS f32x4*)(scr + (cg * TN * TM + i) * 1024 + l * 16) = acc[i / TM][i % TM];
      }
    } else {
#pragma unroll
      for (int mi = 0; mi < TM; ++mi) *(LAS f32x4*)(scr + (w * TM + mi) * 1024 + l * 16) = kh ? acc[0][mi] : acc[1][mi];
    }
    asm volatile("s_waitcnt vmcnt(0)" : "+v"(peek), "+v"(hold)::"memory");
    if (w == 0) nw[0] = level();
    WAITL(0);
    TS(4);
    bar();
    TS(5);
    if (have && w == 7) post(p, st.role, u);
    int lev = __builtin_amdgcn_readfirstlane(nw[0]);
    if (pre) {
      if (!kh) {
#pragma unroll
        for (int i = 0; i < TN * TM; ++i) acc[i / TM][i % TM] += *(LAS const f32x4*)(scr + (cg * TN * TM + i) * 1024 + l * 16);
      }
      TS(6);
      if (TM == 2 && w == 0 && none(peek < u + 4u)) {
#pragma nounroll
        for (int j = st.si; j < 8; j += st.sn)
          touch(hold, rw, l * 128, st.a_off + (u + 3) * st.a_unit + ((j >> 2) + st.shift) * (st.a_unit >> 1) + (j & 3) * 8192);
      }
      if (!kh) {
#pragma unroll
        for (int i = 0; i < TN * TM; ++i) out(u, i / TM, i % TM, acc[i / TM][i % TM]);
      }
      have = true;
    } else {
#pragma unroll
      for (int mi = 0; mi < TM; ++mi) out(u, kh, mi, (kh ? acc[1][mi] : acc[0][mi]) + *(LAS const f32x4*)(scr + ((w ^ 4) * TM + mi) * 1024 + l * 16));
      TS(6);
      asked = lev > 0;
      if (asked) {
        issue(u + 1, true);
        waitv<ND>();
        bar();
      } else {
        if (w == 0) peek_load(peek, flagp(p, st.dep, st.ndep));
        asm volatile("s_waitcnt vmcnt(0)" : "+v"(peek)::"memory");
        if (w == 0) nw[1] = level();
        WAITL(0);
        bar();
        lev = __builtin_amdgcn_readfirstlane(nw[1]);
      }
      if (w == 7) post(p, st.role, u + 1);
      have = false;
    }
    if (PRIO && !kh) __builtin_amdgcn_s_setprio(0);
    seen = lev > 0;
    pre = lev == 2;
    TS(2);
    TS_OUT(st.role, u);
  }
}

__device__ __forceinline__ float psum(float v) {
  v += dppf<0xB1>(v); v += dppf<0x4E>(v); v += dppf<0x128>(v);
  return xsum1632(v);
}
template <int NP, class F> __device__ __forceinline__ void norm_pairs(f32x4 (&v)[NP], LAS char* red, int w, int l, F atbar) {
#pragma unroll
  for (int i = 0; i < NP; ++i) {
    const float s = psum(hsum(v[i])), q = psum(hsum(v[i] * v[i]));
    *(LAS f32x2*)(red + ((i * 2 + ((l >> 2) & 1)) * 8 + w) * 8) = f32x2{s, q};
  }
  atbar(0);
  WAITL(0);
  bar();
  atbar(1);
#pragma unroll
  for (int i = 0; i < NP; ++i) {
    LAS const f32x4* rp = (LAS const f32x4*)(red + (i * 2 + ((l >> 2) & 1)) * 64);
    const f32x4 ts = (rp[0] + rp[1]) + (rp[2] + rp[3]);
    const float mean = (ts[0] + ts[2]) * (1.0f / DM), var = fmaxf((ts[1] + ts[3]) * (1.0f / DM) - mean * mean, 0.f);
    v[i] = (v[i] - mean) * __builtin_amdgcn_rsqf(var + LN_EPS);
  }
}
template <int KIND, int NP> __device__ __forceinline__ void stage_rows(LAS char* lds, int w, int role, int idx) {
  const KP p = kparams();
  const rsrc_t rw = wsr(p), rx = mkrsrc(p->x, ACT_BYTES), ro = mkrsrc(p->out, ACT_BYTES);
  const int l = lane_id(), col = 128 * w + (l >> 3) * 16 + (l & 3) * 4, rl = (l >> 2) & 1, r0 = idx * 2 * NP;
  const float* mod = (const float*)(p->ws + OFF_MOD) + col;
  const f32x4 gate = *(const f32x4*)(mod + (KIND == 0 ? 2 : 5) * DM), sh = *(const f32x4*)(mod + 3 * DM), sc = rnd4(*(const f32x4*)(mod + 4 * DM) + 1.0f);
  asm volatile("s_waitcnt vmcnt(0)" ::"v"(gate), "v"(sh), "v"(sc) : "memory");
  const int vp = (col >> 4) * 1024 + rl * 64 + (l & 3) * 16, vr = rl * 2048 + col * 2, l16 = l * 16;
  constexpr int NU = KIND == 0 ? NSEG : NSEG + 1, NPART = KIND == 1 ? 1 : 4, NOUT = KIND == 0 ? 3 : KIND == 1 ? 2 : 1, ND = NP * NPART + 1;
  const int DEP = KIND == 0 ? R_WO : KIND == 1 ? R_W2 : R_W4;
  constexpr int NDEP = KIND == 1 ? 8 : 32;
  auto issue = [&](int u) {
    LAS char* buf = lds + (u & 1) * 65536;
    const int row = u * 32 + r0;
#pragma unroll
    for (int i = 0; i < NP; ++i)
#pragma unroll
      for (int k = 0; k < NPART; ++k)
        glds<AUX_SC1>(rw, buf + ((i * NPART + k) * 8 + w) * 1024, vp, (KIND == 0 ? OFF_P1 : KIND == 1 ? OFF_P2 : OFF_P4) + k * PSTR + (row >> 4) * 65536 + ((row & 15) + 2 * i) * 64);
    if (KIND == 0) glds<0>(rx, buf + 32768 + (w & 3) * 1024, l16, row * 2048 + (w & 3) * 1024);
    if (KIND == 1) glds<AUX_SC1>(rw, buf + 32768 + w * 1024, l16, OFF_H2 + (row - 2) * 2048 + w * 1024);
    if (KIND == 2) glds<AUX_SC1>(rw, buf + 32768 + w * 1024, l16, (w < 4 ? OFF_X1 - 4096 : OFF_Y1 - 4096) + row * 2048 + w * 1024);
  };
  bool have = false, pre = false;
  int seen = 0;
#pragma nounroll
  for (int u = 0; u < NU; ++u) {
    LAS char* buf = lds + (u & 1) * 65536;
    TS_DECL;
    TS(0);
    if (!have) {
      const int avail = seen > u ? seen : await(p, buf + 61440, w, DEP, NDEP, u + 1);
      if (avail == 0) return;
      pre = avail > u + 1 && u + 2 < NU;
      issue(u);
    }
    TS(1);
    if (pre) issue(u + 1);
    if (!have) {
      if (pre) waitv<ND>(); else waitv<0>();
      bar();
    }
    unsigned peek = 0;
    auto early = [&](int after) {
      if (!after) { if (have) { if (pre) waitv<ND>(); else waitv<0>(); } }
      else { if (have && w == 7) post(p, role, u); if (pre && w == 0) peek_load(peek, flagp(p, DEP, NDEP)); }
    };
    if (KIND == 2 && pre && w == 0) peek_load(peek, flagp(p, DEP, NDEP));
    TS(3);
    f32x4 v[NP];
    auto put = [&](int o) {
#pragma unroll
      for (int i = 0; i < NP; ++i) { const u32x2 d = pk4(v[i]); *(LAS u32x2*)(buf + 40960 + (o * NP + i) * 4096 + vr) = d; v[i] = unpk4(d); }
    };
#pragma unroll
    for (int i = 0; i < NP; ++i) {
      LAS const f32x4* pp = (LAS const f32x4*)(buf + (i * NPART * 8 + w) * 1024 + l16);
      f32x4 s = pp[0];
      if (NPART == 4) s = (s + pp[512]) + (pp[1024] + pp[1536]);
      s = rnd4(s);
      if (KIND == 2) s += unpk4(lds64(buf + 32768 + 4096 + vr));
      if (KIND != 1) s *= gate;
      v[i] = s + unpk4(lds64(buf + 32768 + i * 4096 + vr));
    }
    put(NOUT - 1);
    if (KIND == 0) {
      norm_pairs<NP>(v, buf + 57344, w, l, early);
#pragma unroll
      for (int i = 0; i < NP; ++i) v[i] = v[i] * sc + sh;
      put(1);
    }
    if (KIND == 0) { norm_pairs<NP>(v, buf + 59392, w, l, [](int) {}); put(0); }
    if (KIND == 1) { norm_pairs<NP>(v, buf + 59392, w, l, early); put(0); }
    TS(4);
    asm volatile("s_waitcnt vmcnt(0)" : "+v"(peek)::"memory");
    if (w == 0) *(LAS volatile int*)(buf + 61444) = pre && none(peek < u + 3u) && u + 3 < NU;
    WAITL(0);
    bar();
    TS(5);
#pragma unroll
    for (int jj = 0; jj < (NOUT * NP + 1) / 2; ++jj) {
      const int j = jj * 8 + w;
      if (j < NOUT * NP * 4) {
        const int o = j / (NP * 4), rr = (j % (NP * 4)) >> 1, row = u * 32 + r0 + rr;
        const u32x4 d = lds128(buf + 40960 + j * 1024 + l16);
        const int so = row * 2048 + (j & 1) * 1024;
        if (KIND == 0) st128(d, rw, l16, (o == 0 ? OFF_N2 : o == 1 ? OFF_H2 : OFF_X1) + so);
        if (KIND == 1) st128(d, rw, l16, (o == 0 ? OFF_N3 : OFF_Y1) + so);
        if (KIND == 2 && row >= 2 && row < SEQ + 2) st128(d, ro, l16, so - 4096);
      }
    }
    TS(6);
    if (KIND != 2 && !pre) {
      if (w == 0) peek_load(peek, flagp(p, DEP, NDEP));
      asm volatile("s_waitcnt vmcnt(0)" : "+v"(peek)::"memory");
      if (w == 0) *(LAS volatile int*)(buf + 61448) = none(peek < u + 3u) ? u + 3 : none(peek < u + 2u) ? u + 2 : 0;
      WAITL(0);
      bar();
      if (w == 7) post(p, role, u + 1);
      seen = __builtin_amdgcn_readfirstlane(*(LAS volatile int*)(buf + 61448));
    }
    TS(2);
    TS_OUT(role, u);
    have = pre;
    pre = __builtin_amdgcn_readfirstlane(*(LAS volatile int*)(buf + 61444)) != 0;
  }
}

__device__ __forceinline__ void stream(LAS char* lds, int w) {
  const KP p = kparams();
  const int x = blockIdx.x & 7, i = blockIdx.x >> 3, q = x & 3;
  int kind, idx;
  if (x < 4) { kind = i < 28 ? 6 : 7; idx = q * 4 + (i & 3); }
  else if (i < 8) { kind = 1; idx = i; }
  else if (i < 15) { kind = 2; idx = q * 7 + i - 8; }
  else if (i < 17) { kind = 3; idx = q * 2 + i - 15; }
  else if (i < 19) { kind = 5; idx = q * 2 + i - 17; }
  else if (i < 23) { kind = 4; idx = q * 4 + i - 19; }
  else if (i < 31) { kind = 0; idx = i - 23; }
  else return;
  kind = sgpr(kind); idx = sgpr(idx);
  if (kind == 3) { stage_rows<1, 2>(lds, w, R_LN3 + idx, idx); return; }
  if (kind == 6) { stage_rows<0, 1>(lds, w, R_LN2 + idx, idx); return; }
  if (kind == 7) { stage_rows<2, 1>(lds, w, R_FIN + idx, idx); return; }
  Stage st;
  st.shift = 0; st.si = idx; st.sn = 8;
  if (kind == 0) {
    st.role = R_WO + q * 8 + idx; st.dep = R_QK + q * 8; st.ndep = 8; st.nunit = NSEG;
    st.wgt = p->Wo; st.wbytes = W_BYTES; st.ldw = 2048; st.w_off = q * 1024 * 2048 + idx * 256;
    st.a_off = OFF_OT + q * 16 * 2048; st.a_unit = 2 * 131072; st.a_kt = 2048; st.a_tile = 131072;
    st.o_off = OFF_P1 + q * PSTR + idx * 8192; st.o_tile = 65536;
    stage_gemm<2, 8, false>(lds, w, st);
    return;
  }
  if (kind == 1) {
    st.role = R_W4 + q * 8 + idx; st.dep = R_W3 + q * 7; st.ndep = 7; st.nunit = NSEG + 1;
    st.wgt = p->w4; st.wbytes = DM * FF * 2; st.ldw = 2048; st.w_off = q * 896 * 2048 + idx * 256;
    st.a_off = OFF_G3 + q * 14 * 2048; st.a_unit = 2 * FF * 32; st.a_kt = 2048; st.a_tile = FF * 32;
    st.o_off = OFF_P4 + q * PSTR + idx * 8192; st.o_tile = 65536;
    stage_gemm<2, 7, false>(lds, w, st);
    return;
  }
  if (kind == 5) {
    st.role = R_W2 + idx; st.dep = R_W1; st.ndep = 16; st.nunit = NSEG + 1; st.shift = 1; st.si = idx & 1; st.sn = 2;
    st.wgt = p->w2; st.wbytes = DM * DM * 2; st.ldw = 2048; st.w_off = idx * 256;
    st.a_off = OFF_G; st.a_unit = 65536; st.a_kt = 2048; st.a_tile = 32768;
    st.o_off = OFF_P2 + idx * 8192; st.o_tile = 65536;
    stage_gemm<2, 8, false>(lds, w, st);
    return;
  }
  if (kind == 2) {
    st.role = R_W3 + idx; st.dep = R_LN3; st.ndep = 8; st.nunit = NSEG + 1; st.si = idx - q * 7; st.sn = 7;
    st.wgt = p->w3; st.wbytes = DM * FF * 2; st.ldw = FF * 2; st.w_off = idx * 256;
    st.a_off = OFF_N3; st.a_unit = 65536; st.a_kt = 128; st.a_tile = 0;
    st.o_off = OFF_G3 + idx * 4096; st.o_tile = FF * 32;
    stage_gemm<2, 8, true>(lds, w, st);
    return;
  }
  const int rh = idx >> 3, cb = idx & 7;
  st.role = R_W1 + idx; st.dep = R_LN2 + 8 * rh; st.ndep = 8; st.nunit = NSEG;
  st.wgt = p->w1; st.wbytes = DM * DM * 2; st.ldw = 2048; st.w_off = cb * 256;
  st.a_off = OFF_N2 + rh * 32768; st.a_unit = 65536; st.a_kt = 128; st.a_tile = 0;
  st.o_off = OFF_G + 32768 + rh * 32768 + cb * 4096; st.o_tile = 32768;
  stage_gemm<1, 8, true>(lds, w, st);
}

__global__ __launch_bounds__(512, 2) void k_mega(Params) {
  LAS char* lds = (LAS char*)smem;
  const int b = blockIdx.x, w = __builtin_amdgcn_readfirstlane(threadIdx.x >> 6);
  WSTAMP(234 + (b >> 6), b & 63, 0);
  if ((b & 7) < 4 && (b >> 3) < 24) {
    const int x = b & 3, i = b >> 3;
    if (!chain_role(kparams(), i < 8 ? x * 8 + i : (x & 1) * 16 + i - 8, i < 8 ? -1 : x >> 1, w, lds)) __hip_atomic_store(cword(kparams(), C_TMO), 1u, RLX_AGENT);
    return;
  }
  if (!produce(lds, w)) { __hip_atomic_store(cword(kparams(), C_TMO), 1u, RLX_AGENT); return; }
  stream(lds, w);
}

extern "C" void kernel_launch(void* const* d_in, const int* in_sizes, int n_in, void* d_out, int out_size, void* d_ws, size_t ws_size,
                              hipStream_t stream) {
  Params p;
  p.x = (const bf16_t*)d_in[0]; p.mem0 = (const bf16_t*)d_in[1]; p.Wq = (const bf16_t*)d_in[2]; p.Wk = (const bf16_t*)d_in[3];
  p.Wv = (const bf16_t*)d_in[4]; p.Wo = (const bf16_t*)d_in[5]; p.w1 = (const bf16_t*)d_in[8]; p.w2 = (const bf16_t*)d_in[9];
  p.w3 = (const bf16_t*)d_in[10]; p.w4 = (const bf16_t*)d_in[11]; p.c = (const bf16_t*)d_in[12]; p.wmod = (const bf16_t*)d_in[13];
  p.out = (bf16_t*)d_out; p.ws = (unsigned char*)d_ws;
  (void)hipFuncSetAttribute((const void*)k_mega, hipFuncAttributeMaxDynamicSharedMemorySize, LDS_BYTES);
  k_pre<<<256, 512, 0, stream>>>(p);
  k_mega<<<256, 512, LDS_BYTES, stream>>>(p);
}
```

```cpp
#include <hip/hip_runtime.h>
#include <stdint.h>
#include <type_traits>

#define LAS __attribute__((address_space(3)))
typedef unsigned short bf16_t;
typedef __bf16 bfx8 __attribute__((ext_vector_type(8)));
typedef __bf16 bfx16 __attribute__((ext_vector_type(16)));
typedef __bf16 bfx2 __attribute__((ext_vector_type(2)));
typedef short s16x4 __attribute__((ext_vector_type(4)));
typedef float f32x4 __attribute__((ext_vector_type(4)));
typedef float f32x2 __attribute__((ext_vector_type(2)));
typedef unsigned u32x2 __attribute__((ext_vector_type(2)));
typedef unsigned u32x4 __attribute__((ext_vector_type(4)));
typedef unsigned u32x8 __attribute__((ext_vector_type(8)));
typedef __amdgpu_buffer_rsrc_t rsrc_t;

constexpr int SEQ = 2048, DM = 1024, NH = 32, DH = 128, NSEG = 64, FF = 3584;
constexpr float LN_EPS = 1e-5f;
constexpr unsigned SENT = 0xFFFFFFFFu;
constexpr int AUX_SC1 = 16;

constexpr int ACT_BYTES = SEQ * DM * 2, W_BYTES = DM * NH * DH * 2, WS_BYTES = 256 << 20;
constexpr int OFF_CTRL = 0;
constexpr int OFF_MOD = 65536;
constexpr int OFF_X = 131072, PG = 2097152;
constexpr int XM_SLOT = 131072, XM_COPY = 32768;
constexpr int XV_OFF = 524288, XV_SLOT = 131072;
constexpr int XT_OFF = 786432;
constexpr int XO_OFF = PG;
constexpr int OFF_QKV = OFF_X + 3 * PG;
constexpr int QKV_S = 24576, QKV_H = 64 * QKV_S + 4096;
constexpr int OFF_OT = OFF_QKV + 32 * QKV_H;
constexpr int OFF_H = OFF_OT + SEQ * 4096 * 2;
constexpr int OFF_X1 = OFF_H + ACT_BYTES;
constexpr int OFF_H2 = OFF_X1 + ACT_BYTES;
constexpr int OFF_N2 = OFF_H2 + ACT_BYTES;
constexpr int OFF_G = OFF_N2 + ACT_BYTES;
constexpr int OFF_Y1 = OFF_G + 132 * 32768;
constexpr int OFF_N3 = OFF_Y1 + 2080 * 2048;
constexpr int OFF_G3 = OFF_N3 + 2080 * 2048;
constexpr int PSTR = 130 * 65536;
constexpr int OFF_P1 = OFF_G3 + 130 * FF * 32;
constexpr int OFF_P2 = OFF_P1 + 4 * PSTR;
constexpr int OFF_P4 = OFF_P2 + PSTR;
constexpr int OFF_FLAG = (OFF_P4 + 4 * PSTR + 32767) / 32768 * 32768;
constexpr int OFF_PROD = OFF_FLAG + 262144;
constexpr int OFF_MDBG = OFF_PROD + 65536;
constexpr int WS_END = OFF_MDBG + 65 * 16384;
static_assert(WS_END + (8 << 20) <= WS_BYTES, "workspace");

struct Params {
  const bf16_t *x, *mem0, *Wq, *Wk, *Wv, *Wo, *w1, *w2, *w3, *w4, *c, *wmod;
  bf16_t* out;
  unsigned char* ws;
};
typedef const __attribute__((address_space(4))) Params* KP;
__device__ __forceinline__ KP kparams() { KP k = (KP)__builtin_amdgcn_kernarg_segment_ptr(); asm volatile("" : "+s"(k)); return k; }

__device__ __forceinline__ float bf2f(unsigned short b) { return __builtin_bit_cast(float, (unsigned)b << 16); }
__device__ __forceinline__ float bflo(unsigned u) { return __builtin_bit_cast(float, u << 16); }
__device__ __forceinline__ float bfhi(unsigned u) { return __builtin_bit_cast(float, u & 0xFFFF0000u); }
__device__ __forceinline__ unsigned pk(float lo, float hi) { bfx2 v; v[0] = (__bf16)lo; v[1] = (__bf16)hi; return __builtin_bit_cast(unsigned, v); }
__device__ __forceinline__ float rbf(float x) { return bflo(pk(x, 0.f)); }
__device__ __forceinline__ unsigned san(unsigned u) { return u < 0xFFFFFFFEu ? u : 0xFFFFFFFEu; }
__device__ __forceinline__ f32x4 mfma(u32x4 a, u32x4 b, f32x4 c) {
  return __builtin_amdgcn_mfma_f32_16x16x32_bf16(__builtin_bit_cast(bfx8, a), __builtin_bit_cast(bfx8, b), c, 0, 0, 0);
}
__device__ __forceinline__ f32x4 mfma16(u32x2 a, u32x2 b, f32x4 c) {
  return __builtin_amdgcn_mfma_f32_16x16x16bf16_1k(__builtin_bit_cast(s16x4, a), __builtin_bit_cast(s16x4, b), c, 0, 0, 0);
}
__device__ __forceinline__ f32x4 smfma(u32x4 a, u32x8 b, f32x4 c, int idx) {
  return __builtin_amdgcn_smfmac_f32_16x16x64_bf16(__builtin_bit_cast(bfx8, a), __builtin_bit_cast(bfx16, b), c, idx, 0, 0);
}
__device__ __forceinline__ u32x2 trread(LAS const char* p) { return __builtin_bit_cast(u32x2, __builtin_amdgcn_ds_read_tr16_b64_v4i16((LAS s16x4*)p)); }
__device__ __forceinline__ u32x4 lds128(LAS const char* p) { return *(LAS const u32x4*)p; }
__device__ __forceinline__ u32x2 lds64(LAS const char* p) { return *(LAS const u32x2*)p; }
__device__ __forceinline__ rsrc_t mkrsrc(const void* p, int bytes) { return __builtin_amdgcn_make_buffer_rsrc((void*)p, 0, bytes, 0x00020000); }
__device__ __forceinline__ rsrc_t wsr(KP p) { return mkrsrc(p->ws, WS_BYTES); }
template <int AUX> __device__ __forceinline__ void glds(rsrc_t rs, LAS char* dst, int voff, int soff) {
  const unsigned d = (unsigned)(size_t)dst;
  if (AUX) asm volatile("s_mov_b32 m0, %0\n\ts_nop 0\n\tbuffer_load_dwordx4 %1, %2, %3 offen sc1 lds" ::"s"(d), "v"(voff), "s"(rs), "s"(soff) : "memory");
  else asm volatile("s_mov_b32 m0, %0\n\ts_nop 0\n\tbuffer_load_dwordx4 %1, %2, %3 offen lds" ::"s"(d), "v"(voff), "s"(rs), "s"(soff) : "memory");
}
__device__ __forceinline__ void swap16(float& a, float& b) {
  auto r = __builtin_amdgcn_permlane16_swap(__builtin_bit_cast(unsigned, a), __builtin_bit_cast(unsigned, b), false, false);
  a = __builtin_bit_cast(float, (unsigned)r[0]); b = __builtin_bit_cast(float, (unsigned)r[1]);
}
__device__ __forceinline__ void swap32(float& a, float& b) {
  auto r = __builtin_amdgcn_permlane32_swap(__builtin_bit_cast(unsigned, a), __builtin_bit_cast(unsigned, b), false, false);
  a = __builtin_bit_cast(float, (unsigned)r[0]); b = __builtin_bit_cast(float, (unsigned)r[1]);
}
__device__ __forceinline__ float xsum1632(float v) {
  float a = v, b = v; swap16(a, b); v = a + b;
  a = v; b = v; swap32(a, b); return a + b;
}
__device__ __forceinline__ float xmax1632(float v) {
  float a = v, b = v; swap16(a, b); v = __builtin_fmaxf(a, b);
  a = v; b = v; swap32(a, b); return __builtin_fmaxf(a, b);
}
template <int CTRL> __device__ __forceinline__ float dppf(float v) { return __builtin_bit_cast(float, __builtin_amdgcn_mov_dpp(__builtin_bit_cast(int, v), CTRL, 0xf, 0xf, true)); }
__device__ __forceinline__ float ror8(float v) { return dppf<0x128>(v); }
__device__ __forceinline__ float gelu_tanh(float x) {
  const float u = x * (1.0f + 0.044715f * x * x) * (-2.0f * 0.7978845608028654f * 1.4426950408889634f);
  return x * __builtin_amdgcn_rcpf(1.0f + __builtin_amdgcn_exp2f(u));
}
__device__ __forceinline__ int xoff(KP p) { return (int)((((size_t)(p->ws + OFF_X) + PG - 1) & ~(size_t)(PG - 1)) - (size_t)p->ws); }
__device__ __forceinline__ unsigned xcc_id() { unsigned x; asm volatile("s_getreg_b32 %0, hwreg(HW_REG_XCC_ID)" : "=s"(x)); return x & 7u; }
#define WAITV(n) asm volatile("s_waitcnt vmcnt(" #n ")" ::: "memory")
#define WAITL(n) asm volatile("s_waitcnt lgkmcnt(" #n ")" ::: "memory")
template <int N> __device__ __forceinline__ void waitv() { asm volatile("s_waitcnt vmcnt(%0)" ::"i"(N) : "memory"); }
__device__ __forceinline__ void bar() { asm volatile("" ::: "memory"); __builtin_amdgcn_s_barrier(); asm volatile("" ::: "memory"); }
#define SCHED() __builtin_amdgcn_sched_barrier(0)
__device__ __forceinline__ int opaque(int x) { asm volatile("" : "+v"(x)); return x; }
__device__ __forceinline__ int sgpr(int x) { int r; asm volatile("s_nop 0\n\tv_readfirstlane_b32 %0, %1\n\ts_nop 4" : "=s"(r) : "v"(x)); return r; }
__device__ __forceinline__ u32x4 ld128(rsrc_t rs, int voff, int soff) { return __builtin_amdgcn_raw_buffer_load_b128(rs, voff, soff, AUX_SC1); }
__device__ __forceinline__ u32x2 ld64(rsrc_t rs, int voff, int soff) { return __builtin_amdgcn_raw_buffer_load_b64(rs, voff, soff, AUX_SC1); }
__device__ __forceinline__ void st128(u32x4 v, rsrc_t rs, int voff, int soff) {
  __builtin_amdgcn_raw_buffer_store_b128(v, rs, voff, soff, AUX_SC1);
  asm volatile("s_nop 1" ::"v"(v) : "memory");
}
__device__ __forceinline__ void st64(u32x2 v, rsrc_t rs, int voff, int soff) { __builtin_amdgcn_raw_buffer_store_b64(v, rs, voff, soff, AUX_SC1); }
__device__ __forceinline__ u32x4 pk8(f32x4 a, f32x4 b) { return u32x4{pk(a[0], a[1]), pk(a[2], a[3]), pk(b[0], b[1]), pk(b[2], b[3])}; }
__device__ __forceinline__ void unpk8(u32x4 u, f32x4& a, f32x4& b) {
  a = f32x4{bflo(u[0]), bfhi(u[0]), bflo(u[1]), bfhi(u[1])}; b = f32x4{bflo(u[2]), bfhi(u[2]), bflo(u[3]), bfhi(u[3])};
}
__device__ __forceinline__ void rnd8(f32x4& a, f32x4& b) { unpk8(pk8(a, b), a, b); }
__device__ __forceinline__ void add8(f32x4& a, f32x4& b, u32x4 r) { f32x4 x, y; unpk8(r, x, y); a += x; b += y; }
__device__ __forceinline__ u32x2 pk4(f32x4 a) { u32x2 u = {pk(a[0], a[1]), pk(a[2], a[3])}; asm("" : "+v"(u)); return u; }
__device__ __forceinline__ f32x4 unpk4(u32x2 u) { return f32x4{bflo(u.x), bfhi(u.x), bflo(u.y), bfhi(u.y)}; }
__device__ __forceinline__ f32x4 rnd4(f32x4 a) { return unpk4(pk4(a)); }
__device__ __forceinline__ int lane_id() { int l; asm volatile("v_mbcnt_lo_u32_b32 %0, -1, 0\n\tv_mbcnt_hi_u32_b32 %0, -1, %0" : "=v"(l)); return l; }
#define RLX_AGENT __ATOMIC_RELAXED, __HIP_MEMORY_SCOPE_AGENT

constexpr int C_TMO = 0, C_H = 1  , C_LINES = 17, C_PROBE = 48  ;
constexpr int R_WO = 0, R_W4 = 32, R_W3 = 64, R_LN3 = 120, R_W1 = 128, R_W2 = 144, R_LN2 = 160, R_FIN = 176, R_QK = 192;
__device__ __forceinline__ unsigned* cword(KP p, int line) { return (unsigned*)(p->ws + OFF_CTRL) + line * 32; }
__device__ __forceinline__ bool none(bool x) { return __builtin_amdgcn_ballot_w64(x) == 0ull; }
__device__ __forceinline__ void post(KP p, int role, unsigned v) {
  const int l = lane_id();
  if (l < 8) __hip_atomic_store((unsigned*)(p->ws + OFF_FLAG + (role * 8 + l) * 128), v, RLX_AGENT);
}
constexpr unsigned SPIN_CAP = 400000u;
#define WSLEEP 2
#define DMAALL 1
#define PRIO 1
#define FPOS 12
#define PACEW 1

#define WSTAMPS 0
#define WSTAMP(role, u, k)
#define TS(k)
#define TS_ARG
#define TS_DECL
#define TS_OUT(role, u)

__device__ __forceinline__ void touch(unsigned& d, rsrc_t rs, int voff, int soff) {
  asm volatile("buffer_load_dword %0, %1, %2, %3 offen" : "+v"(d) : "v"(voff), "s"(rs), "s"(soff) : "memory");
}
__global__ __launch_bounds__(512) void k_pre(Params) {
  const KP p = kparams();
  __shared__ float red[128][33];
  const int tid = threadIdx.x, b = blockIdx.x;
  if (b < 192) {
    const int cg = tid & 3, kr = tid >> 2;
    float acc[8];
#pragma unroll
    for (int i = 0; i < 8; ++i) acc[i] = 0.f;
    u32x4 w[8];
#pragma unroll
    for (int it = 0; it < 8; ++it) w[it] = *(const u32x4*)(p->wmod + (size_t)(it * 128 + kr) * 6144 + b * 32 + cg * 8);
    unsigned short cs[8];
#pragma unroll
    for (int it = 0; it < 8; ++it) cs[it] = p->c[it * 128 + kr];
#pragma unroll
    for (int it = 0; it < 8; ++it) {
      const float cv = bf2f(cs[it]);
      const float sv = rbf(cv / (1.0f + __expf(-cv)));
#pragma unroll
      for (int i = 0; i < 4; ++i) { acc[2 * i] += sv * bflo(w[it][i]); acc[2 * i + 1] += sv * bfhi(w[it][i]); }
    }
#pragma unroll
    for (int i = 0; i < 8; ++i) red[kr][cg * 8 + i] = acc[i];
    __syncthreads();
    if (tid < 128) {
      float sum = 0.f;
#pragma unroll
      for (int r = 0; r < 32; ++r) sum += red[(tid >> 5) * 32 + r][tid & 31];
      red[(tid >> 5) * 32][tid & 31] = sum;
    }
    __syncthreads();
    if (tid < 32) ((float*)(p->ws + OFF_MOD))[b * 32 + tid] = rbf((red[0][tid] + red[32][tid]) + (red[64][tid] + red[96][tid]));
  } else if (b < 202) {
    if (tid < 64) {
      const int v = b - 192;
      unsigned char* q = p->ws + xoff(p) + (v / 5) * PG + (v % 5) * 131072 + 1048576;
      if (tid < 10) __hip_atomic_store((unsigned*)(q + (tid & 1) * 8192 + (tid >> 1) * 16384), SENT, RLX_AGENT);
      WAITV(0);
      unsigned best[2] = {1u << 30, 1u << 30};
#pragma unroll
      for (int r = 0; r < 10; ++r) {
        unsigned d; unsigned long long t0, t1;
        asm volatile("s_memtime %0\n\ts_waitcnt lgkmcnt(0)" : "=s"(t0)::"memory");
        asm volatile("global_load_dword %0, %1, off sc1\n\ts_waitcnt vmcnt(0)" : "=v"(d) : "v"(q + (r & 1) * 8192 + (r >> 1) * 16384) : "memory");
        asm volatile("s_memtime %0\n\ts_waitcnt lgkmcnt(0)" : "=s"(t1)::"memory");
        best[r & 1] = min(best[r & 1], (unsigned)(t1 - t0));
      }
      if (tid == 0) cword(p, C_PROBE)[v] = (unsigned)(best[1] < best[0]) ^ (xcc_id() >> 2);
    }
  } else if (b == 202) {
    const u32x4 z = {0u, 0u, 0u, 0u};
#pragma unroll
    for (int i = 0; i < 4; ++i) {
      *(u32x4*)(p->ws + OFF_G + (i * 512 + tid) * 16) = z;
      *(u32x4*)(p->ws + OFF_G + 129 * 32768 + (i * 512 + tid) * 16) = z;
      *(unsigned*)(p->ws + OFF_FLAG + (i * 512 + tid) * 128) = 0u;
    }
    *(unsigned*)(p->ws + OFF_PROD + tid * 128) = 0u;
    if (tid < C_LINES) *cword(p, tid) = 0u;
  } else {
    const u32x4 sv = {SENT, SENT, SENT, SENT};
    unsigned char* x = p->ws + xoff(p);
    for (int i = (b - 203) * 512 + tid; i < (802816 + PG / 2) / 16; i += 53 * 512) {
      const int o = i * 16;
      *(u32x4*)(x + (o < 802816 ? o : o + (PG - 802816))) = sv;
    }
  }
}

__device__ __forceinline__ float wsum(float v) {
  v += dppf<0xB1>(v);
  v += dppf<0x4E>(v);
  v += dppf<0x124>(v);
  v += dppf<0x128>(v);
  return xsum1632(v);
}
struct Row { f32x4 v[4]; };
__device__ __forceinline__ float hsum(f32x4 a) { return (a[0] + a[1]) + (a[2] + a[3]); }
__device__ __forceinline__ void norm_row(Row& r) {
  const float mean = wsum(hsum((r.v[0] + r.v[1]) + (r.v[2] + r.v[3]))) * (1.0f / DM);
  f32x4 q = {0.f, 0.f, 0.f, 0.f};
#pragma unroll
  for (int i = 0; i < 4; ++i) { const f32x4 d = r.v[i] - mean; q += d * d; }
  const float sd = rbf(__builtin_amdgcn_sqrtf(rbf(rbf(wsum(hsum(q)) * (1.0f / DM)) + LN_EPS))), mb = rbf(mean), rs = __builtin_amdgcn_rcpf(sd);
#pragma unroll
  for (int i = 0; i < 4; ++i) r.v[i] = rnd4(r.v[i] - mb);
#pragma unroll
  for (int i = 0; i < 4; ++i) { const f32x4 t = r.v[i] * rs; r.v[i] = __builtin_elementwise_fma(__builtin_elementwise_fma(-t, (f32x4)sd, r.v[i]), (f32x4)rs, t); }
}
template <int NR> __device__ __forceinline__ void ln1_rows(KP p, int row, int step) {
  const int l16 = lane_id() * 16;
  u32x4 a[NR], b[NR];
#pragma unroll
  for (int r = 0; r < NR; ++r) {
    const unsigned char* src = (const unsigned char*)p->x + (row + r * step) * 2048 + l16;
    a[r] = *(const u32x4*)src; b[r] = *(const u32x4*)(src + 1024);
  }
  const float* sp = (const float*)(p->ws + OFF_MOD) + l16 / 2;
  Row sc, sh;
#pragma unroll
  for (int i = 0; i < 4; ++i) { sh.v[i] = *(const f32x4*)(sp + (i >> 1) * 512 + (i & 1) * 4); sc.v[i] = rnd4(*(const f32x4*)(sp + DM + (i >> 1) * 512 + (i & 1) * 4) + 1.0f); }
#pragma unroll
  for (int r = 0; r < NR; ++r) {
    Row v;
    unpk8(a[r], v.v[0], v.v[1]); unpk8(b[r], v.v[2], v.v[3]);
    norm_row(v);
#pragma unroll
    for (int i = 0; i < 4; ++i) v.v[i] = rnd4(rnd4(v.v[i]) * sc.v[i]) + sh.v[i];
    a[r] = pk8(v.v[0], v.v[1]); b[r] = pk8(v.v[2], v.v[3]);
  }
#pragma unroll
  for (int r = 0; r < NR; ++r) { st128(a[r], wsr(p), l16, OFF_H + (row + r * step) * 2048); st128(b[r], wsr(p), l16 + 1024, OFF_H + (row + r * step) * 2048); }
}

extern __shared__ __attribute__((aligned(16))) char smem[];

template <int BM, int BN, class TILE> __device__ __forceinline__ void qkv_tiles(LAS char* lds, int w, int ntile, TILE tile  ) {
  constexpr int WN = 4, D = BN == 256 ? 2 : 3, NK = 16, TM = BM / 32, TN = BN / WN / 16;
  constexpr int A_BYTES = BM * 128, B_BYTES = BN * 128, B_BASE = D * A_BYTES;
  constexpr int LA = BM / 64, LB = BN / 64, LD = LA + LB;
  constexpr int RB = BN * 2;
  constexpr int NMMA = TM * TN, NRD = TM + 2 * TN, BROWS = 8192 / RB;
  typedef const __attribute__((address_space(4))) char* KC;
  const KP p = kparams();
  const rsrc_t rw = wsr(p);
  const int l = lane_id(), r16 = l & 15, g = l >> 4, wm = w / WN, wn = w % WN;
  const int va = (8 * w + (l >> 3)) * 2048 + (((l & 7) ^ ((4 * w + (l >> 4)) & 7)) << 4);
  int vb;
  if (BN == 256) { const int k = 2 * w + (l >> 5), f = (k & 3) | (((k >> 3) & 1) << 2); vb = k * 8192 + (((l & 31) ^ (f << 1)) << 4); }
  else { const int k = 4 * w + (l >> 4), f = (k & 3) | (((k >> 3) & 1) << 2); vb = k * 8192 + (((l & 15) ^ (f << 1)) << 4); }
  auto wptr = [&](int code) { return *(const bf16_t* const __attribute__((address_space(4)))*)((KC)p + 16 + ((code >> 5) & 3) * 8); };
  const bf16_t* lw;
  int la, lb;
  auto aim = [&](int code) { lw = wptr(code); la = OFF_H + (code >> 7) * 128 * 2048; lb = (code & 31) * 256; };
  auto pa = [&](int i, int slot) { glds<AUX_SC1>(rw, lds + slot * A_BYTES + w * 1024 + i * 8192, va, la + i * 64 * 2048); };
  auto pb = [&](int i, int slot) { glds<0>(mkrsrc(lw, W_BYTES), lds + B_BASE + slot * B_BYTES + w * 1024 + i * 8192, vb, lb + i * BROWS * 8192); };
  const int fa = (wm * TM * 16 + r16) * 128 + ((g ^ (r16 >> 1)) << 4);
  int fb[TN];
#pragma unroll
  for (int ni = 0; ni < TN; ++ni) fb[ni] = B_BASE + (8 * g + (r16 >> 2)) * RB + (((wn * TN + ni) ^ ((r16 >> 2) | ((g & 1) << 2))) << 5) + (r16 & 3) * 8;
  u32x4 A0[TM], A1[TM];
  u32x2 B0[TN][2], B1[TN][2];
  int sa = 0, sb = 0;
  auto rd = [&](int i, int ks, u32x4 (&Af)[TM], u32x2 (&Bf)[TN][2]) {
    if (i < 2 * TN) Bf[i >> 1][i & 1] = trread(lds + sb * B_BYTES + fb[i >> 1] + (32 * ks + 4 * (i & 1)) * RB);
    else Af[i - 2 * TN] = lds128(lds + sa * A_BYTES + (fa ^ (ks * 64)) + (i - 2 * TN) * 2048);
  };
  auto count = [&](int code) {
    const int l = lane_id();
    if (l < BM / 128 * (BN / 128)) __hip_atomic_fetch_add((unsigned*)(p->ws + OFF_PROD + (((code >> 7) + (l >> 1)) * 32 + (code & 31) + (l & 1)) * 128), 1u, RLX_AGENT);
  };
  int code = sgpr(tile(0)), prev = -1;
  aim(code);
#pragma nounroll
  for (int s = 0; s < D; ++s) {
#pragma unroll
    for (int i = 0; i < LB; ++i) pb(i, s);
#pragma unroll
    for (int i = 0; i < LA; ++i) pa(i, s);
    la += 128; lb += 64 * 8192;
  }
  waitv<(D - 1) * LD>();
  bar();
#pragma unroll
  for (int i = 0; i < NRD; ++i) rd(i, 0, A0, B0);
#pragma nounroll
  for (int t = 0; t < ntile; ++t) {
    TS_DECL;
    TS(0);
    const int next = t + 1 < ntile ? sgpr(tile(t + 1)) : -1;
    f32x4 acc[TN][TM];
#pragma unroll
    for (int i = 0; i < TN; ++i)
#pragma unroll
      for (int j = 0; j < TM; ++j) acc[i][j] = f32x4{0.f, 0.f, 0.f, 0.f};
    {
      auto mm = [&](int i, const u32x4 (&Af)[TM], const u32x2 (&Bf)[TN][2]) {
        const int mi = i / TN, ni = i % TN;
        acc[ni][mi] = mfma(u32x4{Bf[ni][0].x, Bf[ni][0].y, Bf[ni][1].x, Bf[ni][1].y}, Af[mi], acc[ni][mi]);
      };
#pragma nounroll
      for (int kt = 0; kt < NK; ++kt) {
        if (kt + D == NK) aim(next);
        const bool more = kt + D < NK || next >= 0;
        const int fbs = sb == 0 ? D : sb - 1;
#pragma unroll
        for (int i = 0; i < NMMA; ++i) {
          mm(i, A0, B0);
#pragma unroll
          for (int r = 0; r < NRD; ++r) if (r * (NMMA * 3 / 4) / NRD == i) rd(r, 1, A1, B1);
#pragma unroll
          for (int r = 0; r < LB; ++r) if ((r * NMMA + NMMA / 2) / LB == i && more) pb(r, fbs);
          SCHED();
        }
        WAITL(0);
        if (more) waitv<(D - 2) * LD + LB>(); else WAITV(0);
        bar();
        if (kt == 1 && prev >= 0 && w == 0) count(prev);
        const int fas = sa;
        sa = sa + 1 == D ? 0 : sa + 1;
        sb = sb == D ? 0 : sb + 1;
#pragma unroll
        for (int i = 0; i < NMMA; ++i) {
          mm(i, A1, B1);
#pragma unroll
          for (int r = 0; r < NRD; ++r) if (r * (NMMA * 3 / 4) / NRD == i) rd(r, 0, A0, B0);
#pragma unroll
          for (int r = 0; r < LA; ++r) if (r * NMMA / (2 * LA) + 1 == i && more) pa(r, fas);
          SCHED();
        }
        la += 128; lb += 64 * 8192;
      }
    }
    TS(4);
    const int le = lane_id(), l16 = le * 16;
    const int which = (code >> 5) & 3;
    const int so = OFF_QKV + ((code & 31) + (BN == 256 ? wn >> 1 : 0)) * QKV_H + ((code >> 7) * 4 + wm * (TM / 2)) * QKV_S + which * 8192;
    const int c16 = (wn * TN) & 7;
    if (which == 2) {
      LAS char* tp = lds + B_BASE + (sb == 0 ? D : sb - 1) * B_BYTES + w * 1024;
      LAS char* wp = tp + (le & 15) * 64;
      const int wx = ((le >> 4) ^ (le & 7)) * 8, rr = le >> 2;
      LAS const char* rp = tp + rr * 64;
      const int rx = ((le & 3) ^ (rr & 7)) * 8;
#pragma unroll
      for (int sg = 0; sg < TM / 2; ++sg)
#pragma unroll
        for (int np = 0; np < TN / 2; ++np) {
#pragma unroll
          for (int j = 0; j < 4; ++j) *(LAS u32x2*)(wp + (j >> 1) * 8192 + (wx ^ ((j & 1) * 32))) = pk4(acc[2 * np + (j & 1)][2 * sg + (j >> 1)]);
          asm volatile("" ::: "memory");
#pragma unroll
          for (int j = 0; j < 2; ++j) {
            const u32x2 lo = trread(rp + (rx ^ (j * 32))), hi = trread(rp + 8192 + (rx ^ (j * 32)));
            st128(u32x4{lo.x, lo.y, hi.x, hi.y}, rw, l16, so + sg * QKV_S + (c16 + 2 * np + j) * 1024);
          }
        }
    } else {
#pragma unroll
      for (int mi = 0; mi < TM; ++mi)
#pragma unroll
        for (int np = 0; np < TN / 2; ++np) st128(pk8(acc[2 * np][mi], acc[2 * np + 1][mi]), rw, l16, so + (mi >> 1) * QKV_S + ((mi & 1) * 4 + (c16 >> 1) + np) * 1024);
    }
    TS(1);
    TS_OUT(240 + (code >> 7), (code & 127) >> 1);
    prev = code;
    code = next;
  }
  WAITV(0);
  WAITL(0);
  bar();
  if (w == 0) count(prev);
}

constexpr int NREG = 24, NLDS = 8;
constexpr int CL_M = 0;
constexpr int CL_KX = 16384;
constexpr int CL_IMG = 24576;
constexpr int CL_PX = 28672;
constexpr int CL_W = 32768;
constexpr int LDS_BYTES = 163840;

__device__ __forceinline__ void wstage(rsrc_t rw, int soff, LAS char* dst, int w, int l) {
  const int r = 4 * w + (l >> 4);
  const int f = (r & 1) | (((r >> 2) & 3) << 1);
  const int v = r * 8192 + (((l & 15) ^ (f << 1)) << 4);
  glds<0>(rw, dst + w * 1024, v, soff);
  glds<0>(rw, dst + 8192 + w * 1024, v, soff + 32 * 8192);
}
__device__ __forceinline__ u32x8 wfrag(LAS const char* img, int nt, int l) {
  const int g = l >> 4, q = (l & 15) >> 2, pp = l & 3;
  const int rx = 16 * (g >> 1) + 4 * (g & 1) + (q & 1) + 8 * (q >> 1);
  const int f = (rx & 1) | (((rx >> 2) & 3) << 1);
  LAS const char* a = img + rx * 256 + ((nt ^ f) << 5) + pp * 8;
  const u32x2 x0 = trread(a), x1 = trread(a + 2 * 256), x2 = trread(a + 32 * 256), x3 = trread(a + 34 * 256);
  return u32x8{x0.x, x0.y, x1.x, x1.y, x2.x, x2.y, x3.x, x3.y};
}
__device__ __forceinline__ void put_frag(LAS char* d, u32x8 f) {
  *(LAS u32x4*)d = u32x4{f[0], f[1], f[2], f[3]};
  *(LAS u32x4*)(d + 1024) = u32x4{f[4], f[5], f[6], f[7]};
}
template <int NB, int NREGB, class SRC> __device__ __forceinline__ void load_ring(LAS char* lds, int w, int l, SRC src, u32x8* out) {
#pragma unroll
  for (int n = 0; n < 8; ++n) src(n, lds + CL_W + n * 16384);
#pragma unroll
  for (int n = 0; n < NB; n += 2) {
    constexpr int dummy = 0; (void)dummy;
    const int left = NB - n - 2 < 6 ? NB - n - 2 : 6;
    if (left == 6) WAITV(12); else if (left == 4) WAITV(8); else if (left == 2) WAITV(4); else WAITV(0);
    bar();
    const u32x8 f0 = wfrag(lds + CL_W + (n & 7) * 16384, w, l), f1 = wfrag(lds + CL_W + ((n + 1) & 7) * 16384, w, l);
    WAITL(0);
    bar();
    if (n + 8 < NB) { src(n + 8, lds + CL_W + (n & 7) * 16384); src(n + 9, lds + CL_W + ((n + 1) & 7) * 16384); }
    if (n < NREGB) { out[n < NREGB ? n : 0] = f0; out[n < NREGB ? n + 1 : 0] = f1; }
    else { put_frag(lds + CL_W + (n & 7) * 16384 + w * 2048 + l * 16, f0); put_frag(lds + CL_W + ((n + 1) & 7) * 16384 + w * 2048 + l * 16, f1); }
    SCHED();
  }
}
__device__ __forceinline__ void stage_m0(KP p, LAS char* lds, int w, int l) {
#pragma unroll
  for (int i = 0; i < 2; ++i)
    *(LAS u32x4*)(lds + CL_M + (2 * w + i) * 1024 + l * 16) = *(const u32x4*)(p->mem0 + (l & 7) * DM + 64 * (2 * w + i) + 16 * (l >> 4) + 8 * ((l >> 3) & 1));
  WAITL(0);
}

__device__ __forceinline__ bool qk_loop(const u32x8 (&W)[NREG], rsrc_t rw, u32x8 sc, int fo, u32x2 po) {
  int ok;
  asm volatile(
    "s_mov_b32 %0, 0\n"
    "s_mov_b32 s95, 0\n"
    "v_mbcnt_lo_u32_b32 v63, -1, 0\n"
    "v_mbcnt_hi_u32_b32 v63, -1, v63\n"
    "v_lshlrev_b32 v0, 4, v63\n"
    "v_and_b32 v62, 15, v63\n"
    "v_lshrrev_b32 v61, 4, v63\n"
    "v_and_b32 v60, 8, v63\n"
    "v_cmp_ne_u32 vcc, 0, v60\n"
    "v_mov_b32 v1, 0x4444\n"
    "v_mov_b32 v60, 0xeeee\n"
    "v_cndmask_b32 v1, v1, v60, vcc\n"
    "s_lshl_b32 s76, s68, 11\n"
    "s_add_u32 s76, s76, 32768\n"
    "v_add_u32 v2, s76, v0\n"
    "s_lshl_b32 s76, s68, 11\n"
    "v_add_u32 v3, s76, v0\n"
    "s_lshl_b32 s76, s68, 9\n"
    "s_add_u32 s76, s76, 24576\n"
    "v_lshlrev_b32 v10, 5, v62\n"
    "v_lshl_add_u32 v10, v61, 3, v10\n"
    "v_add_u32 v10, s76, v10\n"
    "v_and_b32 v11, 31, v63\n"
    "v_lshlrev_b32 v11, 3, v11\n"
    "v_and_b32 v60, 7, v63\n"
    "v_lshlrev_b32 v60, 4, v60\n"
    "v_lshl_add_u32 v12, v61, 7, v60\n"
    "v_add_u32 v12, 28672, v12\n"
    "v_and_b32 v59, 1, v61\n"
    "v_lshl_add_u32 v13, v59, 3, v60\n"
    "v_add_u32 v13, 29184, v13\n"
    "v_and_b32 v14, 3, v63\n"
    "v_lshlrev_b32 v14, 4, v14\n"
    "v_lshl_add_u32 v14, v59, 3, v14\n"
    "v_lshrrev_b32 v58, 1, v61\n"
    "v_lshl_add_u32 v14, v58, 6, v14\n"
    "v_lshrrev_b32 v57, 2, v62\n"
    "v_lshl_add_u32 v14, v57, 16, v14\n"
    "v_cmp_le_u32 s[78:79], 32, v63\n"
    "s_lshl_b32 s77, s68, 10\n"
    "s_add_u32 s98, s77, 16384\n"
    "s_sub_u32 s76, s68, 1\n"
    "s_lshl_b32 s76, s76, 4\n"
    "v_lshlrev_b32 v60, 2, v61\n"
    "v_sub_u32 v60, v62, v60\n"
    "v_add_u32 v60, s76, v60\n"
    "v_cmp_gt_i32 s[40:41], 0, v60\n"
    "v_cmp_gt_i32 s[48:49], 16, v60\n"
    "v_cmp_gt_i32 s[42:43], 1, v60\n"
    "v_cmp_gt_i32 s[50:51], 17, v60\n"
    "v_cmp_gt_i32 s[44:45], 2, v60\n"
    "v_cmp_gt_i32 s[52:53], 18, v60\n"
    "v_cmp_gt_i32 s[46:47], 3, v60\n"
    "v_cmp_gt_i32 s[54:55], 19, v60\n"
    "s_bfe_u32 s85, s99, 0x2000d\n"
    "s_add_u32 s85, s85, 4\n"
    "s_lshl_b32 s85, s85, 7\n"
    "s_add_u32 s85, s85, s99\n"
    "s_mov_b32 s80, 0\n"
    "s_mov_b32 s81, s73\n"
    "s_mov_b32 s82, s74\n"
    "s_mov_b32 s86, 0x3e0293ee\n"
    "s_mov_b32 s87, 0x3db504f3\n"
    "s_mov_b32 s88, 0x3fb8aa3b\n"
    "s_add_u32 s76, s81, s77\n"
    "s_add_u32 s76, s76, 8192\n"
    "s_mov_b32 m0, s98\n"
    "s_nop 0\n"
    "buffer_load_dwordx4 v0, s[64:67], s76 offen sc1 lds\n"
    "s_add_u32 s76, s76, 8192\n"
    "buffer_load_dwordx4 v[4:7], v0, s[64:67], s76 offen sc1\n"
    "ds_read_b128 v[56:59], v3\n"
    "ds_read_b128 v[60:63], v3 offset:1024\n"
    "s_waitcnt vmcnt(0)\n"
    "s_memrealtime s[96:97]\n"
    "v_mov_b32 v16, 29312\n"
    "s_waitcnt lgkmcnt(0)\n"
    "v_mov_b32 v18, s96\n"
    "v_mov_b32 v19, s96\n"
    "v_mov_b32 v20, 0\n"
    "ds_write_b64 v16, v[18:19]\n"
    "ds_write_b32 v16, v20 offset:16\n"
    "s_getpc_b64 s[56:57]\n"
    "pc0_%=:\n"
    "s_mul_i32 s76, s68, var1_%=-var0_%=\n"
    "s_add_u32 s76, s76, var0_%=-pc0_%=\n"
    "s_add_u32 s56, s56, s76\n"
    "s_addc_u32 s57, s57, 0\n"
    "s_and_b32 s76, s80, 1\n"
    "s_lshl_b32 s76, s76, 17\n"
    "s_add_u32 s83, s71, s76\n"
    "s_and_b32 s76, s80, 3\n"
    "s_lshl_b32 s76, s76, 18\n"
    "s_add_u32 s84, s72, s76\n"
    "s_mov_b32 s94, 0\n"
    "v_and_b32 v9, 0x33f, v0\n"
    "v_bfe_u32 v8, v0, 6, 1\n"
    "v_lshl_add_u32 v9, v8, 7, v9\n"
    "v_bfe_u32 v8, v0, 7, 1\n"
    "v_lshl_add_u32 v9, v8, 6, v9\n"
    "v_mov_b64 v[16:17], 0\n"
    "v_mov_b64 v[18:19], 0\n"
    "v_mov_b64 v[20:21], 0\n"
    "v_mov_b64 v[22:23], 0\n"
    "v_mov_b32 v8, s80\n"
    "v_add_u32 v15, 0x10000, v2\n"
    "s_cmp_eq_u32 s80, s80\n"
    "s_nop 0\n"
    "s_setpc_b64 s[56:57]\n"
    "var0_%=:\n"
    "ds_read_b128 v[40:43], v2 offset:0\n"
    "ds_read_b128 v[44:47], v2 offset:1024\n"
    "ds_read_b128 v[48:51], v2 offset:16384\n"
    "ds_read_b128 v[52:55], v2 offset:17408\n"
    "s_cbranch_scc1 go0_%=\n"
    "s_nop 0\n"
    "mwait0_%=:\n"
    "s_memrealtime s[96:97]\n"
    "s_waitcnt lgkmcnt(0)\n"
    "s_sub_u32 s96, s92, s96\n"
    "s_sub_u32 s96, s96, 1\n"
    "s_cmp_lt_u32 s96, 1000\n"
    "s_cbranch_scc1 mwait0_%=\n"
    "mpoll0_%=:\n"
    "buffer_load_dwordx4 v[56:59], v9, s[64:67], s91 offen sc1\n"
    "buffer_load_dwordx4 v[60:63], v9, s[64:67], s91 offen offset:1024 sc1\n"
    "s_waitcnt vmcnt(1)\n"
    "ds_write_b128 v3, v[56:59]\n"
    "v_max3_u32 v24, v56, v57, v58\n"
    "s_waitcnt vmcnt(0)\n"
    "ds_write_b128 v3, v[60:63] offset:1024\n"
    "v_max3_u32 v24, v24, v59, v60\n"
    "v_max3_u32 v24, v24, v61, v62\n"
    "v_max_u32 v24, v24, v63\n"
    "v_cmp_eq_u32 vcc, -1, v24\n"
    "s_cbranch_vccnz mretry0_%=\n"
    "go0_%=:\n"
    "s_waitcnt lgkmcnt(0)\n"
    "s_barrier\n"
    "ds_read_b128 v[24:27], v0 offset:2048\n"
    "ds_read_b128 v[28:31], v0 offset:3072\n"
    "ds_read_b128 v[32:35], v0 offset:4096\n"
    "ds_read_b128 v[36:39], v0 offset:5120\n"
    "v_smfmac_f32_16x16x64_bf16 v[16:19], v[56:59], v[64:71], v1\n"
    "v_smfmac_f32_16x16x64_bf16 v[20:23], v[56:59], v[192:199], v1\n"
    "ds_read_b128 v[56:59], v0 offset:6144\n"
    "v_smfmac_f32_16x16x64_bf16 v[20:23], v[60:63], v[40:47], v1\n"
    "ds_read_b128 v[40:43], v2 offset:32768\n"
    "v_smfmac_f32_16x16x64_bf16 v[16:19], v[60:63], v[128:135], v1\n"
    "ds_read_b128 v[44:47], v2 offset:33792\n"
    "s_waitcnt lgkmcnt(6)\n"
    "v_smfmac_f32_16x16x64_bf16 v[16:19], v[24:27], v[72:79], v1\n"
    "ds_read_b128 v[60:63], v0 offset:7168\n"
    "v_smfmac_f32_16x16x64_bf16 v[20:23], v[24:27], v[200:207], v1\n"
    "ds_read_b128 v[24:27], v0 offset:8192\n"
    "s_waitcnt lgkmcnt(7)\n"
    "v_smfmac_f32_16x16x64_bf16 v[20:23], v[28:31], v[48:55], v1\n"
    "ds_read_b128 v[48:51], v2 offset:49152\n"
    "v_smfmac_f32_16x16x64_bf16 v[16:19], v[28:31], v[136:143], v1\n"
    "ds_read_b128 v[52:55], v2 offset:50176\n"
    "s_waitcnt lgkmcnt(8)\n"
    "v_smfmac_f32_16x16x64_bf16 v[16:19], v[32:35], v[80:87], v1\n"
    "ds_read_b128 v[28:31], v0 offset:9216\n"
    "v_smfmac_f32_16x16x64_bf16 v[20:23], v[32:35], v[208:215], v1\n"
    "ds_read_b128 v[32:35], v0 offset:10240\n"
    "s_waitcnt lgkmcnt(9)\n"
    "s_waitcnt lgkmcnt(6)\n"
    "v_smfmac_f32_16x16x64_bf16 v[20:23], v[36:39], v[40:47], v1\n"
    "ds_read_b128 v[40:43], v15 offset:0\n"
    "v_smfmac_f32_16x16x64_bf16 v[16:19], v[36:39], v[144:151], v1\n"
    "ds_read_b128 v[44:47], v15 offset:1024\n"
    "v_smfmac_f32_16x16x64_bf16 v[16:19], v[56:59], v[88:95], v1\n"
    "ds_read_b128 v[36:39], v0 offset:11264\n"
    "v_smfmac_f32_16x16x64_bf16 v[20:23], v[56:59], v[216:223], v1\n"
    "ds_read_b128 v[56:59], v0 offset:12288\n"
    "s_waitcnt lgkmcnt(9)\n"
    "s_waitcnt lgkmcnt(6)\n"
    "v_smfmac_f32_16x16x64_bf16 v[20:23], v[60:63], v[48:55], v1\n"
    "ds_read_b128 v[48:51], v15 offset:16384\n"
    "v_smfmac_f32_16x16x64_bf16 v[16:19], v[60:63], v[152:159], v1\n"
    "ds_read_b128 v[52:55], v15 offset:17408\n"
    "v_smfmac_f32_16x16x64_bf16 v[16:19], v[24:27], v[96:103], v1\n"
    "ds_read_b128 v[60:63], v0 offset:13312\n"
    "v_smfmac_f32_16x16x64_bf16 v[20:23], v[24:27], v[224:231], v1\n"
    "ds_read_b128 v[24:27], v0 offset:14336\n"
    "s_waitcnt lgkmcnt(9)\n"
    "s_waitcnt lgkmcnt(6)\n"
    "v_smfmac_f32_16x16x64_bf16 v[20:23], v[28:31], v[40:47], v1\n"
    "ds_read_b128 v[40:43], v15 offset:32768\n"
    "v_smfmac_f32_16x16x64_bf16 v[16:19], v[28:31], v[160:167], v1\n"
    "ds_read_b128 v[44:47], v15 offset:33792\n"
    "v_smfmac_f32_16x16x64_bf16 v[16:19], v[32:35], v[104:111], v1\n"
    "ds_read_b128 v[28:31], v0 offset:15360\n"
    "v_smfmac_f32_16x16x64_bf16 v[20:23], v[32:35], v[232:239], v1\n"
    "s_waitcnt lgkmcnt(8)\n"
    "s_waitcnt lgkmcnt(5)\n"
    "v_smfmac_f32_16x16x64_bf16 v[20:23], v[36:39], v[48:55], v1\n"
    "ds_read_b128 v[48:51], v15 offset:49152\n"
    "v_smfmac_f32_16x16x64_bf16 v[16:19], v[36:39], v[168:175], v1\n"
    "ds_read_b128 v[52:55], v15 offset:50176\n"
    "v_smfmac_f32_16x16x64_bf16 v[16:19], v[56:59], v[112:119], v1\n"
    "v_smfmac_f32_16x16x64_bf16 v[20:23], v[56:59], v[240:247], v1\n"
    "s_waitcnt lgkmcnt(6)\n"
    "s_waitcnt lgkmcnt(3)\n"
    "v_smfmac_f32_16x16x64_bf16 v[20:23], v[60:63], v[40:47], v1\n"
    "v_smfmac_f32_16x16x64_bf16 v[16:19], v[60:63], v[176:183], v1\n"
    "v_smfmac_f32_16x16x64_bf16 v[16:19], v[24:27], v[120:127], v1\n"
    "v_smfmac_f32_16x16x64_bf16 v[20:23], v[24:27], v[248:255], v1\n"
    "s_waitcnt lgkmcnt(2)\n"
    "s_waitcnt lgkmcnt(0)\n"
    "v_smfmac_f32_16x16x64_bf16 v[20:23], v[28:31], v[48:55], v1\n"
    "v_smfmac_f32_16x16x64_bf16 v[16:19], v[28:31], v[184:191], v1\n"
    "s_branch gemvdone_%=\n"
    "mretry0_%=:\n"
    "s_nop 7\n"
    "s_sleep 1\n"
    "s_add_u32 s94, s94, 1\n"
    "s_cmp_lt_u32 s94, 400000\n"
    "s_cbranch_scc1 mpoll0_%=\n"
    "s_branch fail_%=\n"
    "var1_%=:\n"
    "ds_read_b128 v[40:43], v2 offset:16384\n"
    "ds_read_b128 v[44:47], v2 offset:17408\n"
    "ds_read_b128 v[48:51], v2 offset:0\n"
    "ds_read_b128 v[52:55], v2 offset:1024\n"
    "s_cbranch_scc1 go1_%=\n"
    "s_nop 0\n"
    "mwait1_%=:\n"
    "s_memrealtime s[96:97]\n"
    "s_waitcnt lgkmcnt(0)\n"
    "s_sub_u32 s96, s92, s96\n"
    "s_sub_u32 s96, s96, 1\n"
    "s_cmp_lt_u32 s96, 1000\n"
    "s_cbranch_scc1 mwait1_%=\n"
    "mpoll1_%=:\n"
    "buffer_load_dwordx4 v[56:59], v9, s[64:67], s91 offen sc1\n"
    "buffer_load_dwordx4 v[60:63], v9, s[64:67], s91 offen offset:1024 sc1\n"
    "s_waitcnt vmcnt(1)\n"
    "ds_write_b128 v3, v[56:59]\n"
    "v_max3_u32 v24, v56, v57, v58\n"
    "s_waitcnt vmcnt(0)\n"
    "ds_write_b128 v3, v[60:63] offset:1024\n"
    "v_max3_u32 v24, v24, v59, v60\n"
    "v_max3_u32 v24, v24, v61, v62\n"
    "v_max_u32 v24, v24, v63\n"
    "v_cmp_eq_u32 vcc, -1, v24\n"
    "s_cbranch_vccnz mretry1_%=\n"
    "go1_%=:\n"
    "s_waitcnt lgkmcnt(0)\n"
    "s_barrier\n"
    "ds_read_b128 v[24:27], v0 offset:0\n"
    "ds_read_b128 v[28:31], v0 offset:1024\n"
    "ds_read_b128 v[32:35], v0 offset:4096\n"
    "ds_read_b128 v[36:39], v0 offset:5120\n"
    "v_smfmac_f32_16x16x64_bf16 v[16:19], v[56:59], v[72:79], v1\n"
    "v_smfmac_f32_16x16x64_bf16 v[20:23], v[56:59], v[200:207], v1\n"
    "ds_read_b128 v[56:59], v0 offset:6144\n"
    "v_smfmac_f32_16x16x64_bf16 v[20:23], v[60:63], v[40:47], v1\n"
    "ds_read_b128 v[40:43], v2 offset:32768\n"
    "v_smfmac_f32_16x16x64_bf16 v[16:19], v[60:63], v[136:143], v1\n"
    "ds_read_b128 v[44:47], v2 offset:33792\n"
    "s_waitcnt lgkmcnt(6)\n"
    "v_smfmac_f32_16x16x64_bf16 v[16:19], v[24:27], v[64:71], v1\n"
    "ds_read_b128 v[60:63], v0 offset:7168\n"
    "v_smfmac_f32_16x16x64_bf16 v[20:23], v[24:27], v[192:199], v1\n"
    "ds_read_b128 v[24:27], v0 offset:8192\n"
    "s_waitcnt lgkmcnt(7)\n"
    "v_smfmac_f32_16x16x64_bf16 v[20:23], v[28:31], v[48:55], v1\n"
    "ds_read_b128 v[48:51], v2 offset:49152\n"
    "v_smfmac_f32_16x16x64_bf16 v[16:19], v[28:31], v[128:135], v1\n"
    "ds_read_b128 v[52:55], v2 offset:50176\n"
    "s_waitcnt lgkmcnt(8)\n"
    "v_smfmac_f32_16x16x64_bf16 v[16:19], v[32:35], v[80:87], v1\n"
    "ds_read_b128 v[28:31], v0 offset:9216\n"
    "v_smfmac_f32_16x16x64_bf16 v[20:23], v[32:35], v[208:215], v1\n"
    "ds_read_b128 v[32:35], v0 offset:10240\n"
    "s_waitcnt lgkmcnt(9)\n"
    "s_waitcnt lgkmcnt(6)\n"
    "v_smfmac_f32_16x16x64_bf16 v[20:23], v[36:39], v[40:47], v1\n"
    "ds_read_b128 v[40:43], v15 offset:0\n"
    "v_smfmac_f32_16x16x64_bf16 v[16:19], v[36:39], v[144:151], v1\n"
    "ds_read_b128 v[44:47], v15 offset:1024\n"
    "v_smfmac_f32_16x16x64_bf16 v[16:19], v[56:59], v[88:95], v1\n"
    "ds_read_b128 v[36:39], v0 offset:11264\n"
    "v_smfmac_f32_16x16x64_bf16 v[20:23], v[56:59], v[216:223], v1\n"
    "ds_read_b128 v[56:59], v0 offset:12288\n"
    "s_waitcnt lgkmcnt(9)\n"
    "s_waitcnt lgkmcnt(6)\n"
    "v_smfmac_f32_16x16x64_bf16 v[20:23], v[60:63], v[48:55], v1\n"
    "ds_read_b128 v[48:51], v15 offset:16384\n"
    "v_smfmac_f32_16x16x64_bf16 v[16:19], v[60:63], v[152:159], v1\n"
    "ds_read_b128 v[52:55], v15 offset:17408\n"
    "v_smfmac_f32_16x16x64_bf16 v[16:19], v[24:27], v[96:103], v1\n"
    "ds_read_b128 v[60:63], v0 offset:13312\n"
    "v_smfmac_f32_16x16x64_bf16 v[20:23], v[24:27], v[224:231], v1\n"
    "ds_read_b128 v[24:27], v0 offset:14336\n"
    "s_waitcnt lgkmcnt(9)\n"
    "s_waitcnt lgkmcnt(6)\n"
    "v_smfmac_f32_16x16x64_bf16 v[20:23], v[28:31], v[40:47], v1\n"
    "ds_read_b128 v[40:43], v15 offset:32768\n"
    "v_smfmac_f32_16x16x64_bf16 v[16:19], v[28:31], v[160:167], v1\n"
    "ds_read_b128 v[44:47], v15 offset:33792\n"
    "v_smfmac_f32_16x16x64_bf16 v[16:19], v[32:35], v[104:111], v1\n"
    "ds_read_b128 v[28:31], v0 offset:15360\n"
    "v_smfmac_f32_16x16x64_bf16 v[20:23], v[32:35], v[232:239], v1\n"
    "s_waitcnt lgkmcnt(8)\n"
    "s_waitcnt lgkmcnt(5)\n"
    "v_smfmac_f32_16x16x64_bf16 v[20:23], v[36:39], v[48:55], v1\n"
    "ds_read_b128 v[48:51], v15 offset:49152\n"
    "v_smfmac_f32_16x16x64_bf16 v[16:19], v[36:39], v[168:175], v1\n"
    "ds_read_b128 v[52:55], v15 offset:50176\n"
    "v_smfmac_f32_16x16x64_bf16 v[16:19], v[56:59], v[112:119], v1\n"
    "v_smfmac_f32_16x16x64_bf16 v[20:23], v[56:59], v[240:247], v1\n"
    "s_waitcnt lgkmcnt(6)\n"
    "s_waitcnt lgkmcnt(3)\n"
    "v_smfmac_f32_16x16x64_bf16 v[20:23], v[60:63], v[40:47], v1\n"
    "v_smfmac_f32_16x16x64_bf16 v[16:19], v[60:63], v[176:183], v1\n"
    "v_smfmac_f32_16x16x64_bf16 v[16:19], v[24:27], v[120:127], v1\n"
    "v_smfmac_f32_16x16x64_bf16 v[20:23], v[24:27], v[248:255], v1\n"
    "s_waitcnt lgkmcnt(2)\n"
    "s_waitcnt lgkmcnt(0)\n"
    "v_smfmac_f32_16x16x64_bf16 v[20:23], v[28:31], v[48:55], v1\n"
    "v_smfmac_f32_16x16x64_bf16 v[16:19], v[28:31], v[184:191], v1\n"
    "s_branch gemvdone_%=\n"
    "mretry1_%=:\n"
    "s_nop 7\n"
    "s_sleep 1\n"
    "s_add_u32 s94, s94, 1\n"
    "s_cmp_lt_u32 s94, 400000\n"
    "s_cbranch_scc1 mpoll1_%=\n"
    "s_branch fail_%=\n"
    "var2_%=:\n"
    "ds_read_b128 v[40:43], v2 offset:32768\n"
    "ds_read_b128 v[44:47], v2 offset:33792\n"
    "ds_read_b128 v[48:51], v2 offset:0\n"
    "ds_read_b128 v[52:55], v2 offset:1024\n"
    "s_cbranch_scc1 go2_%=\n"
    "s_nop 0\n"
    "mwait2_%=:\n"
    "s_memrealtime s[96:97]\n"
    "s_waitcnt lgkmcnt(0)\n"
    "s_sub_u32 s96, s92, s96\n"
    "s_sub_u32 s96, s96, 1\n"
    "s_cmp_lt_u32 s96, 1000\n"
    "s_cbranch_scc1 mwait2_%=\n"
    "mpoll2_%=:\n"
    "buffer_load_dwordx4 v[56:59], v9, s[64:67], s91 offen sc1\n"
    "buffer_load_dwordx4 v[60:63], v9, s[64:67], s91 offen offset:1024 sc1\n"
    "s_waitcnt vmcnt(1)\n"
    "ds_write_b128 v3, v[56:59]\n"
    "v_max3_u32 v24, v56, v57, v58\n"
    "s_waitcnt vmcnt(0)\n"
    "ds_write_b128 v3, v[60:63] offset:1024\n"
    "v_max3_u32 v24, v24, v59, v60\n"
    "v_max3_u32 v24, v24, v61, v62\n"
    "v_max_u32 v24, v24, v63\n"
    "v_cmp_eq_u32 vcc, -1, v24\n"
    "s_cbranch_vccnz mretry2_%=\n"
    "go2_%=:\n"
    "s_waitcnt lgkmcnt(0)\n"
    "s_barrier\n"
    "ds_read_b128 v[24:27], v0 offset:0\n"
    "ds_read_b128 v[28:31], v0 offset:1024\n"
    "ds_read_b128 v[32:35], v0 offset:2048\n"
    "ds_read_b128 v[36:39], v0 offset:3072\n"
    "v_smfmac_f32_16x16x64_bf16 v[16:19], v[56:59], v[80:87], v1\n"
    "v_smfmac_f32_16x16x64_bf16 v[20:23], v[56:59], v[208:215], v1\n"
    "ds_read_b128 v[56:59], v0 offset:6144\n"
    "v_smfmac_f32_16x16x64_bf16 v[20:23], v[60:63], v[40:47], v1\n"
    "ds_read_b128 v[40:43], v2 offset:16384\n"
    "v_smfmac_f32_16x16x64_bf16 v[16:19], v[60:63], v[144:151], v1\n"
    "ds_read_b128 v[44:47], v2 offset:17408\n"
    "s_waitcnt lgkmcnt(6)\n"
    "v_smfmac_f32_16x16x64_bf16 v[16:19], v[24:27], v[64:71], v1\n"
    "ds_read_b128 v[60:63], v0 offset:7168\n"
    "v_smfmac_f32_16x16x64_bf16 v[20:23], v[24:27], v[192:199], v1\n"
    "ds_read_b128 v[24:27], v0 offset:8192\n"
    "s_waitcnt lgkmcnt(7)\n"
    "v_smfmac_f32_16x16x64_bf16 v[20:23], v[28:31], v[48:55], v1\n"
    "ds_read_b128 v[48:51], v2 offset:49152\n"
    "v_smfmac_f32_16x16x64_bf16 v[16:19], v[28:31], v[128:135], v1\n"
    "ds_read_b128 v[52:55], v2 offset:50176\n"
    "s_waitcnt lgkmcnt(8)\n"
    "v_smfmac_f32_16x16x64_bf16 v[16:19], v[32:35], v[72:79], v1\n"
    "ds_read_b128 v[28:31], v0 offset:9216\n"
    "v_smfmac_f32_16x16x64_bf16 v[20:23], v[32:35], v[200:207], v1\n"
    "ds_read_b128 v[32:35], v0 offset:10240\n"
    "s_waitcnt lgkmcnt(9)\n"
    "s_waitcnt lgkmcnt(6)\n"
    "v_smfmac_f32_16x16x64_bf16 v[20:23], v[36:39], v[40:47], v1\n"
    "ds_read_b128 v[40:43], v15 offset:0\n"
    "v_smfmac_f32_16x16x64_bf16 v[16:19], v[36:39], v[136:143], v1\n"
    "ds_read_b128 v[44:47], v15 offset:1024\n"
    "v_smfmac_f32_16x16x64_bf16 v[16:19], v[56:59], v[88:95], v1\n"
    "ds_read_b128 v[36:39], v0 offset:11264\n"
    "v_smfmac_f32_16x16x64_bf16 v[20:23], v[56:59], v[216:223], v1\n"
    "ds_read_b128 v[56:59], v0 offset:12288\n"
    "s_waitcnt lgkmcnt(9)\n"
    "s_waitcnt lgkmcnt(6)\n"
    "v_smfmac_f32_16x16x64_bf16 v[20:23], v[60:63], v[48:55], v1\n"
    "ds_read_b128 v[48:51], v15 offset:16384\n"
    "v_smfmac_f32_16x16x64_bf16 v[16:19], v[60:63], v[152:159], v1\n"
    "ds_read_b128 v[52:55], v15 offset:17408\n"
    "v_smfmac_f32_16x16x64_bf16 v[16:19], v[24:27], v[96:103], v1\n"
    "ds_read_b128 v[60:63], v0 offset:13312\n"
    "v_smfmac_f32_16x16x64_bf16 v[20:23], v[24:27], v[224:231], v1\n"
    "ds_read_b128 v[24:27], v0 offset:14336\n"
    "s_waitcnt lgkmcnt(9)\n"
    "s_waitcnt lgkmcnt(6)\n"
    "v_smfmac_f32_16x16x64_bf16 v[20:23], v[28:31], v[40:47], v1\n"
    "ds_read_b128 v[40:43], v15 offset:32768\n"
    "v_smfmac_f32_16x16x64_bf16 v[16:19], v[28:31], v[160:167], v1\n"
    "ds_read_b128 v[44:47], v15 offset:33792\n"
    "v_smfmac_f32_16x16x64_bf16 v[16:19], v[32:35], v[104:111], v1\n"
    "ds_read_b128 v[28:31], v0 offset:15360\n"
    "v_smfmac_f32_16x16x64_bf16 v[20:23], v[32:35], v[232:239], v1\n"
    "s_waitcnt lgkmcnt(8)\n"
    "s_waitcnt lgkmcnt(5)\n"
    "v_smfmac_f32_16x16x64_bf16 v[20:23], v[36:39], v[48:55], v1\n"
    "ds_read_b128 v[48:51], v15 offset:49152\n"
    "v_smfmac_f32_16x16x64_bf16 v[16:19], v[36:39], v[168:175], v1\n"
    "ds_read_b128 v[52:55], v15 offset:50176\n"
    "v_smfmac_f32_16x16x64_bf16 v[16:19], v[56:59], v[112:119], v1\n"
    "v_smfmac_f32_16x16x64_bf16 v[20:23], v[56:59], v[240:247], v1\n"
    "s_waitcnt lgkmcnt(6)\n"
    "s_waitcnt lgkmcnt(3)\n"
    "v_smfmac_f32_16x16x64_bf16 v[20:23], v[60:63], v[40:47], v1\n"
    "v_smfmac_f32_16x16x64_bf16 v[16:19], v[60:63], v[176:183], v1\n"
    "v_smfmac_f32_16x16x64_bf16 v[16:19], v[24:27], v[120:127], v1\n"
    "v_smfmac_f32_16x16x64_bf16 v[20:23], v[24:27], v[248:255], v1\n"
    "s_waitcnt lgkmcnt(2)\n"
    "s_waitcnt lgkmcnt(0)\n"
    "v_smfmac_f32_16x16x64_bf16 v[20:23], v[28:31], v[48:55], v1\n"
    "v_smfmac_f32_16x16x64_bf16 v[16:19], v[28:31], v[184:191], v1\n"
    "s_branch gemvdone_%=\n"
    "mretry2_%=:\n"
    "s_nop 7\n"
    "s_sleep 1\n"
    "s_add_u32 s94, s94, 1\n"
    "s_cmp_lt_u32 s94, 400000\n"
    "s_cbranch_scc1 mpoll2_%=\n"
    "s_branch fail_%=\n"
    "var3_%=:\n"
    "ds_read_b128 v[40:43], v2 offset:49152\n"
    "ds_read_b128 v[44:47], v2 offset:50176\n"
    "ds_read_b128 v[48:51], v2 offset:0\n"
    "ds_read_b128 v[52:55], v2 offset:1024\n"
    "s_cbranch_scc1 go3_%=\n"
    "s_nop 0\n"
    "mwait3_%=:\n"
    "s_memrealtime s[96:97]\n"
    "s_waitcnt lgkmcnt(0)\n"
    "s_sub_u32 s96, s92, s96\n"
    "s_sub_u32 s96, s96, 1\n"
    "s_cmp_lt_u32 s96, 1000\n"
    "s_cbranch_scc1 mwait3_%=\n"
    "mpoll3_%=:\n"
    "buffer_load_dwordx4 v[56:59], v9, s[64:67], s91 offen sc1\n"
    "buffer_load_dwordx4 v[60:63], v9, s[64:67], s91 offen offset:1024 sc1\n"
    "s_waitcnt vmcnt(1)\n"
    "ds_write_b128 v3, v[56:59]\n"
    "v_max3_u32 v24, v56, v57, v58\n"
    "s_waitcnt vmcnt(0)\n"
    "ds_write_b128 v3, v[60:63] offset:1024\n"
    "v_max3_u32 v24, v24, v59, v60\n"
    "v_max3_u32 v24, v24, v61, v62\n"
    "v_max_u32 v24, v24, v63\n"
    "v_cmp_eq_u32 vcc, -1, v24\n"
    "s_cbranch_vccnz mretry3_%=\n"
    "go3_%=:\n"
    "s_waitcnt lgkmcnt(0)\n"
    "s_barrier\n"
    "ds_read_b128 v[24:27], v0 offset:0\n"
    "ds_read_b128 v[28:31], v0 offset:1024\n"
    "ds_read_b128 v[32:35], v0 offset:2048\n"
    "ds_read_b128 v[36:39], v0 offset:3072\n"
    "v_smfmac_f32_16x16x64_bf16 v[16:19], v[56:59], v[88:95], v1\n"
    "v_smfmac_f32_16x16x64_bf16 v[20:23], v[56:59], v[216:223], v1\n"
    "ds_read_b128 v[56:59], v0 offset:4096\n"
    "v_smfmac_f32_16x16x64_bf16 v[20:23], v[60:63], v[40:47], v1\n"
    "ds_read_b128 v[40:43], v2 offset:16384\n"
    "v_smfmac_f32_16x16x64_bf16 v[16:19], v[60:63], v[152:159], v1\n"
    "ds_read_b128 v[44:47], v2 offset:17408\n"
    "s_waitcnt lgkmcnt(6)\n"
    "v_smfmac_f32_16x16x64_bf16 v[16:19], v[24:27], v[64:71], v1\n"
    "ds_read_b128 v[60:63], v0 offset:5120\n"
    "v_smfmac_f32_16x16x64_bf16 v[20:23], v[24:27], v[192:199], v1\n"
    "ds_read_b128 v[24:27], v0 offset:8192\n"
    "s_waitcnt lgkmcnt(7)\n"
    "v_smfmac_f32_16x16x64_bf16 v[20:23], v[28:31], v[48:55], v1\n"
    "ds_read_b128 v[48:51], v2 offset:32768\n"
    "v_smfmac_f32_16x16x64_bf16 v[16:19], v[28:31], v[128:135], v1\n"
    "ds_read_b128 v[52:55], v2 offset:33792\n"
    "s_waitcnt lgkmcnt(8)\n"
    "v_smfmac_f32_16x16x64_bf16 v[16:19], v[32:35], v[72:79], v1\n"
    "ds_read_b128 v[28:31], v0 offset:9216\n"
    "v_smfmac_f32_16x16x64_bf16 v[20:23], v[32:35], v[200:207], v1\n"
    "ds_read_b128 v[32:35], v0 offset:10240\n"
    "s_waitcnt lgkmcnt(9)\n"
    "s_waitcnt lgkmcnt(6)\n"
    "v_smfmac_f32_16x16x64_bf16 v[20:23], v[36:39], v[40:47], v1\n"
    "ds_read_b128 v[40:43], v15 offset:0\n"
    "v_smfmac_f32_16x16x64_bf16 v[16:19], v[36:39], v[136:143], v1\n"
    "ds_read_b128 v[44:47], v15 offset:1024\n"
    "v_smfmac_f32_16x16x64_bf16 v[16:19], v[56:59], v[80:87], v1\n"
    "ds_read_b128 v[36:39], v0 offset:11264\n"
    "v_smfmac_f32_16x16x64_bf16 v[20:23], v[56:59], v[208:215], v1\n"
    "ds_read_b128 v[56:59], v0 offset:12288\n"
    "s_waitcnt lgkmcnt(9)\n"
    "s_waitcnt lgkmcnt(6)\n"
    "v_smfmac_f32_16x16x64_bf16 v[20:23], v[60:63], v[48:55], v1\n"
    "ds_read_b128 v[48:51], v15 offset:16384\n"
    "v_smfmac_f32_16x16x64_bf16 v[16:19], v[60:63], v[144:151], v1\n"
    "ds_read_b128 v[52:55], v15 offset:17408\n"
    "v_smfmac_f32_16x16x64_bf16 v[16:19], v[24:27], v[96:103], v1\n"
    "ds_read_b128 v[60:63], v0 offset:13312\n"
    "v_smfmac_f32_16x16x64_bf16 v[20:23], v[24:27], v[224:231], v1\n"
    "ds_read_b128 v[24:27], v0 offset:14336\n"
    "s_waitcnt lgkmcnt(9)\n"
    "s_waitcnt lgkmcnt(6)\n"
    "v_smfmac_f32_16x16x64_bf16 v[20:23], v[28:31], v[40:47], v1\n"
    "ds_read_b128 v[40:43], v15 offset:32768\n"
    "v_smfmac_f32_16x16x64_bf16 v[16:19], v[28:31], v[160:167], v1\n"
    "ds_read_b128 v[44:47], v15 offset:33792\n"
    "v_smfmac_f32_16x16x64_bf16 v[16:19], v[32:35], v[104:111], v1\n"
    "ds_read_b128 v[28:31], v0 offset:15360\n"
    "v_smfmac_f32_16x16x64_bf16 v[20:23], v[32:35], v[232:239], v1\n"
    "s_waitcnt lgkmcnt(8)\n"
    "s_waitcnt lgkmcnt(5)\n"
    "v_smfmac_f32_16x16x64_bf16 v[20:23], v[36:39], v[48:55], v1\n"
    "ds_read_b128 v[48:51], v15 offset:49152\n"
    "v_smfmac_f32_16x16x64_bf16 v[16:19], v[36:39], v[168:175], v1\n"
    "ds_read_b128 v[52:55], v15 offset:50176\n"
    "v_smfmac_f32_16x16x64_bf16 v[16:19], v[56:59], v[112:119], v1\n"
    "v_smfmac_f32_16x16x64_bf16 v[20:23], v[56:59], v[240:247], v1\n"
    "s_waitcnt lgkmcnt(6)\n"
    "s_waitcnt lgkmcnt(3)\n"
    "v_smfmac_f32_16x16x64_bf16 v[20:23], v[60:63], v[40:47], v1\n"
    "v_smfmac_f32_16x16x64_bf16 v[16:19], v[60:63], v[176:183], v1\n"
    "v_smfmac_f32_16x16x64_bf16 v[16:19], v[24:27], v[120:127], v1\n"
    "v_smfmac_f32_16x16x64_bf16 v[20:23], v[24:27], v[248:255], v1\n"
    "s_waitcnt lgkmcnt(2)\n"
    "s_waitcnt lgkmcnt(0)\n"
    "v_smfmac_f32_16x16x64_bf16 v[20:23], v[28:31], v[48:55], v1\n"
    "v_smfmac_f32_16x16x64_bf16 v[16:19], v[28:31], v[184:191], v1\n"
    "s_branch gemvdone_%=\n"
    "mretry3_%=:\n"
    "s_nop 7\n"
    "s_sleep 1\n"
    "s_add_u32 s94, s94, 1\n"
    "s_cmp_lt_u32 s94, 400000\n"
    "s_cbranch_scc1 mpoll3_%=\n"
    "s_branch fail_%=\n"
    "var4_%=:\n"
    "ds_read_b128 v[40:43], v15 offset:0\n"
    "ds_read_b128 v[44:47], v15 offset:1024\n"
    "ds_read_b128 v[48:51], v2 offset:0\n"
    "ds_read_b128 v[52:55], v2 offset:1024\n"
    "s_cbranch_scc1 go4_%=\n"
    "s_nop 0\n"
    "mwait4_%=:\n"
    "s_memrealtime s[96:97]\n"
    "s_waitcnt lgkmcnt(0)\n"
    "s_sub_u32 s96, s92, s96\n"
    "s_sub_u32 s96, s96, 1\n"
    "s_cmp_lt_u32 s96, 1000\n"
    "s_cbranch_scc1 mwait4_%=\n"
    "mpoll4_%=:\n"
    "buffer_load_dwordx4 v[56:59], v9, s[64:67], s91 offen sc1\n"
    "buffer_load_dwordx4 v[60:63], v9, s[64:67], s91 offen offset:1024 sc1\n"
    "s_waitcnt vmcnt(1)\n"
    "ds_write_b128 v3, v[56:59]\n"
    "v_max3_u32 v24, v56, v57, v58\n"
    "s_waitcnt vmcnt(0)\n"
    "ds_write_b128 v3, v[60:63] offset:1024\n"
    "v_max3_u32 v24, v24, v59, v60\n"
    "v_max3_u32 v24, v24, v61, v62\n"
    "v_max_u32 v24, v24, v63\n"
    "v_cmp_eq_u32 vcc, -1, v24\n"
    "s_cbranch_vccnz mretry4_%=\n"
    "go4_%=:\n"
    "s_waitcnt lgkmcnt(0)\n"
    "s_barrier\n"
    "ds_read_b128 v[24:27], v0 offset:0\n"
    "ds_read_b128 v[28:31], v0 offset:1024\n"
    "ds_read_b128 v[32:35], v0 offset:2048\n"
    "ds_read_b128 v[36:39], v0 offset:3072\n"
    "v_smfmac_f32_16x16x64_bf16 v[16:19], v[56:59], v[96:103], v1\n"
    "v_smfmac_f32_16x16x64_bf16 v[20:23], v[56:59], v[224:231], v1\n"
    "ds_read_b128 v[56:59], v0 offset:4096\n"
    "v_smfmac_f32_16x16x64_bf16 v[20:23], v[60:63], v[40:47], v1\n"
    "ds_read_b128 v[40:43], v2 offset:16384\n"
    "v_smfmac_f32_16x16x64_bf16 v[16:19], v[60:63], v[160:167], v1\n"
    "ds_read_b128 v[44:47], v2 offset:17408\n"
    "s_waitcnt lgkmcnt(6)\n"
    "v_smfmac_f32_16x16x64_bf16 v[16:19], v[24:27], v[64:71], v1\n"
    "ds_read_b128 v[60:63], v0 offset:5120\n"
    "v_smfmac_f32_16x16x64_bf16 v[20:23], v[24:27], v[192:199], v1\n"
    "ds_read_b128 v[24:27], v0 offset:6144\n"
    "s_waitcnt lgkmcnt(7)\n"
    "v_smfmac_f32_16x16x64_bf16 v[20:23], v[28:31], v[48:55], v1\n"
    "ds_read_b128 v[48:51], v2 offset:32768\n"
    "v_smfmac_f32_16x16x64_bf16 v[16:19], v[28:31], v[128:135], v1\n"
    "ds_read_b128 v[52:55], v2 offset:33792\n"
    "s_waitcnt lgkmcnt(8)\n"
    "v_smfmac_f32_16x16x64_bf16 v[16:19], v[32:35], v[72:79], v1\n"
    "ds_read_b128 v[28:31], v0 offset:7168\n"
    "v_smfmac_f32_16x16x64_bf16 v[20:23], v[32:35], v[200:207], v1\n"
    "ds_read_b128 v[32:35], v0 offset:10240\n"
    "s_waitcnt lgkmcnt(9)\n"
    "s_waitcnt lgkmcnt(6)\n"
    "v_smfmac_f32_16x16x64_bf16 v[20:23], v[36:39], v[40:47], v1\n"
    "ds_read_b128 v[40:43], v2 offset:49152\n"
    "v_smfmac_f32_16x16x64_bf16 v[16:19], v[36:39], v[136:143], v1\n"
    "ds_read_b128 v[44:47], v2 offset:50176\n"
    "v_smfmac_f32_16x16x64_bf16 v[16:19], v[56:59], v[80:87], v1\n"
    "ds_read_b128 v[36:39], v0 offset:11264\n"
    "v_smfmac_f32_16x16x64_bf16 v[20:23], v[56:59], v[208:215], v1\n"
    "ds_read_b128 v[56:59], v0 offset:12288\n"
    "s_waitcnt lgkmcnt(9)\n"
    "s_waitcnt lgkmcnt(6)\n"
    "v_smfmac_f32_16x16x64_bf16 v[20:23], v[60:63], v[48:55], v1\n"
    "ds_read_b128 v[48:51], v15 offset:16384\n"
    "v_smfmac_f32_16x16x64_bf16 v[16:19], v[60:63], v[144:151], v1\n"
    "ds_read_b128 v[52:55], v15 offset:17408\n"
    "v_smfmac_f32_16x16x64_bf16 v[16:19], v[24:27], v[88:95], v1\n"
    "ds_read_b128 v[60:63], v0 offset:13312\n"
    "v_smfmac_f32_16x16x64_bf16 v[20:23], v[24:27], v[216:223], v1\n"
    "ds_read_b128 v[24:27], v0 offset:14336\n"
    "s_waitcnt lgkmcnt(9)\n"
    "s_waitcnt lgkmcnt(6)\n"
    "v_smfmac_f32_16x16x64_bf16 v[20:23], v[28:31], v[40:47], v1\n"
    "ds_read_b128 v[40:43], v15 offset:32768\n"
    "v_smfmac_f32_16x16x64_bf16 v[16:19], v[28:31], v[152:159], v1\n"
    "ds_read_b128 v[44:47], v15 offset:33792\n"
    "v_smfmac_f32_16x16x64_bf16 v[16:19], v[32:35], v[104:111], v1\n"
    "ds_read_b128 v[28:31], v0 offset:15360\n"
    "v_smfmac_f32_16x16x64_bf16 v[20:23], v[32:35], v[232:239], v1\n"
    "s_waitcnt lgkmcnt(8)\n"
    "s_waitcnt lgkmcnt(5)\n"
    "v_smfmac_f32_16x16x64_bf16 v[20:23], v[36:39], v[48:55], v1\n"
    "ds_read_b128 v[48:51], v15 offset:49152\n"
    "v_smfmac_f32_16x16x64_bf16 v[16:19], v[36:39], v[168:175], v1\n"
    "ds_read_b128 v[52:55], v15 offset:50176\n"
    "v_smfmac_f32_16x16x64_bf16 v[16:19], v[56:59], v[112:119], v1\n"
    "v_smfmac_f32_16x16x64_bf16 v[20:23], v[56:59], v[240:247], v1\n"
    "s_waitcnt lgkmcnt(6)\n"
    "s_waitcnt lgkmcnt(3)\n"
    "v_smfmac_f32_16x16x64_bf16 v[20:23], v[60:63], v[40:47], v1\n"
    "v_smfmac_f32_16x16x64_bf16 v[16:19], v[60:63], v[176:183], v1\n"
    "v_smfmac_f32_16x16x64_bf16 v[16:19], v[24:27], v[120:127], v1\n"
    "v_smfmac_f32_16x16x64_bf16 v[20:23], v[24:27], v[248:255], v1\n"
    "s_waitcnt lgkmcnt(2)\n"
    "s_waitcnt lgkmcnt(0)\n"
    "v_smfmac_f32_16x16x64_bf16 v[20:23], v[28:31], v[48:55], v1\n"
    "v_smfmac_f32_16x16x64_bf16 v[16:19], v[28:31], v[184:191], v1\n"
    "s_branch gemvdone_%=\n"
    "mretry4_%=:\n"
    "s_nop 7\n"
    "s_sleep 1\n"
    "s_add_u32 s94, s94, 1\n"
    "s_cmp_lt_u32 s94, 400000\n"
    "s_cbranch_scc1 mpoll4_%=\n"
    "s_branch fail_%=\n"
    "var5_%=:\n"
    "ds_read_b128 v[40:43], v15 offset:16384\n"
    "ds_read_b128 v[44:47], v15 offset:17408\n"
    "ds_read_b128 v[48:51], v2 offset:0\n"
    "ds_read_b128 v[52:55], v2 offset:1024\n"
    "s_cbranch_scc1 go5_%=\n"
    "s_nop 0\n"
    "mwait5_%=:\n"
    "s_memrealtime s[96:97]\n"
    "s_waitcnt lgkmcnt(0)\n"
    "s_sub_u32 s96, s92, s96\n"
    "s_sub_u32 s96, s96, 1\n"
    "s_cmp_lt_u32 s96, 1000\n"
    "s_cbranch_scc1 mwait5_%=\n"
    "mpoll5_%=:\n"
    "buffer_load_dwordx4 v[56:59], v9, s[64:67], s91 offen sc1\n"
    "buffer_load_dwordx4 v[60:63], v9, s[64:67], s91 offen offset:1024 sc1\n"
    "s_waitcnt vmcnt(1)\n"
    "ds_write_b128 v3, v[56:59]\n"
    "v_max3_u32 v24, v56, v57, v58\n"
    "s_waitcnt vmcnt(0)\n"
    "ds_write_b128 v3, v[60:63] offset:1024\n"
    "v_max3_u32 v24, v24, v59, v60\n"
    "v_max3_u32 v24, v24, v61, v62\n"
    "v_max_u32 v24, v24, v63\n"
    "v_cmp_eq_u32 vcc, -1, v24\n"
    "s_cbranch_vccnz mretry5_%=\n"
    "go5_%=:\n"
    "s_waitcnt lgkmcnt(0)\n"
    "s_barrier\n"
    "ds_read_b128 v[24:27], v0 offset:0\n"
    "ds_read_b128 v[28:31], v0 offset:1024\n"
    "ds_read_b128 v[32:35], v0 offset:2048\n"
    "ds_read_b128 v[36:39], v0 offset:3072\n"
    "v_smfmac_f32_16x16x64_bf16 v[16:19], v[56:59], v[104:111], v1\n"
    "v_smfmac_f32_16x16x64_bf16 v[20:23], v[56:59], v[232:239], v1\n"
    "ds_read_b128 v[56:59], v0 offset:4096\n"
    "v_smfmac_f32_16x16x64_bf16 v[20:23], v[60:63], v[40:47], v1\n"
    "ds_read_b128 v[40:43], v2 offset:16384\n"
    "v_smfmac_f32_16x16x64_bf16 v[16:19], v[60:63], v[168:175], v1\n"
    "ds_read_b128 v[44:47], v2 offset:17408\n"
    "s_waitcnt lgkmcnt(6)\n"
    "v_smfmac_f32_16x16x64_bf16 v[16:19], v[24:27], v[64:71], v1\n"
    "ds_read_b128 v[60:63], v0 offset:5120\n"
    "v_smfmac_f32_16x16x64_bf16 v[20:23], v[24:27], v[192:199], v1\n"
    "ds_read_b128 v[24:27], v0 offset:6144\n"
    "s_waitcnt lgkmcnt(7)\n"
    "v_smfmac_f32_16x16x64_bf16 v[20:23], v[28:31], v[48:55], v1\n"
    "ds_read_b128 v[48:51], v2 offset:32768\n"
    "v_smfmac_f32_16x16x64_bf16 v[16:19], v[28:31], v[128:135], v1\n"
    "ds_read_b128 v[52:55], v2 offset:33792\n"
    "s_waitcnt lgkmcnt(8)\n"
    "v_smfmac_f32_16x16x64_bf16 v[16:19], v[32:35], v[72:79], v1\n"
    "ds_read_b128 v[28:31], v0 offset:7168\n"
    "v_smfmac_f32_16x16x64_bf16 v[20:23], v[32:35], v[200:207], v1\n"
    "ds_read_b128 v[32:35], v0 offset:8192\n"
    "s_waitcnt lgkmcnt(9)\n"
    "s_waitcnt lgkmcnt(6)\n"
    "v_smfmac_f32_16x16x64_bf16 v[20:23], v[36:39], v[40:47], v1\n"
    "ds_read_b128 v[40:43], v2 offset:49152\n"
    "v_smfmac_f32_16x16x64_bf16 v[16:19], v[36:39], v[136:143], v1\n"
    "ds_read_b128 v[44:47], v2 offset:50176\n"
    "v_smfmac_f32_16x16x64_bf16 v[16:19], v[56:59], v[80:87], v1\n"
    "ds_read_b128 v[36:39], v0 offset:9216\n"
    "v_smfmac_f32_16x16x64_bf16 v[20:23], v[56:59], v[208:215], v1\n"
    "ds_read_b128 v[56:59], v0 offset:12288\n"
    "s_waitcnt lgkmcnt(9)\n"
    "s_waitcnt lgkmcnt(6)\n"
    "v_smfmac_f32_16x16x64_bf16 v[20:23], v[60:63], v[48:55], v1\n"
    "ds_read_b128 v[48:51], v15 offset:0\n"
    "v_smfmac_f32_16x16x64_bf16 v[16:19], v[60:63], v[144:151], v1\n"
    "ds_read_b128 v[52:55], v15 offset:1024\n"
    "v_smfmac_f32_16x16x64_bf16 v[16:19], v[24:27], v[88:95], v1\n"
    "ds_read_b128 v[60:63], v0 offset:13312\n"
    "v_smfmac_f32_16x16x64_bf16 v[20:23], v[24:27], v[216:223], v1\n"
    "ds_read_b128 v[24:27], v0 offset:14336\n"
    "s_waitcnt lgkmcnt(9)\n"
    "s_waitcnt lgkmcnt(6)\n"
    "v_smfmac_f32_16x16x64_bf16 v[20:23], v[28:31], v[40:47], v1\n"
    "ds_read_b128 v[40:43], v15 offset:32768\n"
    "v_smfmac_f32_16x16x64_bf16 v[16:19], v[28:31], v[152:159], v1\n"
    "ds_read_b128 v[44:47], v15 offset:33792\n"
    "v_smfmac_f32_16x16x64_bf16 v[16:19], v[32:35], v[96:103], v1\n"
    "ds_read_b128 v[28:31], v0 offset:15360\n"
    "v_smfmac_f32_16x16x64_bf16 v[20:23], v[32:35], v[224:231], v1\n"
    "s_waitcnt lgkmcnt(8)\n"
    "s_waitcnt lgkmcnt(5)\n"
    "v_smfmac_f32_16x16x64_bf16 v[20:23], v[36:39], v[48:55], v1\n"
    "ds_read_b128 v[48:51], v15 offset:49152\n"
    "v_smfmac_f32_16x16x64_bf16 v[16:19], v[36:39], v[160:167], v1\n"
    "ds_read_b128 v[52:55], v15 offset:50176\n"
    "v_smfmac_f32_16x16x64_bf16 v[16:19], v[56:59], v[112:119], v1\n"
    "v_smfmac_f32_16x16x64_bf16 v[20:23], v[56:59], v[240:247], v1\n"
    "s_waitcnt lgkmcnt(6)\n"
    "s_waitcnt lgkmcnt(3)\n"
    "v_smfmac_f32_16x16x64_bf16 v[20:23], v[60:63], v[40:47], v1\n"
    "v_smfmac_f32_16x16x64_bf16 v[16:19], v[60:63], v[176:183], v1\n"
    "v_smfmac_f32_16x16x64_bf16 v[16:19], v[24:27], v[120:127], v1\n"
    "v_smfmac_f32_16x16x64_bf16 v[20:23], v[24:27], v[248:255], v1\n"
    "s_waitcnt lgkmcnt(2)\n"
    "s_waitcnt lgkmcnt(0)\n"
    "v_smfmac_f32_16x16x64_bf16 v[20:23], v[28:31], v[48:55], v1\n"
    "v_smfmac_f32_16x16x64_bf16 v[16:19], v[28:31], v[184:191], v1\n"
    "s_branch gemvdone_%=\n"
    "mretry5_%=:\n"
    "s_nop 7\n"
    "s_sleep 1\n"
    "s_add_u32 s94, s94, 1\n"
    "s_cmp_lt_u32 s94, 400000\n"
    "s_cbranch_scc1 mpoll5_%=\n"
    "s_branch fail_%=\n"
    "var6_%=:\n"
    "ds_read_b128 v[40:43], v15 offset:32768\n"
    "ds_read_b128 v[44:47], v15 offset:33792\n"
    "ds_read_b128 v[48:51], v2 offset:0\n"
    "ds_read_b128 v[52:55], v2 offset:1024\n"
    "s_cbranch_scc1 go6_%=\n"
    "s_nop 0\n"
    "mwait6_%=:\n"
    "s_memrealtime s[96:97]\n"
    "s_waitcnt lgkmcnt(0)\n"
    "s_sub_u32 s96, s92, s96\n"
    "s_sub_u32 s96, s96, 1\n"
    "s_cmp_lt_u32 s96, 1000\n"
    "s_cbranch_scc1 mwait6_%=\n"
    "mpoll6_%=:\n"
    "buffer_load_dwordx4 v[56:59], v9, s[64:67], s91 offen sc1\n"
    "buffer_load_dwordx4 v[60:63], v9, s[64:67], s91 offen offset:1024 sc1\n"
    "s_waitcnt vmcnt(1)\n"
    "ds_write_b128 v3, v[56:59]\n"
    "v_max3_u32 v24, v56, v57, v58\n"
    "s_waitcnt vmcnt(0)\n"
    "ds_write_b128 v3, v[60:63] offset:1024\n"
    "v_max3_u32 v24, v24, v59, v60\n"
    "v_max3_u32 v24, v24, v61, v62\n"
    "v_max_u32 v24, v24, v63\n"
    "v_cmp_eq_u32 vcc, -1, v24\n"
    "s_cbranch_vccnz mretry6_%=\n"
    "go6_%=:\n"
    "s_waitcnt lgkmcnt(0)\n"
    "s_barrier\n"
    "ds_read_b128 v[24:27], v0 offset:0\n"
    "ds_read_b128 v[28:31], v0 offset:1024\n"
    "ds_read_b128 v[32:35], v0 offset:2048\n"
    "ds_read_b128 v[36:39], v0 offset:3072\n"
    "v_smfmac_f32_16x16x64_bf16 v[16:19], v[56:59], v[112:119], v1\n"
    "v_smfmac_f32_16x16x64_bf16 v[20:23], v[56:59], v[240:247], v1\n"
    "ds_read_b128 v[56:59], v0 offset:4096\n"
    "v_smfmac_f32_16x16x64_bf16 v[20:23], v[60:63], v[40:47], v1\n"
    "ds_read_b128 v[40:43], v2 offset:16384\n"
    "v_smfmac_f32_16x16x64_bf16 v[16:19], v[60:63], v[176:183], v1\n"
    "ds_read_b128 v[44:47], v2 offset:17408\n"
    "s_waitcnt lgkmcnt(6)\n"
    "v_smfmac_f32_16x16x64_bf16 v[16:19], v[24:27], v[64:71], v1\n"
    "ds_read_b128 v[60:63], v0 offset:5120\n"
    "v_smfmac_f32_16x16x64_bf16 v[20:23], v[24:27], v[192:199], v1\n"
    "ds_read_b128 v[24:27], v0 offset:6144\n"
    "s_waitcnt lgkmcnt(7)\n"
    "v_smfmac_f32_16x16x64_bf16 v[20:23], v[28:31], v[48:55], v1\n"
    "ds_read_b128 v[48:51], v2 offset:32768\n"
    "v_smfmac_f32_16x16x64_bf16 v[16:19], v[28:31], v[128:135], v1\n"
    "ds_read_b128 v[52:55], v2 offset:33792\n"
    "s_waitcnt lgkmcnt(8)\n"
    "v_smfmac_f32_16x16x64_bf16 v[16:19], v[32:35], v[72:79], v1\n"
    "ds_read_b128 v[28:31], v0 offset:7168\n"
    "v_smfmac_f32_16x16x64_bf16 v[20:23], v[32:35], v[200:207], v1\n"
    "ds_read_b128 v[32:35], v0 offset:8192\n"
    "s_waitcnt lgkmcnt(9)\n"
    "s_waitcnt lgkmcnt(6)\n"
    "v_smfmac_f32_16x16x64_bf16 v[20:23], v[36:39], v[40:47], v1\n"
    "ds_read_b128 v[40:43], v2 offset:49152\n"
    "v_smfmac_f32_16x16x64_bf16 v[16:19], v[36:39], v[136:143], v1\n"
    "ds_read_b128 v[44:47], v2 offset:50176\n"
    "v_smfmac_f32_16x16x64_bf16 v[16:19], v[56:59], v[80:87], v1\n"
    "ds_read_b128 v[36:39], v0 offset:9216\n"
    "v_smfmac_f32_16x16x64_bf16 v[20:23], v[56:59], v[208:215], v1\n"
    "ds_read_b128 v[56:59], v0 offset:10240\n"
    "s_waitcnt lgkmcnt(9)\n"
    "s_waitcnt lgkmcnt(6)\n"
    "v_smfmac_f32_16x16x64_bf16 v[20:23], v[60:63], v[48:55], v1\n"
    "ds_read_b128 v[48:51], v15 offset:0\n"
    "v_smfmac_f32_16x16x64_bf16 v[16:19], v[60:63], v[144:151], v1\n"
    "ds_read_b128 v[52:55], v15 offset:1024\n"
    "v_smfmac_f32_16x16x64_bf16 v[16:19], v[24:27], v[88:95], v1\n"
    "ds_read_b128 v[60:63], v0 offset:11264\n"
    "v_smfmac_f32_16x16x64_bf16 v[20:23], v[24:27], v[216:223], v1\n"
    "ds_read_b128 v[24:27], v0 offset:14336\n"
    "s_waitcnt lgkmcnt(9)\n"
    "s_waitcnt lgkmcnt(6)\n"
    "v_smfmac_f32_16x16x64_bf16 v[20:23], v[28:31], v[40:47], v1\n"
    "ds_read_b128 v[40:43], v15 offset:16384\n"
    "v_smfmac_f32_16x16x64_bf16 v[16:19], v[28:31], v[152:159], v1\n"
    "ds_read_b128 v[44:47], v15 offset:17408\n"
    "v_smfmac_f32_16x16x64_bf16 v[16:19], v[32:35], v[96:103], v1\n"
    "ds_read_b128 v[28:31], v0 offset:15360\n"
    "v_smfmac_f32_16x16x64_bf16 v[20:23], v[32:35], v[224:231], v1\n"
    "s_waitcnt lgkmcnt(8)\n"
    "s_waitcnt lgkmcnt(5)\n"
    "v_smfmac_f32_16x16x64_bf16 v[20:23], v[36:39], v[48:55], v1\n"
    "ds_read_b128 v[48:51], v15 offset:49152\n"
    "v_smfmac_f32_16x16x64_bf16 v[16:19], v[36:39], v[160:167], v1\n"
    "ds_read_b128 v[52:55], v15 offset:50176\n"
    "v_smfmac_f32_16x16x64_bf16 v[16:19], v[56:59], v[104:111], v1\n"
    "v_smfmac_f32_16x16x64_bf16 v[20:23], v[56:59], v[232:239], v1\n"
    "s_waitcnt lgkmcnt(6)\n"
    "s_waitcnt lgkmcnt(3)\n"
    "v_smfmac_f32_16x16x64_bf16 v[20:23], v[60:63], v[40:47], v1\n"
    "v_smfmac_f32_16x16x64_bf16 v[16:19], v[60:63], v[168:175], v1\n"
    "v_smfmac_f32_16x16x64_bf16 v[16:19], v[24:27], v[120:127], v1\n"
    "v_smfmac_f32_16x16x64_bf16 v[20:23], v[24:27], v[248:255], v1\n"
    "s_waitcnt lgkmcnt(2)\n"
    "s_waitcnt lgkmcnt(0)\n"
    "v_smfmac_f32_16x16x64_bf16 v[20:23], v[28:31], v[48:55], v1\n"
    "v_smfmac_f32_16x16x64_bf16 v[16:19], v[28:31], v[184:191], v1\n"
    "s_branch gemvdone_%=\n"
    "mretry6_%=:\n"
    "s_nop 7\n"
    "s_sleep 1\n"
    "s_add_u32 s94, s94, 1\n"
    "s_cmp_lt_u32 s94, 400000\n"
    "s_cbranch_scc1 mpoll6_%=\n"
    "s_branch fail_%=\n"
    "var7_%=:\n"
    "ds_read_b128 v[40:43], v15 offset:49152\n"
    "ds_read_b128 v[44:47], v15 offset:50176\n"
    "ds_read_b128 v[48:51], v2 offset:0\n"
    "ds_read_b128 v[52:55], v2 offset:1024\n"
    "s_cbranch_scc1 go7_%=\n"
    "s_nop 0\n"
    "mwait7_%=:\n"
    "s_memrealtime s[96:97]\n"
    "s_waitcnt lgkmcnt(0)\n"
    "s_sub_u32 s96, s92, s96\n"
    "s_sub_u32 s96, s96, 1\n"
    "s_cmp_lt_u32 s96, 1000\n"
    "s_cbranch_scc1 mwait7_%=\n"
    "mpoll7_%=:\n"
    "buffer_load_dwordx4 v[56:59], v9, s[64:67], s91 offen sc1\n"
    "buffer_load_dwordx4 v[60:63], v9, s[64:67], s91 offen offset:1024 sc1\n"
    "buffer_load_dwordx2 v[26:27], off, s[64:67], s90 sc1\n"
    "v_mov_b32 v25, 29312\n"
    "s_waitcnt vmcnt(2)\n"
    "ds_write_b128 v3, v[56:59]\n"
    "v_max3_u32 v24, v56, v57, v58\n"
    "s_waitcnt vmcnt(1)\n"
    "ds_write_b128 v3, v[60:63] offset:1024\n"
    "v_max3_u32 v24, v24, v59, v60\n"
    "v_max3_u32 v24, v24, v61, v62\n"
    "v_max_u32 v24, v24, v63\n"
    "s_waitcnt vmcnt(0)\n"
    "ds_write_b64 v25, v[26:27]\n"
    "v_max3_u32 v24, v24, v26, v27\n"
    "v_cmp_eq_u32 vcc, -1, v24\n"
    "s_cbranch_vccnz mretry7_%=\n"
    "go7_%=:\n"
    "s_waitcnt lgkmcnt(0)\n"
    "s_barrier\n"
    "ds_read_b128 v[24:27], v0 offset:0\n"
    "ds_read_b128 v[28:31], v0 offset:1024\n"
    "ds_read_b128 v[32:35], v0 offset:2048\n"
    "ds_read_b128 v[36:39], v0 offset:3072\n"
    "s_memrealtime s[58:59]\n"
    "buffer_store_dword v8, off, s[64:67], s85 sc1\n"
    "buffer_store_dword v8, off, s[64:67], s99 sc1\n"
    "v_smfmac_f32_16x16x64_bf16 v[16:19], v[56:59], v[120:127], v1\n"
    "v_smfmac_f32_16x16x64_bf16 v[20:23], v[56:59], v[248:255], v1\n"
    "ds_read_b128 v[56:59], v0 offset:4096\n"
    "v_smfmac_f32_16x16x64_bf16 v[20:23], v[60:63], v[40:47], v1\n"
    "ds_read_b128 v[40:43], v2 offset:16384\n"
    "v_smfmac_f32_16x16x64_bf16 v[16:19], v[60:63], v[184:191], v1\n"
    "ds_read_b128 v[44:47], v2 offset:17408\n"
    "s_waitcnt lgkmcnt(6)\n"
    "v_smfmac_f32_16x16x64_bf16 v[16:19], v[24:27], v[64:71], v1\n"
    "ds_read_b128 v[60:63], v0 offset:5120\n"
    "v_smfmac_f32_16x16x64_bf16 v[20:23], v[24:27], v[192:199], v1\n"
    "ds_read_b128 v[24:27], v0 offset:6144\n"
    "s_waitcnt lgkmcnt(7)\n"
    "v_smfmac_f32_16x16x64_bf16 v[20:23], v[28:31], v[48:55], v1\n"
    "ds_read_b128 v[48:51], v2 offset:32768\n"
    "v_smfmac_f32_16x16x64_bf16 v[16:19], v[28:31], v[128:135], v1\n"
    "ds_read_b128 v[52:55], v2 offset:33792\n"
    "s_waitcnt lgkmcnt(8)\n"
    "v_smfmac_f32_16x16x64_bf16 v[16:19], v[32:35], v[72:79], v1\n"
    "ds_read_b128 v[28:31], v0 offset:7168\n"
    "v_smfmac_f32_16x16x64_bf16 v[20:23], v[32:35], v[200:207], v1\n"
    "ds_read_b128 v[32:35], v0 offset:8192\n"
    "s_waitcnt lgkmcnt(9)\n"
    "s_waitcnt lgkmcnt(6)\n"
    "v_smfmac_f32_16x16x64_bf16 v[20:23], v[36:39], v[40:47], v1\n"
    "ds_read_b128 v[40:43], v2 offset:49152\n"
    "v_smfmac_f32_16x16x64_bf16 v[16:19], v[36:39], v[136:143], v1\n"
    "ds_read_b128 v[44:47], v2 offset:50176\n"
    "v_smfmac_f32_16x16x64_bf16 v[16:19], v[56:59], v[80:87], v1\n"
    "ds_read_b128 v[36:39], v0 offset:9216\n"
    "v_smfmac_f32_16x16x64_bf16 v[20:23], v[56:59], v[208:215], v1\n"
    "ds_read_b128 v[56:59], v0 offset:10240\n"
    "s_waitcnt lgkmcnt(9)\n"
    "s_waitcnt lgkmcnt(6)\n"
    "v_smfmac_f32_16x16x64_bf16 v[20:23], v[60:63], v[48:55], v1\n"
    "ds_read_b128 v[48:51], v15 offset:0\n"
    "v_smfmac_f32_16x16x64_bf16 v[16:19], v[60:63], v[144:151], v1\n"
    "ds_read_b128 v[52:55], v15 offset:1024\n"
    "v_smfmac_f32_16x16x64_bf16 v[16:19], v[24:27], v[88:95], v1\n"
    "ds_read_b128 v[60:63], v0 offset:11264\n"
    "v_smfmac_f32_16x16x64_bf16 v[20:23], v[24:27], v[216:223], v1\n"
    "ds_read_b128 v[24:27], v0 offset:12288\n"
    "s_waitcnt lgkmcnt(9)\n"
    "s_waitcnt lgkmcnt(6)\n"
    "v_smfmac_f32_16x16x64_bf16 v[20:23], v[28:31], v[40:47], v1\n"
    "ds_read_b128 v[40:43], v15 offset:16384\n"
    "v_smfmac_f32_16x16x64_bf16 v[16:19], v[28:31], v[152:159], v1\n"
    "ds_read_b128 v[44:47], v15 offset:17408\n"
    "v_smfmac_f32_16x16x64_bf16 v[16:19], v[32:35], v[96:103], v1\n"
    "ds_read_b128 v[28:31], v0 offset:13312\n"
    "v_smfmac_f32_16x16x64_bf16 v[20:23], v[32:35], v[224:231], v1\n"
    "s_waitcnt lgkmcnt(8)\n"
    "s_waitcnt lgkmcnt(5)\n"
    "v_smfmac_f32_16x16x64_bf16 v[20:23], v[36:39], v[48:55], v1\n"
    "ds_read_b128 v[48:51], v15 offset:32768\n"
    "v_smfmac_f32_16x16x64_bf16 v[16:19], v[36:39], v[160:167], v1\n"
    "ds_read_b128 v[52:55], v15 offset:33792\n"
    "v_smfmac_f32_16x16x64_bf16 v[16:19], v[56:59], v[104:111], v1\n"
    "v_smfmac_f32_16x16x64_bf16 v[20:23], v[56:59], v[232:239], v1\n"
    "s_waitcnt lgkmcnt(6)\n"
    "s_waitcnt lgkmcnt(3)\n"
    "v_smfmac_f32_16x16x64_bf16 v[20:23], v[60:63], v[40:47], v1\n"
    "v_smfmac_f32_16x16x64_bf16 v[16:19], v[60:63], v[168:175], v1\n"
    "v_smfmac_f32_16x16x64_bf16 v[16:19], v[24:27], v[112:119], v1\n"
    "v_smfmac_f32_16x16x64_bf16 v[20:23], v[24:27], v[240:247], v1\n"
    "s_waitcnt lgkmcnt(2)\n"
    "s_waitcnt lgkmcnt(0)\n"
    "v_smfmac_f32_16x16x64_bf16 v[20:23], v[28:31], v[48:55], v1\n"
    "v_smfmac_f32_16x16x64_bf16 v[16:19], v[28:31], v[176:183], v1\n"
    "s_branch gemvdone_%=\n"
    "mretry7_%=:\n"
    "s_nop 7\n"
    "s_sleep 1\n"
    "s_add_u32 s94, s94, 1\n"
    "s_cmp_lt_u32 s94, 400000\n"
    "s_cbranch_scc1 mpoll7_%=\n"
    "s_branch fail_%=\n"
    "gemvdone_%=:\n"
    "s_nop 6\n"
    "v_permlane32_swap_b32 v16, v20\n"
    "v_permlane32_swap_b32 v17, v21\n"
    "v_permlane32_swap_b32 v18, v22\n"
    "v_permlane32_swap_b32 v19, v23\n"
    "v_pk_add_f32 v[16:17], v[16:17], v[20:21]\n"
    "v_pk_add_f32 v[18:19], v[18:19], v[22:23]\n"
    "v_cvt_pk_bf16_f32 v16, v16, v17\n"
    "v_cvt_pk_bf16_f32 v17, v18, v19\n"
    "ds_write_b64 v10, v[16:17]\n"
    "s_cmp_lg_u32 s68, 0\n"
    "s_cbranch_scc1 notw0_%=\n"
    "ds_read_b128 v[44:47], v0 offset:16384\n"
    "ds_read_b128 v[48:51], v0 offset:20480\n"
    "ds_read_b128 v[52:55], v0 offset:17408\n"
    "ds_read_b128 v[56:59], v0 offset:21504\n"
    "ds_read_b128 v[60:63], v0 offset:18432\n"
    "v_lshrrev_b32 v15, 1, v0\n"
    "s_waitcnt lgkmcnt(0)\n"
    "s_barrier\n"
    "ds_read_b64_tr_b16 v[28:29], v15 offset:24576\n"
    "ds_read_b64_tr_b16 v[30:31], v15 offset:25088\n"
    "ds_read_b64_tr_b16 v[32:33], v15 offset:25600\n"
    "ds_read_b64_tr_b16 v[34:35], v15 offset:26112\n"
    "ds_read_b64_tr_b16 v[36:37], v15 offset:26624\n"
    "ds_read_b64_tr_b16 v[38:39], v15 offset:27136\n"
    "ds_read_b64_tr_b16 v[40:41], v15 offset:27648\n"
    "ds_read_b64_tr_b16 v[42:43], v15 offset:28160\n"
    "s_waitcnt lgkmcnt(6)\n"
    "v_mfma_f32_16x16x32_bf16 v[16:19], v[44:47], v[28:31], 0\n"
    "v_mfma_f32_16x16x32_bf16 v[20:23], v[48:51], v[28:31], 0\n"
    "ds_read_b128 v[44:47], v0 offset:22528\n"
    "ds_read_b128 v[48:51], v0 offset:19456\n"
    "buffer_load_dwordx2 v[8:9], v11, s[64:67], s83 offen sc1\n"
    "v_mfma_f32_16x16x32_bf16 v[24:27], v[28:31], v[28:31], 0\n"
    "s_waitcnt lgkmcnt(6)\n"
    "v_mfma_f32_16x16x32_bf16 v[16:19], v[52:55], v[32:35], v[16:19]\n"
    "ds_read_b128 v[52:55], v0 offset:23552\n"
    "v_mfma_f32_16x16x32_bf16 v[20:23], v[56:59], v[32:35], v[20:23]\n"
    "v_mfma_f32_16x16x32_bf16 v[24:27], v[32:35], v[32:35], v[24:27]\n"
    "s_waitcnt lgkmcnt(5)\n"
    "v_mfma_f32_16x16x32_bf16 v[24:27], v[36:39], v[36:39], v[24:27]\n"
    "v_mfma_f32_16x16x32_bf16 v[16:19], v[60:63], v[36:39], v[16:19]\n"
    "s_waitcnt lgkmcnt(3)\n"
    "v_mfma_f32_16x16x32_bf16 v[24:27], v[40:43], v[40:43], v[24:27]\n"
    "s_waitcnt lgkmcnt(2)\n"
    "v_mfma_f32_16x16x32_bf16 v[20:23], v[44:47], v[36:39], v[20:23]\n"
    "s_waitcnt lgkmcnt(1)\n"
    "v_mfma_f32_16x16x32_bf16 v[16:19], v[48:51], v[40:43], v[16:19]\n"
    "s_waitcnt lgkmcnt(0)\n"
    "v_mfma_f32_16x16x32_bf16 v[20:23], v[52:55], v[40:43], v[20:23]\n"
    "v_bfe_u32 v58, v0, 7, 1\n"
    "v_lshl_add_u32 v56, v58, 3, v12\n"
    "v_lshl_add_u32 v57, v58, 1, v13\n"
    "v_lshrrev_b32 v58, 9, v0\n"
    "v_lshl_add_u32 v57, v58, 2, v57\n"
    "v_mov_b32_dpp v24, v25 row_ror:8 row_mask:0xf bank_mask:0xc\n"
    "v_mov_b32_dpp v26, v27 row_ror:8 row_mask:0xf bank_mask:0xc\n"
    "s_nop 1\n"
    "v_permlane32_swap_b32 v24, v26\n"
    "v_mov_b32_dpp v16, v20 row_ror:8 row_mask:0xf bank_mask:0xc\n"
    "v_mov_b32_dpp v17, v21 row_ror:8 row_mask:0xf bank_mask:0xc\n"
    "v_mov_b32_dpp v18, v22 row_ror:8 row_mask:0xf bank_mask:0xc\n"
    "v_mov_b32_dpp v19, v23 row_ror:8 row_mask:0xf bank_mask:0xc\n"
    "v_max3_f32 v20, v26, v16, v17\n"
    "v_max3_f32 v20, v20, v18, v19\n"
    "s_nop 1\n"
    "v_max_f32_dpp v20, v20, v20 row_ror:8 row_mask:0xf bank_mask:0xf\n"
    "v_mov_b32 v21, v20\n"
    "s_nop 1\n"
    "v_permlane16_swap_b32 v20, v21\n"
    "v_max_f32 v20, v20, v21\n"
    "v_mov_b32 v21, v20\n"
    "s_nop 1\n"
    "v_permlane32_swap_b32 v20, v21\n"
    "v_max_f32 v20, v20, v21\n"
    "v_mul_f32 v20, s86, v20\n"
    "v_fma_f32 v16, v16, s86, -v20\n"
    "v_fma_f32 v17, v17, s86, -v20\n"
    "v_fma_f32 v18, v18, s86, -v20\n"
    "v_fma_f32 v19, v19, s86, -v20\n"
    "v_fma_f32 v26, v26, s86, -v20\n"
    "v_exp_f32 v16, v16\n"
    "v_exp_f32 v17, v17\n"
    "v_exp_f32 v18, v18\n"
    "v_exp_f32 v19, v19\n"
    "v_exp_f32 v26, v26\n"
    "v_add_f32 v20, v16, v17\n"
    "v_add_f32 v21, v18, v19\n"
    "v_add_f32 v20, v20, v21\n"
    "v_add_f32 v20, v20, v26\n"
    "s_nop 1\n"
    "v_add_f32_dpp v20, v20, v20 row_ror:8 row_mask:0xf bank_mask:0xf\n"
    "v_mov_b32 v21, v20\n"
    "s_nop 1\n"
    "v_permlane16_swap_b32 v20, v21\n"
    "v_add_f32 v20, v20, v21\n"
    "v_mov_b32 v21, v20\n"
    "s_nop 1\n"
    "v_permlane32_swap_b32 v20, v21\n"
    "v_add_f32 v20, v20, v21\n"
    "v_rcp_f32 v20, v20\n"
    "s_nop 0\n"
    "v_mul_f32 v16, v16, v20\n"
    "v_mul_f32 v17, v17, v20\n"
    "v_mul_f32 v18, v18, v20\n"
    "v_mul_f32 v19, v19, v20\n"
    "v_mul_f32 v26, v26, v20\n"
    "v_cvt_pk_bf16_f32 v16, v16, v17\n"
    "v_cvt_pk_bf16_f32 v17, v18, v19\n"
    "v_cvt_pk_bf16_f32 v26, v26, v26\n"
    "ds_write_b64 v56, v[16:17]\n"
    "ds_write_b16 v57, v26\n"
    "vcheck_%=:\n"
    "s_waitcnt vmcnt(0)\n"
    "vcheck2_%=:\n"
    "v_max_u32 v20, v8, v9\n"
    "v_cmp_eq_u32 vcc, -1, v20\n"
    "s_cbranch_vccnz vretry_%=\n"
    "v_cndmask_b32 v8, v8, 0, s[78:79]\n"
    "v_cndmask_b32 v9, v9, 0, s[78:79]\n"
    "s_waitcnt lgkmcnt(0)\n"
    "s_barrier\n"
    "ds_read_b128 v[16:19], v12\n"
    "ds_read_b64 v[20:21], v13\n"
    "s_waitcnt lgkmcnt(1)\n"
    "v_mfma_f32_16x16x32_bf16 v[24:27], v[4:7], v[16:19], 0\n"
    "s_waitcnt lgkmcnt(0)\n"
    "v_mfma_f32_16x16x16_bf16 v[28:31], v[8:9], v[20:21], 0\n"
    "s_nop 7\n"
    "v_pk_add_f32 v[24:25], v[24:25], v[28:29]\n"
    "v_pk_add_f32 v[26:27], v[26:27], v[30:31]\n"
    "v_cvt_pk_bf16_f32 v24, v24, v25\n"
    "v_cvt_pk_bf16_f32 v25, v26, v27\n"
    "v_min_u32 v24, -2, v24\n"
    "v_min_u32 v25, -2, v25\n"
    "buffer_store_dwordx2 v[24:25], v14, s[64:67], s84 offen sc1\n"
    "s_sub_u32 s76, s68, 1\n"
    "s_memrealtime s[96:97]\n"
    "v_mov_b32 v16, 29312\n"
    "ds_read_b64 v[16:17], v16\n"
    "s_waitcnt lgkmcnt(0)\n"
    "v_readfirstlane_b32 s92, v17\n"
    "s_cmp_lt_u32 s76, 2\n"
    "s_nop 3\n"
    "s_cbranch_scc0 notok_%=\n"
    "v_lshrrev_b32 v15, 1, v0\n"
    "ds_read_b128 v[28:31], v0 offset:16384\n"
    "ds_read_b128 v[32:35], v0 offset:20480\n"
    "ds_read_b64_tr_b16 v[36:37], v15 offset:24576\n"
    "ds_read_b64_tr_b16 v[38:39], v15 offset:25088\n"
    "ds_read_b128 v[40:43], v0 offset:17408\n"
    "ds_read_b128 v[44:47], v0 offset:21504\n"
    "s_waitcnt vmcnt(1)\n"
    "s_waitcnt lgkmcnt(5)\n"
    "v_mfma_f32_16x16x32_bf16 v[16:19], v[28:31], v[48:51], 0\n"
    "ds_read_b64_tr_b16 v[28:29], v15 offset:25600\n"
    "ds_read_b64_tr_b16 v[30:31], v15 offset:26112\n"
    "s_waitcnt lgkmcnt(6)\n"
    "v_mfma_f32_16x16x32_bf16 v[20:23], v[32:35], v[48:51], 0\n"
    "ds_read_b128 v[32:35], v0 offset:18432\n"
    "s_waitcnt lgkmcnt(5)\n"
    "v_mfma_f32_16x16x32_bf16 v[24:27], v[36:39], v[48:51], 0\n"
    "ds_read_b128 v[36:39], v0 offset:22528\n"
    "s_waitcnt lgkmcnt(5)\n"
    "v_mfma_f32_16x16x32_bf16 v[16:19], v[40:43], v[52:55], v[16:19]\n"
    "ds_read_b64_tr_b16 v[40:41], v15 offset:26624\n"
    "ds_read_b64_tr_b16 v[42:43], v15 offset:27136\n"
    "s_waitcnt lgkmcnt(6)\n"
    "v_mfma_f32_16x16x32_bf16 v[20:23], v[44:47], v[52:55], v[20:23]\n"
    "ds_read_b128 v[44:47], v0 offset:19456\n"
    "s_waitcnt lgkmcnt(5)\n"
    "v_mfma_f32_16x16x32_bf16 v[24:27], v[28:31], v[52:55], v[24:27]\n"
    "ds_read_b128 v[28:31], v0 offset:23552\n"
    "s_waitcnt lgkmcnt(5)\n"
    "v_mfma_f32_16x16x32_bf16 v[16:19], v[32:35], v[56:59], v[16:19]\n"
    "ds_read_b64_tr_b16 v[32:33], v15 offset:27648\n"
    "ds_read_b64_tr_b16 v[34:35], v15 offset:28160\n"
    "s_waitcnt lgkmcnt(6)\n"
    "v_mfma_f32_16x16x32_bf16 v[20:23], v[36:39], v[56:59], v[20:23]\n"
    "s_waitcnt lgkmcnt(4)\n"
    "v_mfma_f32_16x16x32_bf16 v[24:27], v[40:43], v[56:59], v[24:27]\n"
    "s_waitcnt lgkmcnt(3)\n"
    "v_mfma_f32_16x16x32_bf16 v[16:19], v[44:47], v[60:63], v[16:19]\n"
    "s_waitcnt lgkmcnt(2)\n"
    "v_mfma_f32_16x16x32_bf16 v[20:23], v[28:31], v[60:63], v[20:23]\n"
    "s_waitcnt lgkmcnt(0)\n"
    "v_mfma_f32_16x16x32_bf16 v[24:27], v[32:35], v[60:63], v[24:27]\n"
    "s_nop 3\n"
    "v_cvt_pk_bf16_f32 v28, v16, v17\n"
    "v_lshlrev_b32 v16, 16, v28\n"
    "v_and_b32 v17, 0xffff0000, v28\n"
    "v_cvt_pk_bf16_f32 v28, v18, v19\n"
    "v_lshlrev_b32 v18, 16, v28\n"
    "v_and_b32 v19, 0xffff0000, v28\n"
    "v_cvt_pk_bf16_f32 v28, v20, v21\n"
    "v_lshlrev_b32 v20, 16, v28\n"
    "v_and_b32 v21, 0xffff0000, v28\n"
    "v_cvt_pk_bf16_f32 v28, v22, v23\n"
    "v_lshlrev_b32 v22, 16, v28\n"
    "v_and_b32 v23, 0xffff0000, v28\n"
    "v_cvt_pk_bf16_f32 v28, v24, v25\n"
    "v_lshlrev_b32 v24, 16, v28\n"
    "v_and_b32 v25, 0xffff0000, v28\n"
    "v_cvt_pk_bf16_f32 v28, v26, v27\n"
    "v_lshlrev_b32 v26, 16, v28\n"
    "v_and_b32 v27, 0xffff0000, v28\n"
    "v_mul_f32 v16, s87, v16\n"
    "v_mul_f32 v17, s87, v17\n"
    "v_mul_f32 v18, s87, v18\n"
    "v_mul_f32 v19, s87, v19\n"
    "v_mul_f32 v20, s87, v20\n"
    "v_mul_f32 v21, s87, v21\n"
    "v_mul_f32 v22, s87, v22\n"
    "v_mul_f32 v23, s87, v23\n"
    "v_mul_f32 v24, s87, v24\n"
    "v_mul_f32 v25, s87, v25\n"
    "v_mul_f32 v26, s87, v26\n"
    "v_mul_f32 v27, s87, v27\n"
    "v_cvt_pk_bf16_f32 v28, v16, v17\n"
    "v_lshlrev_b32 v16, 16, v28\n"
    "v_and_b32 v17, 0xffff0000, v28\n"
    "v_cvt_pk_bf16_f32 v28, v18, v19\n"
    "v_lshlrev_b32 v18, 16, v28\n"
    "v_and_b32 v19, 0xffff0000, v28\n"
    "v_cvt_pk_bf16_f32 v28, v20, v21\n"
    "v_lshlrev_b32 v20, 16, v28\n"
    "v_and_b32 v21, 0xffff0000, v28\n"
    "v_cvt_pk_bf16_f32 v28, v22, v23\n"
    "v_lshlrev_b32 v22, 16, v28\n"
    "v_and_b32 v23, 0xffff0000, v28\n"
    "v_cvt_pk_bf16_f32 v28, v24, v25\n"
    "v_lshlrev_b32 v24, 16, v28\n"
    "v_and_b32 v25, 0xffff0000, v28\n"
    "v_cvt_pk_bf16_f32 v28, v26, v27\n"
    "v_lshlrev_b32 v26, 16, v28\n"
    "v_and_b32 v27, 0xffff0000, v28\n"
    "v_mov_b32 v28, 0xff800000\n"
    "v_cndmask_b32 v16, v16, v28, s[40:41]\n"
    "v_cndmask_b32 v20, v20, v28, s[48:49]\n"
    "v_cndmask_b32 v24, v28, v24, s[78:79]\n"
    "v_cndmask_b32 v17, v17, v28, s[42:43]\n"
    "v_cndmask_b32 v21, v21, v28, s[50:51]\n"
    "v_cndmask_b32 v25, v28, v25, s[78:79]\n"
    "v_cndmask_b32 v18, v18, v28, s[44:45]\n"
    "v_cndmask_b32 v22, v22, v28, s[52:53]\n"
    "v_cndmask_b32 v26, v28, v26, s[78:79]\n"
    "v_cndmask_b32 v19, v19, v28, s[46:47]\n"
    "v_cndmask_b32 v23, v23, v28, s[54:55]\n"
    "v_cndmask_b32 v27, v28, v27, s[78:79]\n"
    "v_max3_f32 v29, v16, v17, v18\n"
    "v_max3_f32 v29, v29, v19, v20\n"
    "v_max3_f32 v29, v29, v21, v22\n"
    "v_max3_f32 v29, v29, v23, v24\n"
    "v_max3_f32 v29, v29, v25, v26\n"
    "v_max_f32 v29, v29, v27\n"
    "v_mov_b32 v30, v29\n"
    "s_nop 1\n"
    "v_permlane16_swap_b32 v29, v30\n"
    "v_max_f32 v29, v29, v30\n"
    "v_mov_b32 v30, v29\n"
    "s_nop 1\n"
    "v_permlane32_swap_b32 v29, v30\n"
    "v_max_f32 v29, v29, v30\n"
    "v_mul_f32 v29, s88, v29\n"
    "v_fma_f32 v16, v16, s88, -v29\n"
    "v_fma_f32 v17, v17, s88, -v29\n"
    "v_fma_f32 v18, v18, s88, -v29\n"
    "v_fma_f32 v19, v19, s88, -v29\n"
    "v_fma_f32 v20, v20, s88, -v29\n"
    "v_fma_f32 v21, v21, s88, -v29\n"
    "v_fma_f32 v22, v22, s88, -v29\n"
    "v_fma_f32 v23, v23, s88, -v29\n"
    "v_fma_f32 v24, v24, s88, -v29\n"
    "v_fma_f32 v25, v25, s88, -v29\n"
    "v_fma_f32 v26, v26, s88, -v29\n"
    "v_fma_f32 v27, v27, s88, -v29\n"
    "v_exp_f32 v16, v16\n"
    "v_exp_f32 v17, v17\n"
    "v_exp_f32 v18, v18\n"
    "v_exp_f32 v19, v19\n"
    "v_exp_f32 v20, v20\n"
    "v_exp_f32 v21, v21\n"
    "v_exp_f32 v22, v22\n"
    "v_exp_f32 v23, v23\n"
    "v_exp_f32 v24, v24\n"
    "v_exp_f32 v25, v25\n"
    "v_exp_f32 v26, v26\n"
    "v_exp_f32 v27, v27\n"
    "v_add_f32 v29, v16, v17\n"
    "v_add_f32 v29, v29, v18\n"
    "v_add_f32 v29, v29, v19\n"
    "v_add_f32 v29, v29, v20\n"
    "v_add_f32 v29, v29, v21\n"
    "v_add_f32 v29, v29, v22\n"
    "v_add_f32 v29, v29, v23\n"
    "v_add_f32 v29, v29, v24\n"
    "v_add_f32 v29, v29, v25\n"
    "v_add_f32 v29, v29, v26\n"
    "v_add_f32 v29, v29, v27\n"
    "v_mov_b32 v30, v29\n"
    "s_nop 1\n"
    "v_permlane16_swap_b32 v29, v30\n"
    "v_add_f32 v29, v29, v30\n"
    "v_mov_b32 v30, v29\n"
    "s_nop 1\n"
    "v_permlane32_swap_b32 v29, v30\n"
    "v_add_f32 v29, v29, v30\n"
    "v_rcp_f32 v29, v29\n"
    "s_nop 0\n"
    "v_mul_f32 v16, v16, v29\n"
    "v_mul_f32 v17, v17, v29\n"
    "v_mul_f32 v18, v18, v29\n"
    "v_mul_f32 v19, v19, v29\n"
    "v_mul_f32 v20, v20, v29\n"
    "v_mul_f32 v21, v21, v29\n"
    "v_mul_f32 v22, v22, v29\n"
    "v_mul_f32 v23, v23, v29\n"
    "v_mul_f32 v24, v24, v29\n"
    "v_mul_f32 v25, v25, v29\n"
    "v_mul_f32 v26, v26, v29\n"
    "v_mul_f32 v27, v27, v29\n"
    "v_cvt_pk_bf16_f32 v32, v16, v17\n"
    "v_cvt_pk_bf16_f32 v33, v18, v19\n"
    "v_cvt_pk_bf16_f32 v34, v20, v21\n"
    "v_cvt_pk_bf16_f32 v35, v22, v23\n"
    "v_cvt_pk_bf16_f32 v36, v24, v25\n"
    "v_cvt_pk_bf16_f32 v37, v26, v27\n"
    "s_sub_u32 s76, s68, 1\n"
    "s_mul_i32 s76, s76, 1536\n"
    "v_add_u32 v28, s76, v0\n"
    "v_xor_b32 v29, 0x100, v15\n"
    "v_add_u32 v29, s76, v29\n"
    "ds_write_b128 v28, v[32:35] offset:29696\n"
    "ds_write_b64 v29, v[36:37] offset:30720\n"
    "notok_%=:\n"
    "s_add_u32 s89, s80, 1\n"
    "s_cmp_eq_u32 s68, 3\n"
    "s_cbranch_scc1 wave3_%=\n"
    "s_cmp_eq_u32 s68, 7\n"
    "s_cbranch_scc1 wave7_%=\n"
    "wave3done_%=:\n"
    "v_mov_b64 v[16:17], -1\n"
    "v_mov_b64 v[18:19], -1\n"
    "v_lshlrev_b32 v20, 9, v0\n"
    "v_and_b32 v20, 0x30000, v20\n"
    "v_and_b32 v21, 0x70, v0\n"
    "v_or_b32 v20, v20, v21\n"
    "v_and_b32 v21, 0xf0, v0\n"
    "s_add_u32 s76, s80, 2\n"
    "s_and_b32 s76, s76, 3\n"
    "s_lshl_b32 s76, s76, 18\n"
    "s_add_u32 s76, s76, s72\n"
    "buffer_store_dwordx4 v[16:19], v21, s[64:67], s83 offen sc1\n"
    "buffer_store_dwordx4 v[16:19], v20, s[64:67], s76 offen sc1\n"
    "v_lshrrev_b32 v15, 1, v0\n"
    "v_lshlrev_b32 v22, 1, v21\n"
    "v_bfe_u32 v23, v0, 8, 1\n"
    "v_lshl_add_u32 v22, v23, 17, v22\n"
    "v_lshrrev_b32 v23, 9, v0\n"
    "v_lshl_add_u32 v22, v23, 4, v22\n"
    "s_add_u32 s81, s81, 24576\n"
    "s_add_u32 s76, s81, s77\n"
    "s_add_u32 s76, s76, 8192\n"
    "s_mov_b32 m0, s98\n"
    "s_waitcnt lgkmcnt(0)\n"
    "s_barrier\n"
    "s_cmp_eq_u32 s89, 64\n"
    "s_cbranch_scc1 nokx_%=\n"
    "buffer_load_dwordx4 v0, s[64:67], s76 offen sc1 lds\n"
    "nokx_%=:\n"
    "ds_read_b128 v[24:27], v0 offset:29696\n"
    "ds_read_b64 v[28:29], v15 offset:30720\n"
    "ds_read_b128 v[32:35], v0 offset:31232\n"
    "ds_read_b64 v[30:31], v15 offset:32256\n"
    "s_waitcnt lgkmcnt(0)\n"
    "v_mfma_f32_16x16x32_bf16 v[36:39], v[4:7], v[24:27], 0\n"
    "v_mfma_f32_16x16x16_bf16 v[40:43], v[8:9], v[28:29], 0\n"
    "v_mfma_f32_16x16x32_bf16 v[44:47], v[4:7], v[32:35], 0\n"
    "v_mfma_f32_16x16x16_bf16 v[48:51], v[8:9], v[30:31], 0\n"
    "s_nop 5\n"
    "v_pk_add_f32 v[36:37], v[36:37], v[40:41]\n"
    "v_pk_add_f32 v[38:39], v[38:39], v[42:43]\n"
    "v_pk_add_f32 v[44:45], v[44:45], v[48:49]\n"
    "v_pk_add_f32 v[46:47], v[46:47], v[50:51]\n"
    "v_cvt_pk_bf16_f32 v36, v36, v37\n"
    "v_cvt_pk_bf16_f32 v37, v38, v39\n"
    "v_cvt_pk_bf16_f32 v38, v44, v45\n"
    "v_cvt_pk_bf16_f32 v39, v46, v47\n"
    "s_nop 0\n"
    "v_permlane16_swap_b32 v36, v38\n"
    "v_permlane16_swap_b32 v37, v39\n"
    "buffer_store_dwordx4 v[36:39], v22, s[64:67], s82 offen sc1\n"
    "s_cmp_eq_u32 s89, 64\n"
    "s_nop 0\n"
    "s_cbranch_scc1 done_%=\n"
    "s_add_u32 s76, s76, 8192\n"
    "buffer_load_dwordx4 v[4:7], v0, s[64:67], s76 offen sc1\n"
    "s_add_u32 s82, s82, 262144\n"
    "s_and_b32 s76, s89, 3\n"
    "s_lshl_b32 s90, s76, 9\n"
    "s_add_u32 s90, s90, s69\n"
    "s_lshl_b32 s76, s76, 17\n"
    "s_add_u32 s91, s70, s76\n"
    "s_mov_b32 s80, s89\n"
    "s_and_b32 s76, s80, 1\n"
    "s_lshl_b32 s76, s76, 17\n"
    "s_add_u32 s83, s71, s76\n"
    "s_and_b32 s76, s80, 3\n"
    "s_lshl_b32 s76, s76, 18\n"
    "s_add_u32 s84, s72, s76\n"
    "s_mov_b32 s94, 0\n"
    "v_and_b32 v9, 0x33f, v0\n"
    "v_bfe_u32 v8, v0, 6, 1\n"
    "v_lshl_add_u32 v9, v8, 7, v9\n"
    "v_bfe_u32 v8, v0, 7, 1\n"
    "v_lshl_add_u32 v9, v8, 6, v9\n"
    "v_mov_b64 v[16:17], 0\n"
    "v_mov_b64 v[18:19], 0\n"
    "v_mov_b64 v[20:21], 0\n"
    "v_mov_b64 v[22:23], 0\n"
    "v_mov_b32 v8, s80\n"
    "v_add_u32 v15, 0x10000, v2\n"
    "s_cmp_lg_u32 s80, s80\n"
    "s_nop 0\n"
    "s_setpc_b64 s[56:57]\n"
    "notw0_%=:\n"
    "s_waitcnt lgkmcnt(0)\n"
    "s_barrier\n"
    "s_sleep 0\n"
    "buffer_load_dwordx2 v[8:9], v11, s[64:67], s83 offen sc1\n"
    "s_sub_u32 s76, s68, 1\n"
    "s_cmp_lt_u32 s76, 2\n"
    "s_cbranch_scc0 vcheck_%=\n"
    "s_sleep 2\n"
    "s_lshl_b32 s76, s76, 12\n"
    "s_add_u32 s76, s76, s81\n"
    "buffer_load_dwordx4 v[48:51], v0, s[64:67], s76 offen offset:0 sc1\n"
    "buffer_load_dwordx4 v[52:55], v0, s[64:67], s76 offen offset:1024 sc1\n"
    "buffer_load_dwordx4 v[56:59], v0, s[64:67], s76 offen offset:2048 sc1\n"
    "buffer_load_dwordx4 v[60:63], v0, s[64:67], s76 offen offset:3072 sc1\n"
    "s_waitcnt vmcnt(4)\n"
    "s_branch vcheck2_%=\n"
    "vretry_%=:\n"
    "s_nop 7\n"
    "s_sleep 1\n"
    "buffer_load_dwordx2 v[8:9], v11, s[64:67], s83 offen sc1\n"
    "s_add_u32 s95, s95, 1\n"
    "s_cmp_lt_u32 s95, 400000\n"
    "s_cbranch_scc1 vcheck_%=\n"
    "s_branch fail_%=\n"
    "wave7_%=:\n"
    "s_sub_u32 s76, s96, s58\n"
    "v_mov_b32 v16, s76\n"
    "buffer_store_dword v16, off, s[64:67], s101 sc1\n"
    "s_nop 0\n"
    "s_branch wave3done_%=\n"
    "wave3_%=:\n"
    "s_and_b32 s76, s89, 0x43\n"
    "s_cmp_eq_u32 s76, 0\n"
    "s_cbranch_scc0 wave3done_%=\n"
    "prodwait_%=:\n"
    "s_lshl_b32 s76, s89, 10\n"
    "s_add_u32 s76, s76, s100\n"
    "v_mov_b32 v16, 0\n"
    "prodloop_%=:\n"
    "buffer_load_dword v17, v16, s[64:67], s76 offen sc1\n"
    "s_waitcnt vmcnt(0)\n"
    "v_cmp_le_u32 vcc, 3, v17\n"
    "s_nop 4\n"
    "s_cbranch_vccnz wave3done_%=\n"
    "s_sleep 2\n"
    "s_add_u32 s95, s95, 1\n"
    "s_cmp_lt_u32 s95, 400000\n"
    "s_cbranch_scc1 prodloop_%=\n"
    "s_branch fail_%=\n"
    "done_%=:\n"
    "s_mov_b32 %0, 1\n"
    "fail_%=:\n"
    "s_waitcnt vmcnt(0) lgkmcnt(0)\n"
    "s_nop 7\n"
    : "=&s"(ok)
    : "{v[64:71]}"(W[0]), "{v[72:79]}"(W[1]), "{v[80:87]}"(W[2]), "{v[88:95]}"(W[3]), "{v[96:103]}"(W[4]), "{v[104:111]}"(W[5]), "{v[112:119]}"(W[6]), "{v[120:127]}"(W[7]), "{v[128:135]}"(W[8]), "{v[136:143]}"(W[9]), "{v[144:151]}"(W[10]), "{v[152:159]}"(W[11]), "{v[160:167]}"(W[12]), "{v[168:175]}"(W[13]), "{v[176:183]}"(W[14]), "{v[184:191]}"(W[15]), "{v[192:199]}"(W[16]), "{v[200:207]}"(W[17]), "{v[208:215]}"(W[18]), "{v[216:223]}"(W[19]), "{v[224:231]}"(W[20]), "{v[232:239]}"(W[21]), "{v[240:247]}"(W[22]), "{v[248:255]}"(W[23]),
      "{s[64:67]}"(rw), "{s[68:75]}"(sc), "{s99}"(fo), "{s[100:101]}"(po)
    : "memory", "vcc", "scc", "v0", "v1", "v2", "v3", "v4", "v5", "v6", "v7", "v8", "v9", "v10", "v11", "v12", "v13", "v14", "v15", "v16", "v17", "v18", "v19", "v20", "v21", "v22", "v23", "v24", "v25", "v26", "v27", "v28", "v29", "v30", "v31", "v32", "v33", "v34", "v35", "v36", "v37", "v38", "v39", "v40", "v41", "v42", "v43", "v44", "v45", "v46", "v47", "v48", "v49", "v50", "v51", "v52", "v53", "v54", "v55", "v56", "v57", "v58", "v59", "v60", "v61", "v62", "v63", "s40", "s41", "s42", "s43", "s44", "s45", "s46", "s47", "s48", "s49", "s50", "s51", "s52", "s53", "s54", "s55", "s56", "s57", "s58", "s59", "s76", "s77", "s78", "s79", "s80", "s81", "s82", "s83", "s84", "s85", "s86", "s87", "s88", "s89", "s90", "s91", "s92", "s93", "s94", "s95", "s96", "s97", "s98");
  return ok != 0;
}

__device__ __forceinline__ bool vo_loop(const u32x8 (&W)[NREG], rsrc_t rw, u32x8 sc, int fo, u32x2 po) {
  int ok;
  asm volatile(
    "s_mov_b32 %0, 0\n"
    "v_mbcnt_lo_u32_b32 v63, -1, 0\n"
    "v_mbcnt_hi_u32_b32 v63, -1, v63\n"
    "v_lshlrev_b32 v0, 4, v63\n"
    "v_and_b32 v62, 15, v63\n"
    "v_lshrrev_b32 v61, 4, v63\n"
    "v_and_b32 v60, 8, v63\n"
    "v_cmp_ne_u32 vcc, 0, v60\n"
    "v_mov_b32 v1, 0x4444\n"
    "v_mov_b32 v60, 0xeeee\n"
    "v_cndmask_b32 v1, v1, v60, vcc\n"
    "v_and_b32 v2, 0x33f, v0\n"
    "v_bfe_u32 v60, v0, 6, 1\n"
    "v_lshl_add_u32 v2, v60, 7, v2\n"
    "v_bfe_u32 v60, v0, 7, 1\n"
    "v_lshl_add_u32 v2, v60, 6, v2\n"
    "s_lshl_b32 s76, s68, 11\n"
    "v_add_u32 v3, s76, v0\n"
    "v_and_b32 v60, 1, v61\n"
    "v_lshlrev_b32 v4, 9, v60\n"
    "v_lshrrev_b32 v60, 1, v61\n"
    "v_lshl_add_u32 v4, v60, 6, v4\n"
    "v_lshl_add_u32 v4, v62, 2, v4\n"
    "s_lshl_b32 s76, s68, 10\n"
    "s_add_u32 s76, s76, 16384\n"
    "v_add_u32 v4, s76, v4\n"
    "v_bfe_u32 v60, v63, 2, 1\n"
    "v_lshlrev_b32 v5, 8, v60\n"
    "v_bfe_u32 v60, v63, 3, 2\n"
    "v_lshl_add_u32 v5, v60, 4, v5\n"
    "v_bfe_u32 v60, v63, 1, 1\n"
    "v_lshl_add_u32 v5, v60, 6, v5\n"
    "v_and_b32 v60, 1, v63\n"
    "v_lshl_add_u32 v5, v60, 3, v5\n"
    "v_lshrrev_b32 v60, 5, v63\n"
    "v_lshl_add_u32 v5, v60, 2, v5\n"
    "v_and_b32 v56, 31, v63\n"
    "v_lshlrev_b32 v56, 3, v56\n"
    "v_lshl_add_u32 v56, v60, 2, v56\n"
    "s_lshr_b32 s76, s68, 2\n"
    "s_lshl_b32 s76, s76, 13\n"
    "v_add_u32 v57, s76, v0\n"
    "s_and_b32 s76, s68, 3\n"
    "s_lshl_b32 s76, s76, 10\n"
    "s_add_u32 s76, s76, 24576\n"
    "v_add_u32 v58, s76, v0\n"
    "s_mov_b32 s80, 0\n"
    "s_lshl_b32 s77, s68, 15\n"
    "s_add_u32 s77, s77, s73\n"
    "s_memrealtime s[96:97]\n"
    "v_mov_b32 v16, 29312\n"
    "s_waitcnt lgkmcnt(0)\n"
    "v_mov_b32 v18, s96\n"
    "v_mov_b32 v19, s96\n"
    "v_mov_b32 v20, 0\n"
    "ds_write_b64 v16, v[18:19]\n"
    "ds_write_b32 v16, v20 offset:16\n"
    "v_mov_b64 v[8:9], 0\n"
    "v_mov_b64 v[10:11], 0\n"
    "s_mov_b32 s86, 0\n"
    "s_mov_b32 s81, 1792\n"
    "s_mov_b32 s87, 1600\n"
    "s_nop 0\n"
    "s_branch step_%=\n"
    "next7_%=:\n"
    "s_nop 0\n"
    "mwait7_%=:\n"
    "s_memrealtime s[96:97]\n"
    "s_waitcnt lgkmcnt(0)\n"
    "s_sub_u32 s96, s93, s96\n"
    "s_sub_u32 s96, s96, 1\n"
    "s_cmp_lt_u32 s96, 1000\n"
    "s_cbranch_scc1 mwait7_%=\n"
    "mpoll7_%=:\n"
    "buffer_load_dwordx4 v[16:19], v2, s[64:67], s91 offen sc1\n"
    "buffer_load_dwordx4 v[20:23], v2, s[64:67], s91 offen offset:1024 sc1\n"
    "buffer_load_dwordx2 v[26:27], off, s[64:67], s90 sc1\n"
    "v_mov_b32 v25, 29312\n"
    "s_waitcnt vmcnt(2)\n"
    "ds_write_b128 v3, v[16:19]\n"
    "v_max3_u32 v24, v16, v17, v18\n"
    "s_waitcnt vmcnt(1)\n"
    "ds_write_b128 v3, v[20:23] offset:1024\n"
    "v_max3_u32 v24, v24, v19, v20\n"
    "v_max3_u32 v24, v24, v21, v22\n"
    "v_max_u32 v24, v24, v23\n"
    "s_waitcnt vmcnt(0)\n"
    "ds_write_b64 v25, v[26:27]\n"
    "v_max3_u32 v24, v24, v26, v27\n"
    "v_cmp_eq_u32 vcc, -1, v24\n"
    "s_cbranch_vccnz mretry7_%=\n"
    "s_branch step_%=\n"
    "next_%=:\n"
    "s_nop 2\n"
    "mwait_%=:\n"
    "s_memrealtime s[96:97]\n"
    "s_waitcnt lgkmcnt(0)\n"
    "s_sub_u32 s96, s93, s96\n"
    "s_sub_u32 s96, s96, 1\n"
    "s_cmp_lt_u32 s96, 1000\n"
    "s_cbranch_scc1 mwait_%=\n"
    "mpoll_%=:\n"
    "buffer_load_dwordx4 v[16:19], v2, s[64:67], s91 offen sc1\n"
    "buffer_load_dwordx4 v[20:23], v2, s[64:67], s91 offen offset:1024 sc1\n"
    "s_waitcnt vmcnt(1)\n"
    "ds_write_b128 v3, v[16:19]\n"
    "v_max3_u32 v24, v16, v17, v18\n"
    "s_waitcnt vmcnt(0)\n"
    "ds_write_b128 v3, v[20:23] offset:1024\n"
    "v_max3_u32 v24, v24, v19, v20\n"
    "v_max3_u32 v24, v24, v21, v22\n"
    "v_max_u32 v24, v24, v23\n"
    "v_cmp_eq_u32 vcc, -1, v24\n"
    "s_cbranch_vccnz mretry_%=\n"
    "step_%=:\n"
    "s_waitcnt lgkmcnt(0)\n"
    "s_barrier\n"
    "ds_read_b128 v[16:19], v57 offset:0\n"
    "ds_read_b128 v[20:23], v57 offset:1024\n"
    "ds_read_b128 v[24:27], v57 offset:2048\n"
    "ds_read_b128 v[28:31], v57 offset:3072\n"
    "ds_read_b128 v[32:35], v57 offset:4096\n"
    "ds_read_b128 v[36:39], v57 offset:5120\n"
    "ds_read_b128 v[40:43], v57 offset:6144\n"
    "ds_read_b128 v[44:47], v57 offset:7168\n"
    "s_and_b32 s76, s80, 1\n"
    "s_lshl_b32 s76, s76, 17\n"
    "s_add_u32 s83, s71, s76\n"
    "s_and_b32 s76, s80, 3\n"
    "s_lshl_b32 s76, s76, 18\n"
    "s_add_u32 s84, s72, s76\n"
    "s_add_u32 s89, s80, 1\n"
    "s_waitcnt lgkmcnt(7)\n"
    "v_smfmac_f32_16x16x64_bf16 v[8:11], v[16:19], v[64:71], v1\n"
    "s_waitcnt lgkmcnt(6)\n"
    "v_smfmac_f32_16x16x64_bf16 v[8:11], v[20:23], v[72:79], v1\n"
    "s_waitcnt lgkmcnt(5)\n"
    "v_smfmac_f32_16x16x64_bf16 v[8:11], v[24:27], v[80:87], v1\n"
    "s_waitcnt lgkmcnt(4)\n"
    "v_smfmac_f32_16x16x64_bf16 v[8:11], v[28:31], v[88:95], v1\n"
    "s_waitcnt lgkmcnt(3)\n"
    "v_smfmac_f32_16x16x64_bf16 v[8:11], v[32:35], v[96:103], v1\n"
    "s_waitcnt lgkmcnt(2)\n"
    "v_smfmac_f32_16x16x64_bf16 v[8:11], v[36:39], v[104:111], v1\n"
    "s_waitcnt lgkmcnt(1)\n"
    "v_smfmac_f32_16x16x64_bf16 v[8:11], v[40:43], v[112:119], v1\n"
    "s_waitcnt lgkmcnt(0)\n"
    "v_smfmac_f32_16x16x64_bf16 v[8:11], v[44:47], v[120:127], v1\n"
    "s_cmp_lt_u32 s68, 4\n"
    "s_cbranch_scc0 vupper_%=\n"
    "s_barrier\n"
    "ds_read_b128 v[12:15], v58\n"
    "s_waitcnt lgkmcnt(0)\n"
    "s_nop 2\n"
    "v_pk_add_f32 v[8:9], v[8:9], v[12:13]\n"
    "v_pk_add_f32 v[10:11], v[10:11], v[14:15]\n"
    "s_nop 1\n"
    "v_permlane32_swap_b32 v8, v10\n"
    "v_permlane32_swap_b32 v9, v11\n"
    "v_pk_add_f32 v[8:9], v[8:9], v[10:11]\n"
    "v_cvt_pk_bf16_f32 v8, v8, v9\n"
    "v_min_u32 v8, -2, v8\n"
    "buffer_store_dword v8, v56, s[64:67], s83 offen sc1\n"
    "s_nop 0\n"
    "vdone_%=:\n"
    "s_cmp_eq_u32 s89, 64\n"
    "s_cbranch_scc1 done_%=\n"
    "s_cmp_eq_u32 s68, 4\n"
    "s_cbranch_scc0 norearm_%=\n"
    "v_mov_b64 v[16:17], -1\n"
    "v_mov_b64 v[18:19], -1\n"
    "v_lshlrev_b32 v20, 7, v0\n"
    "v_and_b32 v20, 0x18000, v20\n"
    "v_and_b32 v21, 0x70, v0\n"
    "v_or_b32 v20, v20, v21\n"
    "v_and_b32 v21, 0x80, v0\n"
    "v_lshl_add_u32 v20, v21, 1, v20\n"
    "s_add_u32 s76, s80, 3\n"
    "s_and_b32 s76, s76, 3\n"
    "s_lshl_b32 s76, s76, 17\n"
    "s_add_u32 s76, s76, s73\n"
    "buffer_store_dwordx4 v[16:19], v20, s[64:67], s76 offen sc1\n"
    "s_nop 1\n"
    "norearm_%=:\n"
    "v_mov_b32 v16, 29312\n"
    "ds_read_b64 v[16:17], v16\n"
    "s_waitcnt lgkmcnt(0)\n"
    "v_readfirstlane_b32 s92, v16\n"
    "v_readfirstlane_b32 s93, v17\n"
    "s_lshl_b32 s76, s69, 3\n"
    "s_or_b32 s76, s76, s68\n"
    "s_cmp_eq_u32 s76, 5\n"
    "s_cbranch_scc1 reports_%=\n"
    "reportsdone_%=:\n"
    "s_and_b32 s76, s89, 3\n"
    "s_lshl_b32 s90, s76, 9\n"
    "s_add_u32 s90, s90, s74\n"
    "s_lshl_b32 s76, s76, 17\n"
    "s_add_u32 s91, s70, s76\n"
    "s_add_u32 s82, s77, s76\n"
    "s_mov_b32 s94, 0\n"
    "v_mov_b64 v[8:9], 0\n"
    "v_mov_b64 v[10:11], 0\n"
    "v_mov_b64 v[12:13], 0\n"
    "v_mov_b64 v[14:15], 0\n"
    "v_mov_b64 v[32:33], 0\n"
    "v_mov_b64 v[34:35], 0\n"
    "v_mov_b64 v[36:37], 0\n"
    "v_mov_b64 v[38:39], 0\n"
    "s_nop 1\n"
    "owait_%=:\n"
    "s_memrealtime s[96:97]\n"
    "s_waitcnt lgkmcnt(0)\n"
    "s_sub_u32 s96, s92, s96\n"
    "s_sub_u32 s96, s96, 1\n"
    "s_cmp_lt_u32 s96, 1000\n"
    "s_cbranch_scc1 owait_%=\n"
    "buffer_load_dwordx4 v[16:19], v0, s[64:67], s84 offen offset:0 sc1\n"
    "s_sleep 1\n"
    "buffer_load_dwordx4 v[20:23], v0, s[64:67], s84 offen offset:1024 sc1\n"
    "s_sleep 1\n"
    "buffer_load_dwordx4 v[24:27], v0, s[64:67], s84 offen offset:2048 sc1\n"
    "s_sleep 1\n"
    "buffer_load_dwordx4 v[28:31], v0, s[64:67], s84 offen offset:3072 sc1\n"
    "s_waitcnt vmcnt(3)\n"
    "ochk0_%=:\n"
    "v_max3_u32 v6, v16, v17, v18\n"
    "v_max_u32 v6, v6, v19\n"
    "v_cmp_eq_u32 vcc, -1, v6\n"
    "s_cbranch_vccnz oretry0_%=\n"
    "v_smfmac_f32_16x16x64_bf16 v[32:35], v[16:19], v[144:151], v1\n"
    "v_smfmac_f32_16x16x64_bf16 v[36:39], v[16:19], v[152:159], v1\n"
    "v_smfmac_f32_16x16x64_bf16 v[8:11], v[16:19], v[128:135], v1\n"
    "v_smfmac_f32_16x16x64_bf16 v[12:15], v[16:19], v[136:143], v1\n"
    "s_waitcnt vmcnt(2)\n"
    "ochk1_%=:\n"
    "v_max3_u32 v6, v20, v21, v22\n"
    "v_max_u32 v6, v6, v23\n"
    "v_cmp_eq_u32 vcc, -1, v6\n"
    "s_cbranch_vccnz oretry1_%=\n"
    "v_smfmac_f32_16x16x64_bf16 v[32:35], v[20:23], v[176:183], v1\n"
    "v_smfmac_f32_16x16x64_bf16 v[36:39], v[20:23], v[184:191], v1\n"
    "v_smfmac_f32_16x16x64_bf16 v[8:11], v[20:23], v[160:167], v1\n"
    "v_smfmac_f32_16x16x64_bf16 v[12:15], v[20:23], v[168:175], v1\n"
    "s_waitcnt vmcnt(1)\n"
    "ochk2_%=:\n"
    "v_max3_u32 v6, v24, v25, v26\n"
    "v_max_u32 v6, v6, v27\n"
    "v_cmp_eq_u32 vcc, -1, v6\n"
    "s_cbranch_vccnz oretry2_%=\n"
    "v_smfmac_f32_16x16x64_bf16 v[32:35], v[24:27], v[208:215], v1\n"
    "v_smfmac_f32_16x16x64_bf16 v[36:39], v[24:27], v[216:223], v1\n"
    "v_smfmac_f32_16x16x64_bf16 v[8:11], v[24:27], v[192:199], v1\n"
    "v_smfmac_f32_16x16x64_bf16 v[12:15], v[24:27], v[200:207], v1\n"
    "s_waitcnt vmcnt(0)\n"
    "ochk3_%=:\n"
    "v_max3_u32 v6, v28, v29, v30\n"
    "v_max_u32 v6, v6, v31\n"
    "v_cmp_eq_u32 vcc, -1, v6\n"
    "s_cbranch_vccnz oretry3_%=\n"
    "v_smfmac_f32_16x16x64_bf16 v[32:35], v[28:31], v[240:247], v1\n"
    "v_smfmac_f32_16x16x64_bf16 v[36:39], v[28:31], v[248:255], v1\n"
    "v_smfmac_f32_16x16x64_bf16 v[8:11], v[28:31], v[224:231], v1\n"
    "v_smfmac_f32_16x16x64_bf16 v[12:15], v[28:31], v[232:239], v1\n"
    "s_nop 6\n"
    "v_mov_b32_dpp v32, v8 quad_perm:[0,1,2,3] row_mask:0x5 bank_mask:0xf\n"
    "v_mov_b32_dpp v33, v9 quad_perm:[0,1,2,3] row_mask:0x5 bank_mask:0xf\n"
    "v_mov_b32_dpp v34, v10 quad_perm:[0,1,2,3] row_mask:0x5 bank_mask:0xf\n"
    "v_mov_b32_dpp v35, v11 quad_perm:[0,1,2,3] row_mask:0x5 bank_mask:0xf\n"
    "v_mov_b32_dpp v36, v12 quad_perm:[0,1,2,3] row_mask:0x5 bank_mask:0xf\n"
    "v_mov_b32_dpp v37, v13 quad_perm:[0,1,2,3] row_mask:0x5 bank_mask:0xf\n"
    "v_mov_b32_dpp v38, v14 quad_perm:[0,1,2,3] row_mask:0x5 bank_mask:0xf\n"
    "v_mov_b32_dpp v39, v15 quad_perm:[0,1,2,3] row_mask:0x5 bank_mask:0xf\n"
    "v_permlane32_swap_b32 v32, v36\n"
    "v_permlane32_swap_b32 v33, v37\n"
    "v_permlane32_swap_b32 v34, v38\n"
    "v_permlane32_swap_b32 v35, v39\n"
    "v_pk_add_f32 v[8:9], v[32:33], v[36:37]\n"
    "v_pk_add_f32 v[10:11], v[34:35], v[38:39]\n"
    "ds_write2_b32 v4, v8, v9 offset1:32\n"
    "ds_write2_b32 v4, v10, v11 offset0:64 offset1:96\n"
    "s_cmp_lt_u32 s68, 4\n"
    "s_waitcnt lgkmcnt(0)\n"
    "s_barrier\n"
    "s_cbranch_scc0 nored_%=\n"
    "ds_read_b128 v[16:19], v0 offset:16384\n"
    "ds_read_b128 v[20:23], v0 offset:17408\n"
    "ds_read_b128 v[24:27], v0 offset:18432\n"
    "ds_read_b128 v[28:31], v0 offset:19456\n"
    "ds_read_b128 v[32:35], v0 offset:20480\n"
    "ds_read_b128 v[36:39], v0 offset:21504\n"
    "ds_read_b128 v[40:43], v0 offset:22528\n"
    "ds_read_b128 v[44:47], v0 offset:23552\n"
    "s_waitcnt lgkmcnt(6)\n"
    "v_pk_add_f32 v[16:17], v[16:17], v[20:21]\n"
    "v_pk_add_f32 v[18:19], v[18:19], v[22:23]\n"
    "s_waitcnt lgkmcnt(4)\n"
    "v_pk_add_f32 v[24:25], v[24:25], v[28:29]\n"
    "v_pk_add_f32 v[26:27], v[26:27], v[30:31]\n"
    "v_pk_add_f32 v[16:17], v[16:17], v[24:25]\n"
    "v_pk_add_f32 v[18:19], v[18:19], v[26:27]\n"
    "s_waitcnt lgkmcnt(2)\n"
    "v_pk_add_f32 v[32:33], v[32:33], v[36:37]\n"
    "v_pk_add_f32 v[34:35], v[34:35], v[38:39]\n"
    "v_pk_add_f32 v[16:17], v[16:17], v[32:33]\n"
    "v_pk_add_f32 v[18:19], v[18:19], v[34:35]\n"
    "s_waitcnt lgkmcnt(0)\n"
    "v_pk_add_f32 v[40:41], v[40:41], v[44:45]\n"
    "v_pk_add_f32 v[42:43], v[42:43], v[46:47]\n"
    "v_pk_add_f32 v[16:17], v[16:17], v[40:41]\n"
    "v_pk_add_f32 v[18:19], v[18:19], v[42:43]\n"
    "s_nop 1\n"
    "v_permlane32_swap_b32 v16, v18\n"
    "v_permlane32_swap_b32 v17, v19\n"
    "v_pk_add_f32 v[16:17], v[16:17], v[18:19]\n"
    "v_cvt_pk_bf16_f32 v16, v16, v17\n"
    "v_min_u32 v16, -2, v16\n"
    "buffer_store_dword v16, v5, s[64:67], s82 offen sc1\n"
    "s_memrealtime s[96:97]\n"
    "s_waitcnt lgkmcnt(0)\n"
    "s_add_u32 s96, s96, 22\n"
    "redjoin_%=:\n"
    "s_sub_i32 s76, s96, s93\n"
    "s_cmp_gt_i32 s76, 0\n"
    "s_cselect_b32 s93, s96, s93\n"
    "v_mov_b64 v[8:9], 0\n"
    "v_mov_b64 v[10:11], 0\n"
    "s_mov_b32 s80, s89\n"
    "s_mov_b32 s94, 0\n"
    "s_cmp_eq_u32 s68, 7\n"
    "s_nop 0\n"
    "s_cbranch_scc1 next7_%=\n"
    "s_branch next_%=\n"
    "vupper_%=:\n"
    "s_nop 7\n"
    "ds_write_b128 v58, v[8:11]\n"
    "s_waitcnt lgkmcnt(0)\n"
    "s_barrier\n"
    "s_branch vdone_%=\n"
    "nored_%=:\n"
    "s_memrealtime s[96:97]\n"
    "s_lshl_b32 s76, s69, 3\n"
    "s_or_b32 s76, s76, s68\n"
    "s_cmp_eq_u32 s76, 5\n"
    "s_waitcnt lgkmcnt(0)\n"
    "s_cbranch_scc1 times_%=\n"
    "timesdone_%=:\n"
    "s_add_u32 s96, s96, 31\n"
    "s_branch redjoin_%=\n"
    "reports_%=:\n"
    "v_lshlrev_b32 v16, 3, v0\n"
    "v_and_b32 v16, 0xf80, v16\n"
    "s_add_u32 s76, s73, 788480\n"
    "buffer_load_dword v17, v16, s[64:67], s76 offen sc1\n"
    "s_waitcnt vmcnt(0)\n"
    "v_max_u32_dpp v17, v17, v17 quad_perm:[1,0,3,2] row_mask:0xf bank_mask:0xf\n"
    "s_nop 1\n"
    "v_max_u32_dpp v17, v17, v17 quad_perm:[2,3,0,1] row_mask:0xf bank_mask:0xf\n"
    "s_nop 1\n"
    "v_max_u32_dpp v17, v17, v17 row_ror:4 row_mask:0xf bank_mask:0xf\n"
    "s_nop 1\n"
    "v_max_u32_dpp v17, v17, v17 row_ror:8 row_mask:0xf bank_mask:0xf\n"
    "s_nop 0\n"
    "v_readlane_b32 s76, v17, 0\n"
    "v_readlane_b32 s88, v17, 16\n"
    "s_max_u32 s76, s76, s88\n"
    "s_cmp_gt_u32 s76, 200\n"
    "s_nop 2\n"
    "s_cbranch_scc1 reportsdone_%=\n"
    "s_lshl_b32 s76, s76, 4\n"
    "s_sub_i32 s76, s76, s81\n"
    "s_min_i32 s76, s76, 192\n"
    "s_ashr_i32 s76, s76, 1\n"
    "s_add_i32 s81, s81, s76\n"
    "s_branch reportsdone_%=\n"
    "times_%=:\n"
    "v_mov_b32 v16, 29312\n"
    "ds_read_b32 v17, v16 offset:16\n"
    "v_mov_b32 v18, 0\n"
    "s_sub_u32 s88, s96, s92\n"
    "s_waitcnt lgkmcnt(0)\n"
    "ds_write_b32 v16, v18 offset:16\n"
    "v_readfirstlane_b32 s76, v17\n"
    "s_min_u32 s88, s88, 150\n"
    "s_add_u32 s88, s88, 39\n"
    "s_lshl_b32 s88, s88, 4\n"
    "s_sub_i32 s88, s88, s87\n"
    "s_ashr_i32 s88, s88, 2\n"
    "s_cmp_eq_u32 s76, 0\n"
    "s_cselect_b32 s88, s88, 0\n"
    "s_add_i32 s87, s87, s88\n"
    "s_cmp_eq_u32 s76, 0\n"
    "s_cselect_b32 s88, -6, 48\n"
    "s_add_i32 s86, s86, s88\n"
    "s_max_i32 s86, s86, 0\n"
    "s_min_i32 s86, s86, 320\n"
    "s_add_u32 s88, s96, 31\n"
    "s_add_u32 s78, s88, 18\n"
    "s_sub_i32 s76, s93, s88\n"
    "s_cmp_ge_i32 s76, 0\n"
    "s_cselect_b32 s88, s93, s78\n"
    "s_add_u32 s76, s81, s86\n"
    "s_lshr_b32 s76, s76, 4\n"
    "s_add_u32 s88, s88, s76\n"
    "s_add_u32 s88, s88, 52\n"
    "v_mov_b32 v18, s88\n"
    "s_lshr_b32 s76, s87, 4\n"
    "s_add_u32 s88, s88, s76\n"
    "v_mov_b32 v19, s88\n"
    "v_min_u32 v18, -2, v18\n"
    "v_min_u32 v19, -2, v19\n"
    "v_lshlrev_b32 v16, 3, v0\n"
    "s_and_b32 s76, s89, 3\n"
    "s_lshl_b32 s76, s76, 9\n"
    "s_add_u32 s88, s73, 786432\n"
    "s_add_u32 s76, s76, s88\n"
    "s_mov_b64 exec, 15\n"
    "buffer_store_dwordx2 v[18:19], v16, s[64:67], s76 offen sc1\n"
    "v_mov_b64 v[20:21], -1\n"
    "s_add_u32 s76, s80, 3\n"
    "s_and_b32 s76, s76, 3\n"
    "s_lshl_b32 s76, s76, 9\n"
    "s_add_u32 s76, s76, s88\n"
    "buffer_store_dwordx2 v[20:21], v16, s[64:67], s76 offen sc1\n"
    "s_mov_b64 exec, -1\n"
    "s_nop 0\n"
    "s_branch timesdone_%=\n"
    "oretry0_%=:\n"
    "v_mov_b32 v6, 29312\n"
    "v_mov_b32 v7, 1\n"
    "ds_write_b32 v6, v7 offset:16\n"
    "s_sleep 1\n"
    "s_waitcnt lgkmcnt(0)\n"
    "buffer_load_dwordx4 v[16:19], v0, s[64:67], s84 offen offset:0 sc1\n"
    "buffer_load_dwordx4 v[20:23], v0, s[64:67], s84 offen offset:1024 sc1\n"
    "buffer_load_dwordx4 v[24:27], v0, s[64:67], s84 offen offset:2048 sc1\n"
    "buffer_load_dwordx4 v[28:31], v0, s[64:67], s84 offen offset:3072 sc1\n"
    "s_add_u32 s94, s94, 1\n"
    "s_cmp_lt_u32 s94, 400000\n"
    "s_waitcnt vmcnt(0)\n"
    "s_cbranch_scc1 ochk0_%=\n"
    "s_branch fail_%=\n"
    "oretry1_%=:\n"
    "v_mov_b32 v6, 29312\n"
    "v_mov_b32 v7, 1\n"
    "ds_write_b32 v6, v7 offset:16\n"
    "s_sleep 1\n"
    "s_waitcnt lgkmcnt(0)\n"
    "buffer_load_dwordx4 v[20:23], v0, s[64:67], s84 offen offset:1024 sc1\n"
    "buffer_load_dwordx4 v[24:27], v0, s[64:67], s84 offen offset:2048 sc1\n"
    "buffer_load_dwordx4 v[28:31], v0, s[64:67], s84 offen offset:3072 sc1\n"
    "s_add_u32 s94, s94, 1\n"
    "s_cmp_lt_u32 s94, 400000\n"
    "s_waitcnt vmcnt(0)\n"
    "s_cbranch_scc1 ochk1_%=\n"
    "s_branch fail_%=\n"
    "oretry2_%=:\n"
    "v_mov_b32 v6, 29312\n"
    "v_mov_b32 v7, 1\n"
    "ds_write_b32 v6, v7 offset:16\n"
    "s_sleep 1\n"
    "s_waitcnt lgkmcnt(0)\n"
    "buffer_load_dwordx4 v[24:27], v0, s[64:67], s84 offen offset:2048 sc1\n"
    "buffer_load_dwordx4 v[28:31], v0, s[64:67], s84 offen offset:3072 sc1\n"
    "s_add_u32 s94, s94, 1\n"
    "s_cmp_lt_u32 s94, 400000\n"
    "s_waitcnt vmcnt(0)\n"
    "s_cbranch_scc1 ochk2_%=\n"
    "s_branch fail_%=\n"
    "oretry3_%=:\n"
    "v_mov_b32 v6, 29312\n"
    "v_mov_b32 v7, 1\n"
    "ds_write_b32 v6, v7 offset:16\n"
    "s_sleep 1\n"
    "s_waitcnt lgkmcnt(0)\n"
    "buffer_load_dwordx4 v[28:31], v0, s[64:67], s84 offen offset:3072 sc1\n"
    "s_add_u32 s94, s94, 1\n"
    "s_cmp_lt_u32 s94, 400000\n"
    "s_waitcnt vmcnt(0)\n"
    "s_cbranch_scc1 ochk3_%=\n"
    "s_branch fail_%=\n"
    "mretry_%=:\n"
    "s_nop 7\n"
    "s_sleep 1\n"
    "s_add_u32 s94, s94, 1\n"
    "s_cmp_lt_u32 s94, 400000\n"
    "s_cbranch_scc1 mpoll_%=\n"
    "s_branch fail_%=\n"
    "mretry7_%=:\n"
    "s_nop 7\n"
    "s_sleep 1\n"
    "s_add_u32 s94, s94, 1\n"
    "s_cmp_lt_u32 s94, 400000\n"
    "s_cbranch_scc1 mpoll7_%=\n"
    "s_branch fail_%=\n"
    "done_%=:\n"
    "s_mov_b32 %0, 1\n"
    "fail_%=:\n"
    "s_waitcnt vmcnt(0) lgkmcnt(0)\n"
    "s_nop 7\n"
    : "=&s"(ok)
    : "{v[64:71]}"(W[0]), "{v[72:79]}"(W[1]), "{v[80:87]}"(W[2]), "{v[88:95]}"(W[3]), "{v[96:103]}"(W[4]), "{v[104:111]}"(W[5]), "{v[112:119]}"(W[6]), "{v[120:127]}"(W[7]), "{v[128:135]}"(W[8]), "{v[136:143]}"(W[9]), "{v[144:151]}"(W[10]), "{v[152:159]}"(W[11]), "{v[160:167]}"(W[12]), "{v[168:175]}"(W[13]), "{v[176:183]}"(W[14]), "{v[184:191]}"(W[15]), "{v[192:199]}"(W[16]), "{v[200:207]}"(W[17]), "{v[208:215]}"(W[18]), "{v[216:223]}"(W[19]), "{v[224:231]}"(W[20]), "{v[232:239]}"(W[21]), "{v[240:247]}"(W[22]), "{v[248:255]}"(W[23]),
      "{s[64:67]}"(rw), "{s[68:75]}"(sc), "{s99}"(fo), "{s[100:101]}"(po)
    : "memory", "vcc", "scc", "v0", "v1", "v2", "v3", "v4", "v5", "v6", "v7", "v8", "v9", "v10", "v11", "v12", "v13", "v14", "v15", "v16", "v17", "v18", "v19", "v20", "v21", "v22", "v23", "v24", "v25", "v26", "v27", "v28", "v29", "v30", "v31", "v32", "v33", "v34", "v35", "v36", "v37", "v38", "v39", "v40", "v41", "v42", "v43", "v44", "v45", "v46", "v47", "v48", "v49", "v50", "v51", "v52", "v53", "v54", "v55", "v56", "v57", "v58", "v59", "v60", "v61", "v62", "v63", "s40", "s41", "s42", "s43", "s44", "s45", "s46", "s47", "s48", "s49", "s50", "s51", "s52", "s53", "s54", "s55", "s56", "s57", "s58", "s59", "s76", "s77", "s78", "s79", "s80", "s81", "s82", "s83", "s84", "s85", "s86", "s87", "s88", "s89", "s90", "s91", "s92", "s93", "s94", "s95", "s96", "s97", "s98");
  return ok != 0;
}

__device__ __forceinline__ bool chain_role(KP p, int h, int rh, int w, LAS char* lds) {
  const int l0 = lane_id();
  const unsigned long long votes = __builtin_amdgcn_ballot_w64(l0 < 10 && (cword(p, C_PROBE)[l0 < 10 ? l0 : 0] & 1));
  const int xa = xoff(p) + (__builtin_popcountll(votes & 31) >= 3 ? 8192 : 0), xb = xoff(p) + (__builtin_popcountll(votes >> 5) >= 3 ? 8192 : 0);
  const int cp = xcc_id() & 3, vpiece = xa + XV_OFF + (h >> 2) * 16384 + (h & 3) * 2048;
  u32x8 sc;
  sc[0] = w;
  sc[2] = xa + cp * XM_COPY + (w >> 2) * 16384 + (w & 3) * 2048;
  sc[7] = WS_END + ((rh + 1) * 32 + h) * 8 * 64 * 48 + w * 64 * 48;
  u32x8 W[NREG];
  if (rh < 0) {
    const int l = lane_id();
    const rsrc_t rq = mkrsrc(p->Wq + h * DH, W_BYTES - h * DH * 2), rk = mkrsrc(p->Wk + h * DH, W_BYTES - h * DH * 2);
    load_ring<32, 24>(lds, w, l, [&](int n, LAS char* dst) { wstage(n < 16 ? rq : rk, (2 * (n & 7) + ((n >> 3) & 1)) * 64 * 8192, dst, w, l); }, W);
    stage_m0(p, lds, w, l);
    WSTAMP(232, h, 0);
    unsigned spins = 0;
#pragma nounroll
    while (w == 0 && (unsigned)__builtin_amdgcn_readfirstlane(__hip_atomic_load((unsigned*)(p->ws + OFF_PROD + h * 128), RLX_AGENT)) < 3u) {
      __builtin_amdgcn_s_sleep(3);
      if (++spins > SPIN_CAP) return false;
    }
    bar();
    WSTAMP(232, h, 1);
    sc[1] = xa + XT_OFF + cp * 128;
    sc[3] = vpiece + w * 256;
    sc[4] = xb + XO_OFF + (h >> 3) * 16384 + (h & 7) * 1024 + w * 128;
    sc[5] = OFF_QKV + h * QKV_H;
    static_assert(R_QK % 32 == 0 && OFF_FLAG % 32768 == 0, "the loop takes h >> 3 from the flag offset");
    sc[6] = OFF_OT + (h * 8 + w) * 512;
    if (!qk_loop(W, wsr(p), sc, OFF_FLAG + (R_QK + h) * 1024  ,
                 u32x2{(unsigned)(OFF_PROD + h * 128), (unsigned)(xa + XT_OFF + 2048 + h * 128)})) return false;
    bar();
    if (w == 0) post(kparams(), R_QK + h, 64);
    WSTAMP(232, h, 2);
  } else {
    const int l = lane_id();
    const rsrc_t rv = mkrsrc(p->Wv, W_BYTES), ro = mkrsrc(p->Wo, W_BYTES);
    const int d = 16 * (l >> 5) + 4 * ((l >> 4) & 1) + ((l >> 2) & 1) + 8 * ((l >> 3) & 1);
    const int voo = (2 * (d & ~7) + (d & 7)) * 2048 + (l & 3) * 16;
    const int vov = (d + ((l >> 1) & 1) * 64) * 8192 + (l & 1) * 16;
    LAS char* wd = lds + w * 20480;
    auto stage = [&](int n) {
#pragma unroll
      for (int i = 0; i < 4; ++i) {
        if (n < 4) glds<0>(rv, wd + (n % 5) * 4096 + i * 1024, vov, ((8 * (w >> 2) + 2 * n) * 64 + 2 * (i & 1) + 32 * (i >> 1)) * 8192 + h * 256 + (4 * rh + (w & 3)) * 32);
        else glds<0>(ro, wd + (n % 5) * 4096 + i * 1024, voo, ((4 * w + ((n - 4) >> 1)) * 128 + 8 * (n & 1) + 2 * (i & 1) + 64 * (i >> 1)) * 2048 + h * 64);
      }
    };
#pragma unroll
    for (int n = 0; n < 5; ++n) stage(n);
#pragma unroll
    for (int n = 0; n < 12; ++n) {
      if (n < 8) WAITV(16); else if (n == 8) WAITV(12); else if (n == 9) WAITV(8); else if (n == 10) WAITV(4); else WAITV(0);
#pragma unroll
      for (int nt = 0; nt < 2; ++nt) {
        LAS const char* a = wd + (n % 5) * 4096 + (l >> 2) * 64 + nt * 32 + (l & 3) * 8;
        const u32x2 x0 = trread(a), x1 = trread(a + 1024), x2 = trread(a + 2048), x3 = trread(a + 3072);
        W[2 * n + nt] = u32x8{x0.x, x0.y, x1.x, x1.y, x2.x, x2.y, x3.x, x3.y};
      }
      WAITL(0);
      SCHED();
      if (n + 5 < 12) stage(n + 5);
    }
    bar();
    stage_m0(p, lds, w, l);
    WSTAMP(233, rh * 32 + h, 0);
    sc[1] = rh * 32 + h;
    sc[3] = vpiece + (4 * rh + (w & 3)) * 256;
    sc[4] = xb + XO_OFF + ((cp & 1) * 2 + rh) * 65536 + (w >> 1) * 16384 + (w & 1) * 4096;
    sc[5] = xa + (h >> 4) * 16384 + ((2 * h) & 31) * 256 + rh * 128;
    sc[6] = xa + XT_OFF + cp * 128;
    if (!vo_loop(W, wsr(p), sc, 0, u32x2{0u, 0u})) return false;
  }
  return true;
}

__device__ __forceinline__ bool produce(LAS char* lds, int w) {
  const int x = blockIdx.x & 7, i = blockIdx.x >> 3;
  {
    const KP p = kparams();
    const int wi = i < 24 ? i * 4 + (x & 3) : 96 + (i - 24) * 8 + x;
    if (wi >= 64) ln1_rows<2>(p, wi * 8 + w, 768); else ln1_rows<1>(p, wi * 8 + w, 0);
    unsigned wd = 0;
    if (w > 0) touch(wd, mkrsrc(p->Wo, W_BYTES), lane_id() * 128, sgpr((wi * 7 + w - 1) * 8192));
    if (w > 0) WAITV(1); else WAITV(0);
    bar();
    const int l = lane_id();
    if (w == 0 && l < (wi >= 64 ? 2 : 1)) __hip_atomic_fetch_add(cword(p, C_H + ((wi + 96 * l) >> 4)), 1u, RLX_AGENT);
    asm volatile("s_waitcnt vmcnt(0)" ::"v"(wd) : "memory");
  }
  auto wait_h = [&](int lo, int n) {
    const KP p = kparams();
    if (w == 0) {
      const unsigned* cp = cword(p, C_H + lo + min(lane_id(), n - 1));
      unsigned spins = 0;
#pragma nounroll
      while (!none(__hip_atomic_load(cp, RLX_AGENT) < 16u) && ++spins < SPIN_CAP) __builtin_amdgcn_s_sleep(3);
      *(LAS volatile int*)(lds + 163824) = spins < SPIN_CAP;
    }
    WAITL(0);
    bar();
    const bool ok = __builtin_amdgcn_readfirstlane(*(LAS volatile int*)(lds + 163824)) != 0;
    bar();
    return ok;
  };
  if (x < 4 && i == 31) return true;
  const bool late = i >= (x < 4 ? 28 : 31);
  const int n = x < 4 ? i - 24 : i, nc = x < 4 ? 1 : 11, h0 = x < 4 ? x * 2 : 8 + (x - 4) * 22, e = (x & 3) * 4 + (i & 3);
  const bool small = !late && n < 2 * nc;
  if (!wait_h(0, small ? 1 : 2)) return false;
  if (small) qkv_tiles<128, 128>(lds, w, 1, [&](int) { return h0 + n; });
  if (small && !wait_h(0, 16)) return false;
  if (late ? e < 8 : n < 3 * nc || x >= 4)
    qkv_tiles<128, 256>(lds, w, 1, [&](int) { return late ? 128 + 8 + (e >> 1) * 22 + 2 * (9 + (e & 1)) : small ? (2 + n / nc) * 128 + h0 + 2 * (n % nc) : 128 + h0 + 2 * (n - 2 * nc); });
  if (!small && !wait_h(0, 16)) return false;
  qkv_tiles<256, 256>(lds, w, late ? 5 : x < 4 ? (small ? 1 : 2) : i < 15 ? 2 : 1, [&](int t) {
    if (x < 4 && !late) return (4 + 2 * ((n ^ 3) + 4 * t)) * 128 + h0;
    const int k = late ? 46 + t * 4 + (e & 3) : t ? 31 + n : small ? n + 9 : n - 22, hb = late ? 8 + (e >> 2) * 22 : h0;
    return (4 + 2 * (k / 11)) * 128 + hb + 2 * (k % 11);
  });
  return true;
}
static_assert(__builtin_offsetof(Params, Wq) == 16 && __builtin_offsetof(Params, Wv) == 32, "argument offsets");

__device__ __forceinline__ const unsigned* flagp(KP p, int dep, int n) {
  return (const unsigned*)(p->ws + OFF_FLAG + ((dep + min(lane_id(), n - 1)) * 8 + (blockIdx.x & 7)) * 128);
}
__device__ __forceinline__ void peek_load(unsigned& d, const unsigned* fp) { asm volatile("global_load_dword %0, %1, off sc1" : "+v"(d) : "v"(fp) : "memory"); }
__device__ __forceinline__ int await(KP p, LAS char* word, int w, int dep, int n, int need) {
  if (w == 0) {
    const unsigned* fp = flagp(p, dep, n);
    unsigned v, spins = 0;
#pragma nounroll
    for (;;) {
      v = __hip_atomic_load(fp, RLX_AGENT);
      if (none(v < (unsigned)need)) break;
      __builtin_amdgcn_s_sleep(WSLEEP);
      if (++spins > SPIN_CAP || ((spins & 63u) == 0u && __builtin_amdgcn_readfirstlane(__hip_atomic_load(cword(p, C_TMO), RLX_AGENT)) != 0)) {
        __hip_atomic_store(cword(p, C_TMO), 1u, RLX_AGENT);
        v = 0;
        break;
      }
    }
    *(LAS volatile unsigned*)word = !none(v == 0u) ? 0 : none(v < need + 1u) ? need + 1 : need;
  }
  WAITL(0);
  bar();
  return __builtin_amdgcn_readfirstlane(*(LAS volatile int*)word);
}

struct Stage {
  int role, dep, ndep, nunit;
  const bf16_t* wgt; int wbytes, w_off, ldw;
  int a_off, a_unit, a_kt, a_tile;
  int shift;
  int o_off, o_tile;
  int si, sn;
};
template <int TM, int NKT, bool GELU> __device__ __forceinline__ void stage_gemm(LAS char* lds, int w, const Stage& st) {
  constexpr int TN = 2, KTB = TM * 2048, ND = TM == 2 ? NKT : NKT / 2;
  static_assert(TM == 2 || NKT == 8, "shape");
  const KP p = kparams();
  const rsrc_t rw = wsr(p);
  const int l = lane_id(), r16 = l & 15, g = l >> 4, kh = w >> 2, cg = w & 3;
  u32x4 Wf[2 * NKT][TN];
  {
    const rsrc_t rb = mkrsrc(st.wgt, st.wbytes);
    const int vo = (8 * (l >> 4) + ((l >> 2) & 3)) * st.ldw + (l & 3) * 16;
    LAS char* wd = lds + w * 16384;
#pragma unroll
    for (int hf = 0; hf < 2; ++hf) {
#pragma unroll
      for (int k = 0; k < 2 * NKT; ++k) glds<0>(rb, wd + k * 1024, vo, st.w_off + ((kh * 2 * NKT + hf * NKT + (k >> 1)) * 32 + (k & 1) * 4) * st.ldw + cg * 64);
      WAITV(0);
#pragma unroll
      for (int k = 0; k < 2 * NKT; ++k) {
        LAS const char* a = wd + (k >> 1) * 2048 + (l >> 2) * 64 + (k & 1) * 32 + (l & 3) * 8;
        const u32x2 lo = trread(a), hi = trread(a + 1024);
        Wf[hf * NKT + (k >> 1)][k & 1] = u32x4{lo.x, lo.y, hi.x, hi.y};
      }
      WAITL(0);
      SCHED();
    }
  }
  int va[2][2];
  if (GELU) {
    const int row = 8 * (TM == 2 ? cg : cg & 1) + (l >> 3);
    va[0][0] = va[0][1] = va[1][0] = va[1][1] = row * 2048 + (((l & 7) ^ ((row >> 1) & 7)) << 4);
  } else {
#pragma unroll
    for (int i = 0; i < 4; ++i) {
      const int hh = i < 2 ? kh : 0;
      const int rr = (TM == 2 ? 16 * (cg >> 1) : 0) + ((l >> 1) & 15) + (st.shift ? 14 + (i & 1 ? hh + 1 : -hh - 1) : 0);
      va[i >> 1][i & 1] = (rr >> 4) * st.a_tile + (cg & 1) * 1024 + (l >> 5) * 512 + (rr & 15) * 32 + (l & 1) * 16;
    }
  }
  const int fa = kh * NKT * KTB + (GELU ? r16 * 128 + ((g ^ (r16 >> 1)) << 4) : (g >> 1) * 512 + r16 * 32 + (g & 1) * 16);
  auto piece = [&](int u, int i, int low = 0) {
    const int hh = low ? 0 : kh;
    const int so = st.a_off + u * st.a_unit + (hh * NKT + (TM == 2 ? 0 : cg >> 1)) * st.a_kt;
    glds<AUX_SC1>(rw, lds + (u & 1) * 65536 + hh * NKT * KTB + cg * 1024 + i * 4096, va[low][TM == 2 ? i >> 2 : i >> 1], so + (TM == 2 ? i : 2 * i) * st.a_kt);
  };
  auto chunk = [&](int u, int ch) {
#pragma unroll
    for (int i = ch * TM; i < (ch + 1) * TM; ++i) if (i < ND) piece(u, i);
  };
  auto lead = [&](int u) { chunk(u, 0); __builtin_amdgcn_s_sleep(TM * PACEW); chunk(u, 1); };
  bool have = false, pre = false;
  bool seen = false, asked = false;
  LAS char* scr = lds + 131072;
  unsigned hold = 0;
  const unsigned* fp = flagp(p, st.dep, st.ndep);
  auto post1 = [&](unsigned v) { __builtin_amdgcn_raw_buffer_store_b32(v, rw, (l & 7) * 128, OFF_FLAG + st.role * 1024, AUX_SC1); };
  if (PRIO && kh) __builtin_amdgcn_s_setprio(1);
  LAS volatile int* nw = (LAS volatile int*)(lds + 147456);
  auto out = [&](int u, int ni, int mi, f32x4 r) {
    const int oo = st.o_off + (2 * u + mi) * st.o_tile;
    if (GELU) {
      const f32x4 t = rnd4(r);
      st64(u32x2{pk(gelu_tanh(t[0]), gelu_tanh(t[1])), pk(gelu_tanh(t[2]), gelu_tanh(t[3]))}, rw, r16 * 32 + g * 8, oo + (cg * 2 + ni) * 512);
    } else {
      st128(__builtin_bit_cast(u32x4, r), rw, r16 * 64 + g * 16, oo + (cg * 2 + ni) * 1024);
    }
  };
#pragma nounroll
  for (int u = 0; u < st.nunit; ++u) {
    TS_DECL;
    TS(0);
    if (!have && !seen) {
      const int need = st.shift ? min(u + 1, NSEG) : u + 1;
      const int avail = await(p, (LAS char*)(nw + 2), w, st.dep, st.ndep, need);
      if (avail == 0) return;
      pre = avail > need && u + 2 < st.nunit;
    }
    TS(1);
    f32x4 acc[TN][TM];
    unsigned peek = 0;
    auto compute = [&](auto have_, auto pre_) {
      constexpr bool HAVE = have_.value, PRE = pre_.value;
      if (!HAVE && !asked) lead(u);
#pragma unroll
      for (int ni = 0; ni < TN; ++ni)
#pragma unroll
        for (int mi = 0; mi < TM; ++mi) acc[ni][mi] = f32x4{0.f, 0.f, 0.f, 0.f};
      LAS const char* ab = lds + (u & 1) * 65536 + fa;
      u32x4 Af[8][TM];
      auto rdk = [&](int k) {
#pragma unroll
        for (int mi = 0; mi < TM; ++mi)
          if (k < 2 * NKT) Af[k & 7][mi] = lds128((LAS const char*)((size_t)ab ^ ((k & 1) * (GELU ? 64 : 1024))) + (k >> 1) * KTB + mi * 2048);
      };
      auto landed = [&](int c) {
        if (c < 2) waitv<TM>(); else if (c == 2) waitv<ND - 3 * TM>(); else waitv<PRE ? TM : 0>();
        bar();
        if (c < 2) chunk(u, c + 2); else if (PRE) chunk(u + 1, c - 2);
      };
      if (!HAVE) { landed(0); if (asked && w == 7) post1(u); }
#pragma unroll
      for (int k = 0; k < 4; ++k) rdk(k);
      SCHED();
#pragma unroll
      for (int k = 0; k < 2 * NKT; ++k) {
        if (HAVE) rdk(k + 4);
        else if ((k & 3) == 2 && k + 2 < 2 * NKT) {
          landed((k >> 2) + 1);
#pragma unroll
          for (int j = 0; j < 4; ++j) rdk(k + 2 + j);
        }
#pragma unroll
        for (int ni = 0; ni < TN; ++ni)
#pragma unroll
          for (int mi = 0; mi < TM; ++mi) acc[ni][mi] = mfma(Wf[k][ni], Af[k & 7][mi], acc[ni][mi]);
        if (HAVE && k == (kh ? 2 * NKT - 1 : FPOS)) bar();
        if (HAVE && PRE) { if (DMAALL) { if (k < 2 * ND) piece(u + 1, k >> 1, k & 1); } else if (k < ND) piece(u + 1, k); }
        if (!HAVE && PRE && (k == 11 || k == 12)) chunk(u + 1, k - 9);
        if (k == (HAVE ? 4 : 11) && w == 0) peek_load(peek, fp);
        SCHED();
      }
    };
    if (have) { if (pre && (kh || !DMAALL)) compute(std::true_type(), std::true_type()); else compute(std::true_type(), std::false_type()); }
    else if (pre) compute(std::false_type(), std::true_type());
    else compute(std::false_type(), std::false_type());
    TS(3);
    if (PRIO && !kh) __builtin_amdgcn_s_setprio(2);
    auto level = [&]() {
      const unsigned n1 = st.shift ? min(u + 2, NSEG) : u + 2, n2 = st.shift ? min(u + 3, NSEG) : u + 3;
      return none(peek < n2) && u + 3 < st.nunit ? 2 : none(peek < n1) && u + 1 < st.nunit ? 1 : 0;
    };
    if (pre) {
      if (kh) {
#pragma unroll
        for (int i = 0; i < TN * TM; ++i) *(LAS f32x4*)(scr + (cg * TN * TM + i) * 1024 + l * 16) = acc[i / TM][i % TM];
      }
    } else {
#pragma unroll
      for (int mi = 0; mi < TM; ++mi) *(LAS f32x4*)(scr + (w * TM + mi) * 1024 + l * 16) = kh ? acc[0][mi] : acc[1][mi];
    }
    asm volatile("s_waitcnt vmcnt(0)" : "+v"(peek), "+v"(hold)::"memory");
    if (w == 0) nw[0] = level();
    WAITL(0);
    TS(4);
    bar();
    TS(5);
    if (have && w == 7) post1(u);
    int lev = __builtin_amdgcn_readfirstlane(nw[0]);
    if (pre) {
      if (!kh) {
#pragma unroll
        for (int i = 0; i < TN * TM; ++i) acc[i / TM][i % TM] += *(LAS const f32x4*)(scr + (cg * TN * TM + i) * 1024 + l * 16);
      }
      TS(6);
      if (TM == 2 && w == 0 && none(peek < u + 4u)) {
#pragma nounroll
        for (int j = st.si; j < 8; j += st.sn)
          touch(hold, rw, l * 128, st.a_off + (u + 3) * st.a_unit + ((j >> 2) + st.shift) * (st.a_unit >> 1) + (j & 3) * 8192);
      }
      if (!kh) {
#pragma unroll
        for (int i = 0; i < TN * TM; ++i) out(u, i / TM, i % TM, acc[i / TM][i % TM]);
      }
      have = true;
    } else {
#pragma unroll
      for (int mi = 0; mi < TM; ++mi) out(u, kh, mi, (kh ? acc[1][mi] : acc[0][mi]) + *(LAS const f32x4*)(scr + ((w ^ 4) * TM + mi) * 1024 + l * 16));
      TS(6);
      asked = lev > 0;
      if (asked) {
        lead(u + 1);
      } else {
        if (w == 0) peek_load(peek, fp);
        asm volatile("s_waitcnt vmcnt(0)" : "+v"(peek)::"memory");
        if (w == 0) nw[1] = level();
        WAITL(0);
        bar();
        if (w == 7) post1(u + 1);
        lev = __builtin_amdgcn_readfirstlane(nw[1]);
      }
      have = false;
    }
    if (PRIO && !kh) __builtin_amdgcn_s_setprio(0);
    seen = lev > 0;
    pre = lev == 2;
    TS(2);
    TS_OUT(st.role, u);
  }
}

__device__ __forceinline__ float psum(float v) {
  v += dppf<0xB1>(v); v += dppf<0x4E>(v); v += dppf<0x128>(v);
  return xsum1632(v);
}
template <int NP, class F> __device__ __forceinline__ void norm_pairs(f32x4 (&v)[NP], LAS char* red, int w, int l, F atbar) {
#pragma unroll
  for (int i = 0; i < NP; ++i) {
    const float s = psum(hsum(v[i])), q = psum(hsum(v[i] * v[i]));
    *(LAS f32x2*)(red + ((i * 2 + ((l >> 2) & 1)) * 8 + w) * 8) = f32x2{s, q};
  }
  atbar(0);
  WAITL(0);
  bar();
  atbar(1);
#pragma unroll
  for (int i = 0; i < NP; ++i) {
    LAS const f32x4* rp = (LAS const f32x4*)(red + (i * 2 + ((l >> 2) & 1)) * 64);
    const f32x4 ts = (rp[0] + rp[1]) + (rp[2] + rp[3]);
    const float mean = (ts[0] + ts[2]) * (1.0f / DM), var = fmaxf((ts[1] + ts[3]) * (1.0f / DM) - mean * mean, 0.f);
    v[i] = (v[i] - mean) * __builtin_amdgcn_rsqf(var + LN_EPS);
  }
}
template <int KIND, int NP> __device__ __forceinline__ void stage_rows(LAS char* lds, int w, int role, int idx) {
  const KP p = kparams();
  const rsrc_t rw = wsr(p), rx = mkrsrc(p->x, ACT_BYTES), ro = mkrsrc(p->out, ACT_BYTES);
  const int l = lane_id(), col = 128 * w + (l >> 3) * 16 + (l & 3) * 4, rl = (l >> 2) & 1, r0 = idx * 2 * NP;
  const float* mod = (const float*)(p->ws + OFF_MOD) + col;
  const f32x4 gate = *(const f32x4*)(mod + (KIND == 0 ? 2 : 5) * DM), sh = *(const f32x4*)(mod + 3 * DM), sc = rnd4(*(const f32x4*)(mod + 4 * DM) + 1.0f);
  asm volatile("s_waitcnt vmcnt(0)" ::"v"(gate), "v"(sh), "v"(sc) : "memory");
  const int vp = (col >> 4) * 1024 + rl * 64 + (l & 3) * 16, vr = rl * 2048 + col * 2, l16 = l * 16;
  constexpr int NU = KIND == 0 ? NSEG : NSEG + 1, NPART = KIND == 1 ? 1 : 4, NOUT = KIND == 0 ? 3 : KIND == 1 ? 2 : 1, ND = NP * NPART + 1;
  const int DEP = KIND == 0 ? R_WO : KIND == 1 ? R_W2 : R_W4;
  constexpr int NDEP = KIND == 1 ? 8 : 32;
  auto issue = [&](int u) {
    LAS char* buf = lds + (u & 1) * 65536;
    const int row = u * 32 + r0;
#pragma unroll
    for (int i = 0; i < NP; ++i)
#pragma unroll
      for (int k = 0; k < NPART; ++k)
        glds<AUX_SC1>(rw, buf + ((i * NPART + k) * 8 + w) * 1024, vp, (KIND == 0 ? OFF_P1 : KIND == 1 ? OFF_P2 : OFF_P4) + k * PSTR + (row >> 4) * 65536 + ((row & 15) + 2 * i) * 64);
    if (KIND == 0) glds<0>(rx, buf + 32768 + (w & 3) * 1024, l16, row * 2048 + (w & 3) * 1024);
    if (KIND == 1) glds<AUX_SC1>(rw, buf + 32768 + w * 1024, l16, OFF_H2 + (row - 2) * 2048 + w * 1024);
    if (KIND == 2) glds<AUX_SC1>(rw, buf + 32768 + w * 1024, l16, (w < 4 ? OFF_X1 - 4096 : OFF_Y1 - 4096) + row * 2048 + w * 1024);
  };
  bool have = false, pre = false;
  int seen = 0;
#pragma nounroll
  for (int u = 0; u < NU; ++u) {
    LAS char* buf = lds + (u & 1) * 65536;
    TS_DECL;
    TS(0);
    if (!have) {
      const int avail = seen > u ? seen : await(p, buf + 61440, w, DEP, NDEP, u + 1);
      if (avail == 0) return;
      pre = avail > u + 1 && u + 2 < NU;
      issue(u);
    }
    TS(1);
    if (pre) issue(u + 1);
    if (!have) {
      if (pre) waitv<ND>(); else waitv<0>();
      bar();
    }
    unsigned peek = 0;
    auto early = [&](int after) {
      if (!after) { if (have) { if (pre) waitv<ND>(); else waitv<0>(); } }
      else { if (have && w == 7) post(p, role, u); if (pre && w == 0) peek_load(peek, flagp(p, DEP, NDEP)); }
    };
    if (KIND == 2 && pre && w == 0) peek_load(peek, flagp(p, DEP, NDEP));
    TS(3);
    f32x4 v[NP];
    auto put = [&](int o) {
#pragma unroll
      for (int i = 0; i < NP; ++i) { const u32x2 d = pk4(v[i]); *(LAS u32x2*)(buf + 40960 + (o * NP + i) * 4096 + vr) = d; v[i] = unpk4(d); }
    };
#pragma unroll
    for (int i = 0; i < NP; ++i) {
      LAS const f32x4* pp = (LAS const f32x4*)(buf + (i * NPART * 8 + w) * 1024 + l16);
      f32x4 s = pp[0];
      if (NPART == 4) s = (s + pp[512]) + (pp[1024] + pp[1536]);
      s = rnd4(s);
      if (KIND == 2) s += unpk4(lds64(buf + 32768 + 4096 + vr));
      if (KIND != 1) s *= gate;
      v[i] = s + unpk4(lds64(buf + 32768 + i * 4096 + vr));
    }
    put(NOUT - 1);
    if (KIND == 0) {
      norm_pairs<NP>(v, buf + 57344, w, l, early);
#pragma unroll
      for (int i = 0; i < NP; ++i) v[i] = v[i] * sc + sh;
      put(1);
    }
    if (KIND == 0) { norm_pairs<NP>(v, buf + 59392, w, l, [](int) {}); put(0); }
    if (KIND == 1) { norm_pairs<NP>(v, buf + 59392, w, l, early); put(0); }
    TS(4);
    asm volatile("s_waitcnt vmcnt(0)" : "+v"(peek)::"memory");
    if (w == 0) *(LAS volatile int*)(buf + 61444) = pre && none(peek < u + 3u) && u + 3 < NU;
    WAITL(0);
    bar();
    TS(5);
#pragma unroll
    for (int jj = 0; jj < (NOUT * NP + 1) / 2; ++jj) {
      const int j = jj * 8 + w;
      if (j < NOUT * NP * 4) {
        const int o = j / (NP * 4), rr = (j % (NP * 4)) >> 1, row = u * 32 + r0 + rr;
        const u32x4 d = lds128(buf + 40960 + j * 1024 + l16);
        const int so = row * 2048 + (j & 1) * 1024;
        if (KIND == 0) st128(d, rw, l16, (o == 0 ? OFF_N2 : o == 1 ? OFF_H2 : OFF_X1) + so);
        if (KIND == 1) st128(d, rw, l16, (o == 0 ? OFF_N3 : OFF_Y1) + so);
        if (KIND == 2 && row >= 2 && row < SEQ + 2) st128(d, ro, l16, so - 4096);
      }
    }
    TS(6);
    if (KIND != 2 && !pre) {
      if (w == 0) peek_load(peek, flagp(p, DEP, NDEP));
      asm volatile("s_waitcnt vmcnt(0)" : "+v"(peek)::"memory");
      if (w == 0) *(LAS volatile int*)(buf + 61448) = none(peek < u + 3u) ? u + 3 : none(peek < u + 2u) ? u + 2 : 0;
      WAITL(0);
      bar();
      if (w == 7) post(p, role, u + 1);
      seen = __builtin_amdgcn_readfirstlane(*(LAS volatile int*)(buf + 61448));
    }
    TS(2);
    TS_OUT(role, u);
    have = pre;
    pre = __builtin_amdgcn_readfirstlane(*(LAS volatile int*)(buf + 61444)) != 0;
  }
}

__device__ __forceinline__ void prefetcher(int w) {
  const KP p = kparams();
  const rsrc_t rw = wsr(p);
  const int h = (blockIdx.x & 3) * 8 + w, l = lane_id();
  unsigned d = 0, spins = 0;
#pragma nounroll
  for (int s = 1; s < NSEG; ++s) {
    const unsigned* fp = l < 32 ? (const unsigned*)(p->ws + OFF_FLAG + (R_QK + h) * 1024) : (const unsigned*)(p->ws + OFF_PROD + ((s >> 2) * 32 + h) * 128);
#pragma nounroll
    while (!none(__hip_atomic_load(fp, RLX_AGENT) + (l < 32 ? 3 : 0) < (l < 32 ? (unsigned)s + 1 : 3u))) {
      __builtin_amdgcn_s_sleep(10);
      if (++spins > SPIN_CAP) return;
    }
    const int so = sgpr(OFF_QKV + h * QKV_H + s * QKV_S);
    touch(d, rw, l * 128, so + 8192); touch(d, rw, l * 128, so + 16384); touch(d, rw, l * 128, so);
    asm volatile("s_waitcnt vmcnt(0)" : "+v"(d)::"memory");
  }
}

__device__ __forceinline__ void stream(LAS char* lds, int w) {
  const KP p = kparams();
  const int x = blockIdx.x & 7, i = blockIdx.x >> 3, q = x & 3;
  if (x < 4 && i == 31) { prefetcher(w); return; }
  int kind, idx;
  if (x < 4 || i == 31) { kind = i < 28 ? 6 : 7; idx = q * 4 + (i & 3); }
  else if (i < 8) { kind = 1; idx = i; }
  else if (i < 15) { kind = 2; idx = q * 7 + i - 8; }
  else if (i < 17) { kind = 3; idx = q * 2 + i - 15; }
  else if (i < 19) { kind = 5; idx = q * 2 + i - 17; }
  else if (i < 23) { kind = 4; idx = q * 4 + i - 19; }
  else { kind = 0; idx = i - 23; }
  kind = sgpr(kind); idx = sgpr(idx);
  if (kind == 3) {
    unsigned d = 0;
    const int l = lane_id();
#pragma nounroll
    for (int j = 0; j < 14; ++j) {
      touch(d, mkrsrc(p->w3, DM * FF * 2), l * 128, ((j * 8 + idx) * 8 + w) * 8192);
      if ((j & 3) == 1) asm volatile("s_waitcnt vmcnt(0)" : "+v"(d)::"memory");
    }
    stage_rows<1, 2>(lds, w, R_LN3 + idx, idx);
    return;
  }
  if (kind == 6) { stage_rows<0, 1>(lds, w, R_LN2 + idx, idx); return; }
  if (kind == 7) { stage_rows<2, 1>(lds, w, R_FIN + idx, idx); return; }
  Stage st;
  st.shift = 0; st.si = idx; st.sn = 8;
  if (kind == 0) {
    st.role = R_WO + q * 8 + idx; st.dep = R_QK + q * 8; st.ndep = 8; st.nunit = NSEG;
    st.wgt = p->Wo; st.wbytes = W_BYTES; st.ldw = 2048; st.w_off = q * 1024 * 2048 + idx * 256;
    st.a_off = OFF_OT + q * 16 * 2048; st.a_unit = 2 * 131072; st.a_kt = 2048; st.a_tile = 131072;
    st.o_off = OFF_P1 + q * PSTR + idx * 8192; st.o_tile = 65536;
    stage_gemm<2, 8, false>(lds, w, st);
    return;
  }
  if (kind == 1) {
    st.role = R_W4 + q * 8 + idx; st.dep = R_W3 + q * 7; st.ndep = 7; st.nunit = NSEG + 1;
    st.wgt = p->w4; st.wbytes = DM * FF * 2; st.ldw = 2048; st.w_off = q * 896 * 2048 + idx * 256;
    st.a_off = OFF_G3 + q * 14 * 2048; st.a_unit = 2 * FF * 32; st.a_kt = 2048; st.a_tile = FF * 32;
    st.o_off = OFF_P4 + q * PSTR + idx * 8192; st.o_tile = 65536;
    stage_gemm<2, 7, false>(lds, w, st);
    return;
  }
  if (kind == 5) {
    st.role = R_W2 + idx; st.dep = R_W1; st.ndep = 16; st.nunit = NSEG + 1; st.shift = 1; st.si = idx & 1; st.sn = 2;
    st.wgt = p->w2; st.wbytes = DM * DM * 2; st.ldw = 2048; st.w_off = idx * 256;
    st.a_off = OFF_G; st.a_unit = 65536; st.a_kt = 2048; st.a_tile = 32768;
    st.o_off = OFF_P2 + idx * 8192; st.o_tile = 65536;
    stage_gemm<2, 8, false>(lds, w, st);
    return;
  }
  if (kind == 2) {
    st.role = R_W3 + idx; st.dep = R_LN3; st.ndep = 8; st.nunit = NSEG + 1; st.si = idx - q * 7; st.sn = 7;
    st.wgt = p->w3; st.wbytes = DM * FF * 2; st.ldw = FF * 2; st.w_off = idx * 256;
    st.a_off = OFF_N3; st.a_unit = 65536; st.a_kt = 128; st.a_tile = 0;
    st.o_off = OFF_G3 + idx * 4096; st.o_tile = FF * 32;
    stage_gemm<2, 8, true>(lds, w, st);
    return;
  }
  const int rh = idx >> 3, cb = idx & 7;
  st.role = R_W1 + idx; st.dep = R_LN2 + 8 * rh; st.ndep = 8; st.nunit = NSEG;
  st.wgt = p->w1; st.wbytes = DM * DM * 2; st.ldw = 2048; st.w_off = cb * 256;
  st.a_off = OFF_N2 + rh * 32768; st.a_unit = 65536; st.a_kt = 128; st.a_tile = 0;
  st.o_off = OFF_G + 32768 + rh * 32768 + cb * 4096; st.o_tile = 32768;
  stage_gemm<1, 8, true>(lds, w, st);
}

__global__ __launch_bounds__(512, 2) void k_mega(Params) {
  LAS char* lds = (LAS char*)smem;
  const int b = blockIdx.x, w = __builtin_amdgcn_readfirstlane(threadIdx.x >> 6);
  WSTAMP(234 + (b >> 6), b & 63, 0);
  if ((b & 7) < 4 && (b >> 3) < 24) {
    const int x = b & 3, i = b >> 3;
    if (!chain_role(kparams(), i < 8 ? x * 8 + i : (x & 1) * 16 + i - 8, i < 8 ? -1 : x >> 1, w, lds)) __hip_atomic_store(cword(kparams(), C_TMO), 1u, RLX_AGENT);
    return;
  }
  if (!produce(lds, w)) { __hip_atomic_store(cword(kparams(), C_TMO), 1u, RLX_AGENT); return; }
  stream(lds, w);
}

extern "C" void kernel_launch(void* const* d_in, const int* in_sizes, int n_in, void* d_out, int out_size, void* d_ws, size_t ws_size,
                              hipStream_t stream) {
  Params p;
  p.x = (const bf16_t*)d_in[0]; p.mem0 = (const bf16_t*)d_in[1]; p.Wq = (const bf16_t*)d_in[2]; p.Wk = (const bf16_t*)d_in[3];
  p.Wv = (const bf16_t*)d_in[4]; p.Wo = (const bf16_t*)d_in[5]; p.w1 = (const bf16_t*)d_in[8]; p.w2 = (const bf16_t*)d_in[9];
  p.w3 = (const bf16_t*)d_in[10]; p.w4 = (const bf16_t*)d_in[11]; p.c = (const bf16_t*)d_in[12]; p.wmod = (const bf16_t*)d_in[13];
  p.out = (bf16_t*)d_out; p.ws = (unsigned char*)d_ws;
  (void)hipFuncSetAttribute((const void*)k_mega, hipFuncAttributeMaxDynamicSharedMemorySize, LDS_BYTES);
  k_pre<<<256, 512, 0, stream>>>(p);
  k_mega<<<256, 512, LDS_BYTES, stream>>>(p);
}
```
